```python
import torch

def _softmax(x, axis=-1):
    return torch.softmax(x, dim=axis)
def _gelu(x):
    return torch.nn.functional.gelu(x, approximate="tanh")
def _silu(x):
    return torch.nn.functional.silu(x)
def _sigmoid(x):
    return torch.sigmoid(x)
def _layernorm(x, axis=-1, eps=1e-5):
    m = x.mean(dim=axis, keepdim=True)
    v = x.var(dim=axis, keepdim=True, unbiased=False)
    return (x - m) / torch.sqrt(v + eps)
def _scatter_add(tgt, idx, src, axis=0):
    return tgt.clone().index_add_(axis, idx, src)
def _segment_sum(src, seg, n):
    out = torch.zeros((n,) + src.shape[1:], dtype=src.dtype)
    return out.index_add_(0, seg, src)
def _pad(x, pw):
    flat = tuple(v for pair in reversed(pw) for v in pair)
    return torch.nn.functional.pad(x, flat)

kernel_name = 'interleave_layers__sliding_block__mixer_channel_mlp__prompt'

def setup_inputs():

    _w = (lambda a: (lambda t: t.to(torch.bfloat16) if t.is_floating_point() else t)(torch.as_tensor(a)))
    def _A_setup():
        B, S, D = 1, 2048, 640
        return {
            "x": _w((lambda *s: torch.randn(*s, dtype=torch.bfloat16) * 0.05)(B, S, D)),
            "wqkv": _w((lambda *s: torch.randn(*s, dtype=torch.bfloat16) * 0.05)(D, 3 * D)),
            "wo": _w((lambda *s: torch.randn(*s, dtype=torch.bfloat16) * 0.05)(D, D)),
        }
    _A_in = _A_setup()

    def _B_setup():
        B, S, D, F = 1, 2048, 640, 1728
        return {
            "x": _w((lambda *s: torch.randn(*s, dtype=torch.bfloat16) * 0.05)(B, S, D)),
            "w1": _w((lambda *s: torch.randn(*s, dtype=torch.bfloat16) * 1.6)(D, F) * (D ** -0.5)),
            "b1": _w((lambda *s: torch.randn(*s, dtype=torch.bfloat16) * 1.6)(F)),
            "w2": _w((lambda *s: torch.randn(*s, dtype=torch.bfloat16) * 0.4)(F, D) * (F ** -0.5)),
            "b2": _w((lambda *s: torch.randn(*s, dtype=torch.bfloat16) * 0.4)(D)),
        }
    _B_in = _B_setup()

    return {
        "A_x": _A_in["x"],
        "A_wqkv": _A_in["wqkv"],
        "A_wo": _A_in["wo"],
        "B_x": _B_in["x"],
        "B_w1": _B_in["w1"],
        "B_b1": _B_in["b1"],
        "B_w2": _B_in["w2"],
        "B_b2": _B_in["b2"],
    }

def reference(A_x, A_wqkv, A_wo, B_x, B_w1, B_b1, B_w2, B_b2):
    def _A_ref(x, wqkv, wo):
        B, S, D = x.shape
        H = 32; Dh = D // H; W = 256
        qkv = (lambda x, *d: x.permute(*d))((x @ wqkv).reshape(B, S, 3, H, Dh), 2, 0, 3, 1, 4)
        q, k, v = qkv[0], qkv[1], qkv[2]
        scores = torch.einsum("bhqd,bhkd->bhqk", q, k) * (Dh ** -0.5)
        idx = torch.arange(S)
        mask = (idx[:, None] - idx[None, :] >= 0) & (idx[:, None] - idx[None, :] < W)
        scores = torch.where(mask[None, None], scores, -1e9)
        p = _softmax(scores, axis=-1)
        o = torch.einsum("bhqk,bhkd->bhqd", p, v)
        return (lambda x, *d: x.permute(*d))(o, 0, 2, 1, 3).reshape(B, S, D) @ wo

    def _B_ref(x, w1, b1, w2, b2):
        y = _layernorm(x, axis=-1)
        h = _gelu(torch.einsum("bsd,df->bsf", y, w1) + b1)
        return torch.einsum("bsf,fd->bsd", h, w2) + b2

    x = A_x
    x = x + _A_ref(_layernorm(x, axis=-1), A_wqkv, A_wo)
    x = x + _B_ref(_layernorm(x, axis=-1), B_w1, B_b1, B_w2, B_b2)
    x = x + _A_ref(_layernorm(x, axis=-1), A_wqkv, A_wo)
    x = x + _B_ref(_layernorm(x, axis=-1), B_w1, B_b1, B_w2, B_b2)
    return x
```

```cpp
#include <hip/hip_runtime.h>
#include <stdint.h>

namespace mk {

typedef unsigned short u16;
typedef unsigned long long u64;
typedef __attribute__((ext_vector_type(2))) unsigned u32x2;
typedef __attribute__((ext_vector_type(4))) unsigned u32x4;
#define DEV __device__ __forceinline__

constexpr int S = 2048, D = 640, F = 1728;
constexpr unsigned OFF_EPOCH = 0u, OFF_TMO = 512u, OFF_F1 = 1024u, OFF_F2 = 4096u, OFF_STAMP = 131072u, OFF_WQKV = 671744u, WQ_SZ = 153600u, OFF_CSQ = 3129344u, OFF_W1 = 3194880u, OFF_CS1 = 5406720u, OFF_WO = 5462016u, OFF_W2 = 6281216u, OFF_X = 8574976u, X_SZ = 2621440u, OFF_ST = 19060736u, ST_SZ = 131072u, OFF_KV = 19585024u, KV_BLK = 6656u, KV_SZ = 6815744u, OFF_O = 33216512u, O_SZ = 2621440u, OFF_H = 38459392u, H_SZ = 7077888u, WS_END = 52615168u;
constexpr unsigned L_BYTES = 163840;
constexpr int AUX_SC1 = 16;

__global__ __launch_bounds__(256, 1) void mk_kernel(const u16* __restrict__ x, const u16* __restrict__ wqkv, const u16* __restrict__ wo,
                                                    const u16* __restrict__ w1, const u16* __restrict__ b1, const u16* __restrict__ w2,
                                                    const u16* __restrict__ b2, u16* __restrict__ out, char* ws) {
    const int w = __builtin_amdgcn_readfirstlane(threadIdx.x >> 6);
    const int b = blockIdx.x;
    asm volatile(
    "s_getpc_b64 s[50:51]\n"
    ".Lmk_pf:\n"
    "s_and_b32 s50, s50, 0xffffff00\n"
    "s_lshr_b32 s52, %1, 3\n"
    "s_lshl_b32 s52, s52, 2\n"
    "s_add_u32 s52, s52, %2\n"
    "s_lshl_b32 s52, s52, 9\n"
    "v_mbcnt_lo_u32_b32 v5, -1, 0\n"
    "v_mbcnt_hi_u32_b32 v5, -1, v5\n"
    "v_lshl_add_u32 v5, v5, 3, s52\n"
    "v_min_u32_e32 v5, .Lmk_end-.Lmk_pf, v5\n"
    "global_load_dwordx2 a[64:65], v5, s[50:51]\n"
    "s_load_dwordx2 s[40:41], %0, 0\n"
    "s_load_dwordx2 s[42:43], %0, 32\n"
    "s_load_dwordx2 s[44:45], %0, 48\n"
    "s_load_dwordx2 s[46:47], %0, 56\n"
    "s_load_dwordx2 s[16:17], %0, 64\n"
    "s_load_dwordx2 s[70:71], %0, 8\n"
    "s_load_dwordx2 s[72:73], %0, 16\n"
    "s_load_dwordx2 s[74:75], %0, 24\n"
    "s_load_dwordx2 s[76:77], %0, 40\n"
    "s_mov_b32 s26, %2\n"
    "s_and_b32 s50, %1, 7\n"
    "s_lshr_b32 s51, %1, 3\n"
    "s_lshr_b32 s52, s50, 1\n"
    "s_lshl_b32 s52, s52, 2\n"
    "s_lshr_b32 s53, s51, 3\n"
    "s_add_u32 s27, s52, s53\n"
    "s_and_b32 s52, s50, 1\n"
    "s_lshl_b32 s52, s52, 3\n"
    "s_and_b32 s53, s51, 7\n"
    "s_add_u32 s28, s52, s53\n"
    "s_mov_b32 s78, %1\n"
    "s_lshr_b32 s30, s28, 3\n"
    "s_and_b32 s31, s28, 7\n"
    "s_lshl_b32 s32, s27, 2\n"
    "s_add_u32 s32, s32, s26\n"
    "s_lshl_b32 s33, s27, 3\n"
    "s_lshl_b32 s50, s30, 2\n"
    "s_add_u32 s33, s33, s50\n"
    "s_add_u32 s33, s33, s26\n"
    "s_and_b32 s34, s26, 1\n"
    "s_lshr_b32 s35, s26, 1\n"
    "s_mov_b32 s29, 0\n"
    "s_mov_b32 s36, 0\n"
    "s_lshl_b32 s50, s28, 2\n"
    "s_mul_i32 s51, s28, 3\n"
    "s_add_u32 s51, s51, 6\n"
    "s_cmp_lt_u32 s28, 6\n"
    "s_cselect_b32 s37, s50, s51\n"
    "s_cselect_b32 s38, 4, 3\n"
    "s_mov_b32 s48, 0\n"
    "s_mov_b32 s49, -1\n"
    "v_mbcnt_lo_u32_b32 v0, -1, 0\n"
    "v_mbcnt_hi_u32_b32 v0, -1, v0\n"
    "v_lshlrev_b32_e32 v0, 4, v0\n"
    "v_lshlrev_b32_e32 v4, 22, v0\n"
    "s_waitcnt lgkmcnt(0)\n"
    "s_lshr_b32 s52, s78, 3\n"
    "s_lshl_b32 s52, s52, 2\n"
    "s_add_u32 s52, s52, s26\n"
    "s_cmp_eq_u32 s52, 127\n"
    "s_cbranch_scc0 .Lmk_tx\n"
    "v_lshlrev_b32_e32 v5, 2, v0\n"
    "v_min_u32_e32 v6, 3452, v5\n"
    "global_load_dword a66, v6, s[42:43]\n"
    "v_min_u32_e32 v5, 1276, v5\n"
    "global_load_dword a67, v5, s[44:45]\n"
    ".Lmk_tx:\n"
    "s_and_b32 s17, s17, 0xffff\n"
    "s_mov_b32 s18, 0x322d800\n"
    "s_mov_b32 s19, 0x20000\n"
    "s_mov_b32 s23, 0x20000\n"
    "s_mov_b32 s50, 0\n"
    "buffer_load_dwordx2 v[2:3], v4, s[16:19], s50 offen sc1\n"
    "v_lshrrev_b32_e32 v5, 4, v0\n"
    "s_lshr_b32 s51, s78, 4\n"
    "s_and_b32 s53, s78, 7\n"
    "s_lshr_b32 s52, s78, 2\n"
    "s_and_b32 s52, s52, 2\n"
    "s_add_u32 s52, s52, s26\n"
    "s_mov_b32 s56, 3760\n"
    "s_cmp_lt_u32 s26, 2\n"
    "s_cselect_b32 s55, s52, 2\n"
    "s_cmp_lg_u32 s55, 2\n"
    "s_cbranch_scc0 .Lmk_pn_2\n"
    "s_mov_b32 s20, s70\n"
    "s_and_b32 s21, s71, 0xffff\n"
    "s_mov_b32 s22, 0x258000\n"
    "s_mul_i32 s54, s53, 307200\n"
    "s_mul_i32 s55, s51, 80\n"
    "s_add_u32 s54, s54, s55\n"
    "s_add_u32 s55, s52, 1\n"
    "s_and_b32 s55, s55, 3\n"
    "s_mul_i32 s55, s55, 1280\n"
    "s_add_u32 s54, s54, s55\n"
    "v_add_u32_e32 v6, 0, v5\n"
    "v_mul_u32_u24_e32 v7, 13108, v6\n"
    "v_lshrrev_b32_e32 v7, 16, v7\n"
    "v_lshlrev_b32_e32 v6, 4, v6\n"
    "v_mad_u32_u24 v6, v7, s56, v6\n"
    "buffer_load_dwordx4 v[32:35], v6, s[20:23], s54 offen\n"
    "v_add_u32_e32 v8, 64, v5\n"
    "v_mul_u32_u24_e32 v9, 13108, v8\n"
    "v_lshrrev_b32_e32 v9, 16, v9\n"
    "v_lshlrev_b32_e32 v8, 4, v8\n"
    "v_mad_u32_u24 v8, v9, s56, v8\n"
    "buffer_load_dwordx4 v[36:39], v8, s[20:23], s54 offen\n"
    "v_add_u32_e32 v6, 128, v5\n"
    "v_mul_u32_u24_e32 v7, 13108, v6\n"
    "v_lshrrev_b32_e32 v7, 16, v7\n"
    "v_lshlrev_b32_e32 v6, 4, v6\n"
    "v_mad_u32_u24 v6, v7, s56, v6\n"
    "buffer_load_dwordx4 v[40:43], v6, s[20:23], s54 offen\n"
    "v_add_u32_e32 v8, 192, v5\n"
    "v_mul_u32_u24_e32 v9, 13108, v8\n"
    "v_lshrrev_b32_e32 v9, 16, v9\n"
    "v_lshlrev_b32_e32 v8, 4, v8\n"
    "v_mad_u32_u24 v8, v9, s56, v8\n"
    "buffer_load_dwordx4 v[44:47], v8, s[20:23], s54 offen\n"
    "v_add_u32_e32 v6, 256, v5\n"
    "v_mul_u32_u24_e32 v7, 13108, v6\n"
    "v_lshrrev_b32_e32 v7, 16, v7\n"
    "v_lshlrev_b32_e32 v6, 4, v6\n"
    "v_mad_u32_u24 v6, v7, s56, v6\n"
    "buffer_load_dwordx4 v[48:51], v6, s[20:23], s54 offen\n"
    "v_add_u32_e32 v8, 320, v5\n"
    "v_mul_u32_u24_e32 v9, 13108, v8\n"
    "v_lshrrev_b32_e32 v9, 16, v9\n"
    "v_lshlrev_b32_e32 v8, 4, v8\n"
    "v_mad_u32_u24 v8, v9, s56, v8\n"
    "buffer_load_dwordx4 v[52:55], v8, s[20:23], s54 offen\n"
    "v_add_u32_e32 v6, 384, v5\n"
    "v_mul_u32_u24_e32 v7, 13108, v6\n"
    "v_lshrrev_b32_e32 v7, 16, v7\n"
    "v_lshlrev_b32_e32 v6, 4, v6\n"
    "v_mad_u32_u24 v6, v7, s56, v6\n"
    "v_mov_b32_e32 v10, 0x7c000000\n"
    "v_cmp_gt_u32_e32 vcc, 16, v5\n"
    "s_nop 1\n"
    "v_cndmask_b32_e32 v6, v10, v6, vcc\n"
    "buffer_load_dwordx4 v[56:59], v6, s[20:23], s54 offen\n"
    "s_branch .Lmk_pj_1\n"
    ".Lmk_pn_2:\n"
    "s_cmp_lt_u32 s26, 2\n"
    "s_cbranch_scc0 .Lmk_pn_3\n"
    "s_branch .Lmk_pj_1\n"
    ".Lmk_pn_3:\n"
    "s_mov_b32 s20, s40\n"
    "s_and_b32 s21, s41, 0xffff\n"
    "s_mov_b32 s22, 0x280000\n"
    "s_mul_i32 s54, s78, 10240\n"
    "s_sub_u32 s55, s26, 2\n"
    "s_mul_i32 s55, s55, 640\n"
    "s_add_u32 s54, s54, s55\n"
    "v_and_b32_e32 v6, 7, v5\n"
    "v_mul_u32_u24_e32 v6, 1280, v6\n"
    "v_lshrrev_b32_e32 v7, 3, v5\n"
    "v_mul_u32_u24_e32 v7, 80, v7\n"
    "v_add_u32_e32 v6, v7, v6\n"
    "buffer_load_dwordx4 v[32:35], v6, s[20:23], s54 offen\n"
    "buffer_load_dwordx4 v[36:39], v6, s[20:23], s54 offen offset:16\n"
    "buffer_load_dwordx4 v[40:43], v6, s[20:23], s54 offen offset:32\n"
    "buffer_load_dwordx4 v[44:47], v6, s[20:23], s54 offen offset:48\n"
    "buffer_load_dwordx4 v[48:51], v6, s[20:23], s54 offen offset:64\n"
    ".Lmk_pj_1:\n"
    "s_mul_i32 s61, s26, 7168\n"
    "v_add_u32_e32 v11, s61, v0\n"
    "v_lshrrev_b32_e32 v12, 5, v5\n"
    "v_lshlrev_b32_e32 v12, 3, v12\n"
    "v_bfe_u32 v14, v5, 2, 2\n"
    "v_add_u32_e32 v12, v14, v12\n"
    "v_mul_u32_u24_e32 v12, 80, v12\n"
    "v_add_u32_e32 v12, s61, v12\n"
    "v_and_b32_e32 v14, 1, v5\n"
    "v_mul_u32_u24_e32 v14, 40, v14\n"
    "v_add_u32_e32 v13, v14, v12\n"
    "v_add_u32_e32 v13, 32, v13\n"
    "v_bfe_u32 v14, v5, 4, 1\n"
    "v_mul_u32_u24_e32 v14, 40, v14\n"
    "v_add_u32_e32 v12, v14, v12\n"
    "v_and_b32_e32 v14, 3, v5\n"
    "v_lshl_add_u32 v12, v14, 3, v12\n"
    "s_waitcnt vmcnt(0)\n"
    "v_add_co_u32_e32 v2, vcc, 1, v2\n"
    "s_nop 1\n"
    "v_addc_co_u32_e32 v3, vcc, 0, v3, vcc\n"
    "v_readfirstlane_b32 s24, v2\n"
    "v_readfirstlane_b32 s25, v3\n"
    "s_cmp_lt_u32 s26, 2\n"
    "s_cselect_b32 s55, s52, 2\n"
    "s_cmp_lg_u32 s55, 2\n"
    "s_cbranch_scc0 .Lmk_pn_5\n"
    "ds_write_b128 v11, v[32:35]\n"
    "ds_write_b128 v11, v[36:39] offset:1024\n"
    "ds_write_b128 v11, v[40:43] offset:2048\n"
    "ds_write_b128 v11, v[44:47] offset:3072\n"
    "ds_write_b128 v11, v[48:51] offset:4096\n"
    "ds_write_b128 v11, v[52:55] offset:5120\n"
    "ds_write_b128 v11, v[56:59] offset:6144\n"
    "s_add_u32 s63, s52, 1\n"
    "s_and_b32 s63, s63, 3\n"
    "s_mul_i32 s63, s63, 40960\n"
    "s_mul_i32 s65, s51, 153600\n"
    "s_add_u32 s65, s65, 0xa4000\n"
    "s_add_u32 s61, s65, s63\n"
    "s_mul_i32 s63, s53, 5120\n"
    "s_add_u32 s61, s61, s63\n"
    "s_mul_i32 s63, s53, 3840\n"
    "s_add_u32 s65, s65, s63\n"
    "s_add_u32 s65, s65, 0x1e000\n"
    "s_lshl_b32 s66, s51, 12\n"
    "s_lshl_b32 s63, s53, 7\n"
    "s_add_u32 s66, s66, s63\n"
    "s_add_u32 s66, s66, 0x2fc000\n"
    "s_lshl_b32 s63, s52, 10\n"
    "s_add_u32 s62, s66, s63\n"
    "s_lshl_b32 s67, s52, 1\n"
    "s_lshr_b32 s67, 0x48, s67\n"
    "s_and_b32 s67, s67, 3\n"
    "s_lshl_b32 s63, s67, 5\n"
    "s_add_u32 s66, s66, s63\n"
    "s_add_u32 s66, s66, 2048\n"
    "s_lshl_b32 s63, s67, 7\n"
    "s_add_u32 s65, s65, s63\n"
    "ds_read_b64_tr_b16 v[96:97], v12 offset:0\n"
    "ds_read_b64_tr_b16 v[98:99], v12 offset:320\n"
    "ds_read_b64_tr_b16 v[100:101], v12 offset:1280\n"
    "ds_read_b64_tr_b16 v[102:103], v12 offset:1600\n"
    "ds_read_b64_tr_b16 v[104:105], v12 offset:2560\n"
    "ds_read_b64_tr_b16 v[106:107], v12 offset:2880\n"
    "ds_read_b64_tr_b16 v[108:109], v12 offset:3840\n"
    "ds_read_b64_tr_b16 v[110:111], v12 offset:4160\n"
    "s_waitcnt lgkmcnt(13)\n"
    "ds_read_b64_tr_b16 v[112:113], v12 offset:5120\n"
    "ds_read_b64_tr_b16 v[114:115], v12 offset:5440\n"
    "s_waitcnt lgkmcnt(13)\n"
    "ds_read_b64_tr_b16 v[116:117], v13 offset:0\n"
    "ds_read_b64_tr_b16 v[118:119], v13 offset:320\n"
    "s_waitcnt lgkmcnt(13)\n"
    "ds_read_b64_tr_b16 v[120:121], v13 offset:1280\n"
    "ds_read_b64_tr_b16 v[122:123], v13 offset:1600\n"
    "s_waitcnt lgkmcnt(13)\n"
    "ds_read_b64_tr_b16 v[124:125], v13 offset:2560\n"
    "ds_read_b64_tr_b16 v[126:127], v13 offset:2880\n"
    "s_waitcnt lgkmcnt(13)\n"
    "ds_read_b64_tr_b16 v[128:129], v13 offset:3840\n"
    "ds_read_b64_tr_b16 v[130:131], v13 offset:4160\n"
    "s_waitcnt lgkmcnt(13)\n"
    "ds_read_b64_tr_b16 v[132:133], v13 offset:5120\n"
    "ds_read_b64_tr_b16 v[134:135], v13 offset:5440\n"
    "v_and_b32_e32 v14, 7, v5\n"
    "v_lshrrev_b32_e32 v15, 5, v5\n"
    "v_mul_u32_u24_e32 v15, 24, v15\n"
    "v_add_u32_e32 v14, v15, v14\n"
    "v_lshlrev_b32_e32 v14, 4, v14\n"
    "v_and_b32_e32 v15, 0x18, v5\n"
    "v_lshl_add_u32 v14, v15, 26, v14\n"
    "buffer_store_dwordx4 v[96:99], v0, s[16:19], s61 offen sc1\n"
    "buffer_store_dwordx4 v[100:103], v0, s[16:19], s61 offen offset:1024 sc1\n"
    "s_waitcnt lgkmcnt(14)\n"
    "buffer_store_dwordx4 v[104:107], v0, s[16:19], s61 offen offset:2048 sc1\n"
    "s_waitcnt lgkmcnt(12)\n"
    "buffer_store_dwordx4 v[108:111], v0, s[16:19], s61 offen offset:3072 sc1\n"
    "s_add_u32 s61, s61, 4096\n"
    "s_waitcnt lgkmcnt(10)\n"
    "buffer_store_dwordx4 v[112:115], v0, s[16:19], s61 offen sc1\n"
    "s_waitcnt lgkmcnt(8)\n"
    "buffer_store_dwordx4 v[116:119], v14, s[16:19], s65 offen sc1\n"
    "s_waitcnt lgkmcnt(6)\n"
    "buffer_store_dwordx4 v[120:123], v14, s[16:19], s65 offen offset:768 sc1\n"
    "s_waitcnt lgkmcnt(4)\n"
    "buffer_store_dwordx4 v[124:127], v14, s[16:19], s65 offen offset:1536 sc1\n"
    "s_waitcnt lgkmcnt(2)\n"
    "buffer_store_dwordx4 v[128:131], v14, s[16:19], s65 offen offset:2304 sc1\n"
    "s_waitcnt lgkmcnt(0)\n"
    "buffer_store_dwordx4 v[132:135], v14, s[16:19], s65 offen offset:3072 sc1\n"
    "v_mov_b32_e32 v180, 0x3f803f80\n"
    "v_mov_b32_e32 v181, 0x3f803f80\n"
    "v_mov_b32_e32 v182, 0x3f803f80\n"
    "v_mov_b32_e32 v183, 0x3f803f80\n"
    "s_nop 1\n"
    "v_mfma_f32_32x32x16_bf16 v[140:155], v[180:183], v[96:99], 0\n"
    "v_mfma_f32_32x32x16_bf16 v[156:171], v[180:183], v[116:119], 0\n"
    "v_mfma_f32_32x32x16_bf16 v[140:155], v[180:183], v[100:103], v[140:155]\n"
    "v_mfma_f32_32x32x16_bf16 v[156:171], v[180:183], v[120:123], v[156:171]\n"
    "v_mfma_f32_32x32x16_bf16 v[140:155], v[180:183], v[104:107], v[140:155]\n"
    "v_mfma_f32_32x32x16_bf16 v[156:171], v[180:183], v[124:127], v[156:171]\n"
    "v_mfma_f32_32x32x16_bf16 v[140:155], v[180:183], v[108:111], v[140:155]\n"
    "v_mfma_f32_32x32x16_bf16 v[156:171], v[180:183], v[128:131], v[156:171]\n"
    "v_mfma_f32_32x32x16_bf16 v[140:155], v[180:183], v[112:115], v[140:155]\n"
    "v_mfma_f32_32x32x16_bf16 v[156:171], v[180:183], v[132:135], v[156:171]\n"
    "v_lshrrev_b32_e32 v184, 2, v0\n"
    "v_mov_b32_e32 v185, 0x7c000000\n"
    "v_cmp_gt_u32_e32 vcc, 512, v0\n"
    "s_nop 1\n"
    "v_cndmask_b32_e32 v184, v185, v184, vcc\n"
    "v_lshrrev_b32_e32 v186, 2, v0\n"
    "v_mov_b32_e32 v187, 0x7c000000\n"
    "v_cmp_gt_u32_e32 vcc, 128, v0\n"
    "s_nop 1\n"
    "v_cndmask_b32_e32 v186, v187, v186, vcc\n"
    "buffer_store_dword v140, v184, s[16:19], s62 offen sc1\n"
    "buffer_store_dword v156, v186, s[16:19], s66 offen sc1\n"
    "s_branch .Lmk_pj_4\n"
    ".Lmk_pn_5:\n"
    "s_cmp_lt_u32 s26, 2\n"
    "s_cbranch_scc0 .Lmk_pn_6\n"
    "s_branch .Lmk_pj_4\n"
    ".Lmk_pn_6:\n"
    "v_lshrrev_b32_e32 v15, 3, v5\n"
    "v_mul_u32_u24_e32 v15, 5, v15\n"
    "v_and_b32_e32 v16, 7, v5\n"
    "v_lshlrev_b32_e32 v16, 4, v16\n"
    "v_lshrrev_b32_e32 v13, 1, v15\n"
    "v_lshlrev_b32_e32 v13, 10, v13\n"
    "v_and_b32_e32 v17, 1, v15\n"
    "v_lshl_add_u32 v13, v17, 9, v13\n"
    "v_add_u32_e32 v13, v16, v13\n"
    "v_add_u32_e32 v15, 1, v15\n"
    "v_lshrrev_b32_e32 v14, 1, v15\n"
    "v_lshlrev_b32_e32 v14, 10, v14\n"
    "v_and_b32_e32 v17, 1, v15\n"
    "v_lshl_add_u32 v14, v17, 9, v14\n"
    "v_add_u32_e32 v14, v16, v14\n"
    "s_lshr_b32 s61, s78, 2\n"
    "s_mul_i32 s61, s61, 40960\n"
    "s_sub_u32 s63, s26, 2\n"
    "s_mul_i32 s64, s63, 20480\n"
    "s_add_u32 s61, s61, s64\n"
    "s_and_b32 s64, s78, 3\n"
    "s_lshl_b32 s64, s64, 7\n"
    "s_add_u32 s61, s61, s64\n"
    "s_add_u32 s61, s61, 0x82d800\n"
    "buffer_store_dwordx4 v[32:35], v13, s[16:19], s61 offen sc1\n"
    "buffer_store_dwordx4 v[36:39], v14, s[16:19], s61 offen sc1\n"
    "buffer_store_dwordx4 v[40:43], v13, s[16:19], s61 offen offset:1024 sc1\n"
    "buffer_store_dwordx4 v[44:47], v14, s[16:19], s61 offen offset:1024 sc1\n"
    "buffer_store_dwordx4 v[48:51], v13, s[16:19], s61 offen offset:2048 sc1\n"
    "v_lshlrev_b32_e32 v140, 16, v32\n"
    "v_and_b32_e32 v141, 0xffff0000, v32\n"
    "v_lshlrev_b32_e32 v142, 16, v33\n"
    "v_and_b32_e32 v143, 0xffff0000, v33\n"
    "v_lshlrev_b32_e32 v144, 16, v34\n"
    "v_and_b32_e32 v145, 0xffff0000, v34\n"
    "v_lshlrev_b32_e32 v146, 16, v35\n"
    "v_and_b32_e32 v147, 0xffff0000, v35\n"
    "v_lshlrev_b32_e32 v148, 16, v36\n"
    "v_and_b32_e32 v149, 0xffff0000, v36\n"
    "v_lshlrev_b32_e32 v150, 16, v37\n"
    "v_and_b32_e32 v151, 0xffff0000, v37\n"
    "v_lshlrev_b32_e32 v152, 16, v38\n"
    "v_and_b32_e32 v153, 0xffff0000, v38\n"
    "v_lshlrev_b32_e32 v154, 16, v39\n"
    "v_and_b32_e32 v155, 0xffff0000, v39\n"
    "v_lshlrev_b32_e32 v156, 16, v40\n"
    "v_and_b32_e32 v157, 0xffff0000, v40\n"
    "v_lshlrev_b32_e32 v158, 16, v41\n"
    "v_and_b32_e32 v159, 0xffff0000, v41\n"
    "v_lshlrev_b32_e32 v160, 16, v42\n"
    "v_and_b32_e32 v161, 0xffff0000, v42\n"
    "v_lshlrev_b32_e32 v162, 16, v43\n"
    "v_and_b32_e32 v163, 0xffff0000, v43\n"
    "v_lshlrev_b32_e32 v164, 16, v44\n"
    "v_and_b32_e32 v165, 0xffff0000, v44\n"
    "v_lshlrev_b32_e32 v166, 16, v45\n"
    "v_and_b32_e32 v167, 0xffff0000, v45\n"
    "v_lshlrev_b32_e32 v168, 16, v46\n"
    "v_and_b32_e32 v169, 0xffff0000, v46\n"
    "v_lshlrev_b32_e32 v170, 16, v47\n"
    "v_and_b32_e32 v171, 0xffff0000, v47\n"
    "v_lshlrev_b32_e32 v172, 16, v48\n"
    "v_and_b32_e32 v173, 0xffff0000, v48\n"
    "v_lshlrev_b32_e32 v174, 16, v49\n"
    "v_and_b32_e32 v175, 0xffff0000, v49\n"
    "v_lshlrev_b32_e32 v176, 16, v50\n"
    "v_and_b32_e32 v177, 0xffff0000, v50\n"
    "v_lshlrev_b32_e32 v178, 16, v51\n"
    "v_and_b32_e32 v179, 0xffff0000, v51\n"
    "v_pk_add_f32 v[16:17], v[140:141], v[142:143]\n"
    "v_pk_add_f32 v[18:19], v[144:145], v[146:147]\n"
    "v_pk_add_f32 v[16:17], v[16:17], v[148:149]\n"
    "v_pk_add_f32 v[18:19], v[18:19], v[150:151]\n"
    "v_pk_add_f32 v[16:17], v[16:17], v[152:153]\n"
    "v_pk_add_f32 v[18:19], v[18:19], v[154:155]\n"
    "v_pk_add_f32 v[16:17], v[16:17], v[156:157]\n"
    "v_pk_add_f32 v[18:19], v[18:19], v[158:159]\n"
    "v_pk_add_f32 v[16:17], v[16:17], v[160:161]\n"
    "v_pk_add_f32 v[18:19], v[18:19], v[162:163]\n"
    "v_pk_add_f32 v[16:17], v[16:17], v[164:165]\n"
    "v_pk_add_f32 v[18:19], v[18:19], v[166:167]\n"
    "v_pk_add_f32 v[16:17], v[16:17], v[168:169]\n"
    "v_pk_add_f32 v[18:19], v[18:19], v[170:171]\n"
    "v_pk_add_f32 v[16:17], v[16:17], v[172:173]\n"
    "v_pk_add_f32 v[18:19], v[18:19], v[174:175]\n"
    "v_pk_add_f32 v[16:17], v[16:17], v[176:177]\n"
    "v_pk_add_f32 v[18:19], v[18:19], v[178:179]\n"
    "v_pk_add_f32 v[16:17], v[16:17], v[18:19]\n"
    "v_add_f32_e32 v16, v16, v17\n"
    "s_nop 1\n"
    "v_add_f32_dpp v16, v16, v16 row_ror:8 row_mask:0xf bank_mask:0xf\n"
    "v_mul_f32_e32 v16, 0x3c4ccccd, v16\n"
    "v_sub_f32_e32 v18, 0, v16\n"
    "v_mov_b32_e32 v19, v18\n"
    "v_pk_add_f32 v[140:141], v[140:141], v[18:19]\n"
    "v_pk_mul_f32 v[20:21], v[140:141], v[140:141]\n"
    "v_pk_add_f32 v[142:143], v[142:143], v[18:19]\n"
    "v_pk_mul_f32 v[22:23], v[142:143], v[142:143]\n"
    "v_pk_add_f32 v[144:145], v[144:145], v[18:19]\n"
    "v_pk_fma_f32 v[20:21], v[144:145], v[144:145], v[20:21]\n"
    "v_pk_add_f32 v[146:147], v[146:147], v[18:19]\n"
    "v_pk_fma_f32 v[22:23], v[146:147], v[146:147], v[22:23]\n"
    "v_pk_add_f32 v[148:149], v[148:149], v[18:19]\n"
    "v_pk_fma_f32 v[20:21], v[148:149], v[148:149], v[20:21]\n"
    "v_pk_add_f32 v[150:151], v[150:151], v[18:19]\n"
    "v_pk_fma_f32 v[22:23], v[150:151], v[150:151], v[22:23]\n"
    "v_pk_add_f32 v[152:153], v[152:153], v[18:19]\n"
    "v_pk_fma_f32 v[20:21], v[152:153], v[152:153], v[20:21]\n"
    "v_pk_add_f32 v[154:155], v[154:155], v[18:19]\n"
    "v_pk_fma_f32 v[22:23], v[154:155], v[154:155], v[22:23]\n"
    "v_pk_add_f32 v[156:157], v[156:157], v[18:19]\n"
    "v_pk_fma_f32 v[20:21], v[156:157], v[156:157], v[20:21]\n"
    "v_pk_add_f32 v[158:159], v[158:159], v[18:19]\n"
    "v_pk_fma_f32 v[22:23], v[158:159], v[158:159], v[22:23]\n"
    "v_pk_add_f32 v[160:161], v[160:161], v[18:19]\n"
    "v_pk_fma_f32 v[20:21], v[160:161], v[160:161], v[20:21]\n"
    "v_pk_add_f32 v[162:163], v[162:163], v[18:19]\n"
    "v_pk_fma_f32 v[22:23], v[162:163], v[162:163], v[22:23]\n"
    "v_pk_add_f32 v[164:165], v[164:165], v[18:19]\n"
    "v_pk_fma_f32 v[20:21], v[164:165], v[164:165], v[20:21]\n"
    "v_pk_add_f32 v[166:167], v[166:167], v[18:19]\n"
    "v_pk_fma_f32 v[22:23], v[166:167], v[166:167], v[22:23]\n"
    "v_pk_add_f32 v[168:169], v[168:169], v[18:19]\n"
    "v_pk_fma_f32 v[20:21], v[168:169], v[168:169], v[20:21]\n"
    "v_pk_add_f32 v[170:171], v[170:171], v[18:19]\n"
    "v_pk_fma_f32 v[22:23], v[170:171], v[170:171], v[22:23]\n"
    "v_pk_add_f32 v[172:173], v[172:173], v[18:19]\n"
    "v_pk_fma_f32 v[20:21], v[172:173], v[172:173], v[20:21]\n"
    "v_pk_add_f32 v[174:175], v[174:175], v[18:19]\n"
    "v_pk_fma_f32 v[22:23], v[174:175], v[174:175], v[22:23]\n"
    "v_pk_add_f32 v[176:177], v[176:177], v[18:19]\n"
    "v_pk_fma_f32 v[20:21], v[176:177], v[176:177], v[20:21]\n"
    "v_pk_add_f32 v[178:179], v[178:179], v[18:19]\n"
    "v_pk_fma_f32 v[22:23], v[178:179], v[178:179], v[22:23]\n"
    "v_pk_add_f32 v[20:21], v[20:21], v[22:23]\n"
    "v_add_f32_e32 v17, v20, v21\n"
    "s_nop 1\n"
    "v_add_f32_dpp v17, v17, v17 row_ror:8 row_mask:0xf bank_mask:0xf\n"
    "s_lshr_b32 s62, s78, 1\n"
    "s_lshl_b32 s62, s62, 10\n"
    "s_lshl_b32 s64, s63, 9\n"
    "s_add_u32 s62, s62, s64\n"
    "s_and_b32 s64, s78, 1\n"
    "s_lshl_b32 s64, s64, 6\n"
    "s_add_u32 s62, s62, s64\n"
    "s_add_u32 s62, s62, 0x122d800\n"
    "v_lshrrev_b32_e32 v15, 4, v5\n"
    "v_lshlrev_b32_e32 v15, 7, v15\n"
    "v_and_b32_e32 v20, 7, v5\n"
    "v_lshl_add_u32 v15, v20, 3, v15\n"
    "v_and_b32_e32 v20, 8, v5\n"
    "v_lshl_add_u32 v15, v20, 27, v15\n"
    "buffer_store_dwordx2 v[16:17], v15, s[16:19], s62 offen sc1\n"
    ".Lmk_pj_4:\n"
    "s_lshr_b32 s56, s78, 3\n"
    "s_lshl_b32 s56, s56, 2\n"
    "s_add_u32 s56, s56, s26\n"
    "s_and_b32 s69, s78, 7\n"
    "s_cmp_lt_u32 s69, 4\n"
    "s_cselect_b32 s50, 24, 16\n"
    "s_add_u32 s50, s50, 54\n"
    "s_cmp_lt_u32 s56, 54\n"
    "s_cbranch_scc0 .Lmk_pn_8\n"
    "s_mov_b32 s20, s74\n"
    "s_and_b32 s21, s75, 0xffff\n"
    "s_mov_b32 s22, 0x21c000\n"
    "s_mov_b32 s61, s56\n"
    "s_mov_b32 s62, s69\n"
    "s_mul_i32 s60, s62, 276480\n"
    "s_lshl_b32 s63, s61, 6\n"
    "s_add_u32 s60, s60, s63\n"
    "v_lshrrev_b32_e32 v7, 2, v5\n"
    "v_mul_u32_u24_e32 v7, 3456, v7\n"
    "v_and_b32_e32 v8, 3, v5\n"
    "v_lshl_add_u32 v7, v8, 4, v7\n"
    "buffer_load_dwordx4 v[72:75], v7, s[20:23], s60 offen\n"
    "s_add_u32 s60, s60, 55296\n"
    "buffer_load_dwordx4 v[76:79], v7, s[20:23], s60 offen\n"
    "s_add_u32 s60, s60, 55296\n"
    "buffer_load_dwordx4 v[80:83], v7, s[20:23], s60 offen\n"
    "s_add_u32 s60, s60, 55296\n"
    "buffer_load_dwordx4 v[84:87], v7, s[20:23], s60 offen\n"
    "s_add_u32 s60, s60, 55296\n"
    "buffer_load_dwordx4 v[88:91], v7, s[20:23], s60 offen\n"
    "s_mul_i32 s57, s61, 40960\n"
    "s_mul_i32 s63, s62, 5120\n"
    "s_add_u32 s57, s57, s63\n"
    "s_add_u32 s57, s57, 0x30c000\n"
    "s_lshl_b32 s58, s61, 10\n"
    "s_lshl_b32 s63, s62, 7\n"
    "s_add_u32 s58, s58, s63\n"
    "s_add_u32 s58, s58, 0x528000\n"
    "s_branch .Lmk_pj_7\n"
    ".Lmk_pn_8:\n"
    "s_cmp_lt_u32 s56, s50\n"
    "s_cbranch_scc0 .Lmk_pn_9\n"
    "s_mov_b32 s20, s72\n"
    "s_and_b32 s21, s73, 0xffff\n"
    "s_mov_b32 s22, 0xc8000\n"
    "s_sub_u32 s61, s56, 54\n"
    "s_and_b32 s62, s61, 7\n"
    "s_lshr_b32 s61, s61, 3\n"
    "s_mul_i32 s63, s69, 3\n"
    "s_lshl_b32 s64, s69, 1\n"
    "s_add_u32 s64, s64, 4\n"
    "s_cmp_lt_u32 s69, 4\n"
    "s_cselect_b32 s63, s63, s64\n"
    "s_add_u32 s63, s63, s61\n"
    "s_mul_i32 s64, s63, 52\n"
    "s_lshr_b32 s64, s64, 8\n"
    "s_mul_i32 s65, s64, 5\n"
    "s_sub_u32 s65, s63, s65\n"
    "s_lshl_b32 s65, s65, 2\n"
    "s_mul_i32 s61, s62, 20\n"
    "s_add_u32 s61, s61, s63\n"
    "s_mul_i32 s60, s64, 204800\n"
    "s_mul_i32 s66, s62, 160\n"
    "s_add_u32 s60, s60, s66\n"
    "v_lshrrev_b32_e32 v8, 4, v5\n"
    "v_bfe_u32 v9, v5, 1, 2\n"
    "v_bfe_u32 v10, v5, 3, 1\n"
    "v_add_u32_e32 v8, s65, v8\n"
    "v_mul_u32_u24_e32 v11, 52, v8\n"
    "v_lshrrev_b32_e32 v11, 8, v11\n"
    "v_mul_u32_u24_e32 v12, 5, v11\n"
    "v_sub_u32_e32 v12, v8, v12\n"
    "v_lshrrev_b32_e32 v13, 1, v12\n"
    "v_mul_u32_u24_e32 v13, 20, v13\n"
    "v_and_b32_e32 v14, 1, v12\n"
    "v_lshl_add_u32 v13, v14, 2, v13\n"
    "v_lshl_add_u32 v13, v10, 3, v13\n"
    "v_mul_u32_u24_e32 v14, 20, v10\n"
    "v_add_u32_e32 v14, 16, v14\n"
    "v_cmp_eq_u32_e32 vcc, 4, v12\n"
    "s_nop 1\n"
    "v_cndmask_b32_e32 v13, v13, v14, vcc\n"
    "v_add_u32_e32 v13, v9, v13\n"
    "v_mul_u32_u24_e32 v11, 40, v11\n"
    "v_add_u32_e32 v7, v11, v13\n"
    "v_mul_u32_u24_e32 v7, 1280, v7\n"
    "v_and_b32_e32 v8, 1, v5\n"
    "v_lshl_add_u32 v7, v8, 4, v7\n"
    "buffer_load_dwordx4 v[72:75], v7, s[20:23], s60 offen\n"
    "buffer_load_dwordx4 v[76:79], v7, s[20:23], s60 offen offset:32\n"
    "buffer_load_dwordx4 v[80:83], v7, s[20:23], s60 offen offset:64\n"
    "buffer_load_dwordx4 v[84:87], v7, s[20:23], s60 offen offset:96\n"
    "buffer_load_dwordx4 v[88:91], v7, s[20:23], s60 offen offset:128\n"
    "s_mul_i32 s57, s61, 5120\n"
    "s_add_u32 s57, s57, 0x535800\n"
    "s_mov_b32 s59, 0\n"
    "s_branch .Lmk_pj_7\n"
    ".Lmk_pn_9:\n"
    "s_mov_b32 s20, s76\n"
    "s_and_b32 s21, s77, 0xffff\n"
    "s_mov_b32 s22, 0x21c000\n"
    "s_sub_u32 s61, s56, s50\n"
    "s_and_b32 s62, s61, 7\n"
    "s_lshr_b32 s64, s61, 3\n"
    "s_mul_i32 s63, s69, 6\n"
    "s_mul_i32 s65, s69, 7\n"
    "s_sub_u32 s65, s65, 4\n"
    "s_cmp_lt_u32 s69, 4\n"
    "s_cselect_b32 s63, s63, s65\n"
    "s_add_u32 s63, s63, s64\n"
    "s_sub_u32 s61, s56, 126\n"
    "s_lshl_b32 s64, s69, 1\n"
    "s_add_u32 s61, s61, s64\n"
    "s_and_b32 s64, s61, 7\n"
    "s_lshr_b32 s65, s61, 3\n"
    "s_add_u32 s65, s65, 52\n"
    "s_cmp_lt_u32 s56, 126\n"
    "s_cselect_b32 s62, s62, s64\n"
    "s_cselect_b32 s63, s63, s65\n"
    "s_mul_i32 s60, s63, 40960\n"
    "s_mul_i32 s66, s62, 160\n"
    "s_add_u32 s60, s60, s66\n"
    "v_lshrrev_b32_e32 v8, 4, v5\n"
    "v_bfe_u32 v9, v5, 1, 2\n"
    "v_bfe_u32 v10, v5, 3, 1\n"
    "v_and_b32_e32 v11, 1, v8\n"
    "v_lshlrev_b32_e32 v11, 4, v11\n"
    "v_lshrrev_b32_e32 v8, 1, v8\n"
    "v_lshl_add_u32 v11, v8, 2, v11\n"
    "v_add_u32_e32 v11, v9, v11\n"
    "v_lshl_add_u32 v7, v10, 3, v11\n"
    "v_mul_u32_u24_e32 v7, 1280, v7\n"
    "v_and_b32_e32 v8, 1, v5\n"
    "v_lshl_add_u32 v7, v8, 4, v7\n"
    "buffer_load_dwordx4 v[72:75], v7, s[20:23], s60 offen\n"
    "buffer_load_dwordx4 v[76:79], v7, s[20:23], s60 offen offset:32\n"
    "buffer_load_dwordx4 v[80:83], v7, s[20:23], s60 offen offset:64\n"
    "buffer_load_dwordx4 v[84:87], v7, s[20:23], s60 offen offset:96\n"
    "buffer_load_dwordx4 v[88:91], v7, s[20:23], s60 offen offset:128\n"
    "s_sub_u32 s66, s63, 24\n"
    "s_mul_i32 s64, s66, 86\n"
    "s_lshr_b32 s64, s64, 8\n"
    "s_mul_i32 s65, s64, 3\n"
    "s_sub_u32 s65, s66, s65\n"
    "s_add_u32 s64, s64, 6\n"
    "s_lshr_b32 s66, s63, 2\n"
    "s_and_b32 s67, s63, 3\n"
    "s_cmp_lt_u32 s63, 24\n"
    "s_cselect_b32 s64, s66, s64\n"
    "s_cselect_b32 s65, s67, s65\n"
    "s_and_b32 s66, s64, 3\n"
    "s_lshr_b32 s67, s64, 2\n"
    "s_lshl_b32 s68, s65, 2\n"
    "s_add_u32 s67, s67, s68\n"
    "s_lshl_b32 s68, s62, 2\n"
    "s_add_u32 s68, s68, s66\n"
    "s_mul_i32 s68, s68, 71680\n"
    "s_add_u32 s68, s68, 0x5fd800\n"
    "s_mul_i32 s57, s67, 5120\n"
    "s_add_u32 s57, s57, s68\n"
    "s_add_u32 s58, s68, 66560\n"
    "s_cmp_eq_u32 s65, 3\n"
    "s_cselect_b32 s59, s66, 0\n"
    "s_lshr_b32 s59, s59, 1\n"
    ".Lmk_pj_7:\n"
    "s_waitcnt vmcnt(5)\n"
    "s_barrier\n"
    "s_lshl_b32 s61, s78, 3\n"
    "s_add_u32 s61, s61, 1024\n"
    "buffer_store_dwordx2 v[2:3], v4, s[16:19], s61 offen sc1\n"
    "s_cmp_lt_u32 s56, 54\n"
    "s_cbranch_scc0 .Lmk_pn_11\n"
    "s_mul_i32 s61, s26, 5120\n"
    "s_add_u32 s61, s61, 16384\n"
    "v_add_u32_e32 v11, s61, v0\n"
    "v_lshrrev_b32_e32 v12, 5, v5\n"
    "v_lshlrev_b32_e32 v12, 9, v12\n"
    "v_bfe_u32 v13, v5, 2, 2\n"
    "v_lshl_add_u32 v12, v13, 6, v12\n"
    "v_bfe_u32 v13, v5, 4, 1\n"
    "v_lshl_add_u32 v12, v13, 5, v12\n"
    "v_and_b32_e32 v13, 3, v5\n"
    "v_lshl_add_u32 v12, v13, 3, v12\n"
    "v_add_u32_e32 v12, s61, v12\n"
    "s_waitcnt vmcnt(5)\n"
    "ds_write_b128 v11, v[72:75]\n"
    "s_waitcnt vmcnt(4)\n"
    "ds_write_b128 v11, v[76:79] offset:1024\n"
    "s_waitcnt vmcnt(3)\n"
    "ds_write_b128 v11, v[80:83] offset:2048\n"
    "s_waitcnt vmcnt(2)\n"
    "ds_write_b128 v11, v[84:87] offset:3072\n"
    "s_waitcnt vmcnt(1)\n"
    "ds_write_b128 v11, v[88:91] offset:4096\n"
    "ds_read_b64_tr_b16 v[96:97], v12 offset:0\n"
    "ds_read_b64_tr_b16 v[98:99], v12 offset:256\n"
    "ds_read_b64_tr_b16 v[100:101], v12 offset:1024\n"
    "ds_read_b64_tr_b16 v[102:103], v12 offset:1280\n"
    "ds_read_b64_tr_b16 v[104:105], v12 offset:2048\n"
    "ds_read_b64_tr_b16 v[106:107], v12 offset:2304\n"
    "ds_read_b64_tr_b16 v[108:109], v12 offset:3072\n"
    "ds_read_b64_tr_b16 v[110:111], v12 offset:3328\n"
    "ds_read_b64_tr_b16 v[112:113], v12 offset:4096\n"
    "ds_read_b64_tr_b16 v[114:115], v12 offset:4352\n"
    "s_waitcnt lgkmcnt(8)\n"
    "buffer_store_dwordx4 v[96:99], v0, s[16:19], s57 offen sc1\n"
    "s_waitcnt lgkmcnt(6)\n"
    "buffer_store_dwordx4 v[100:103], v0, s[16:19], s57 offen offset:1024 sc1\n"
    "s_waitcnt lgkmcnt(4)\n"
    "buffer_store_dwordx4 v[104:107], v0, s[16:19], s57 offen offset:2048 sc1\n"
    "s_waitcnt lgkmcnt(2)\n"
    "buffer_store_dwordx4 v[108:111], v0, s[16:19], s57 offen offset:3072 sc1\n"
    "s_add_u32 s57, s57, 4096\n"
    "s_waitcnt lgkmcnt(0)\n"
    "buffer_store_dwordx4 v[112:115], v0, s[16:19], s57 offen sc1\n"
    "v_mov_b32_e32 v180, 0x3f803f80\n"
    "v_mov_b32_e32 v181, 0x3f803f80\n"
    "v_mov_b32_e32 v182, 0x3f803f80\n"
    "v_mov_b32_e32 v183, 0x3f803f80\n"
    "s_nop 1\n"
    "v_mfma_f32_32x32x16_bf16 v[140:155], v[180:183], v[96:99], 0\n"
    "v_mfma_f32_32x32x16_bf16 v[140:155], v[180:183], v[100:103], v[140:155]\n"
    "v_mfma_f32_32x32x16_bf16 v[140:155], v[180:183], v[104:107], v[140:155]\n"
    "v_mfma_f32_32x32x16_bf16 v[140:155], v[180:183], v[108:111], v[140:155]\n"
    "v_mfma_f32_32x32x16_bf16 v[140:155], v[180:183], v[112:115], v[140:155]\n"
    "v_lshrrev_b32_e32 v184, 2, v0\n"
    "v_mov_b32_e32 v185, 0x7c000000\n"
    "v_cmp_gt_u32_e32 vcc, 512, v0\n"
    "s_nop 1\n"
    "v_cndmask_b32_e32 v184, v185, v184, vcc\n"
    "s_nop 5\n"
    "buffer_store_dword v140, v184, s[16:19], s58 offen sc1\n"
    "s_branch .Lmk_pj_10\n"
    ".Lmk_pn_11:\n"
    "s_mul_i32 s61, s26, 5120\n"
    "s_add_u32 s61, s61, 16384\n"
    "v_lshrrev_b32_e32 v13, 1, v5\n"
    "v_mul_u32_u24_e32 v13, 160, v13\n"
    "v_and_b32_e32 v14, 1, v5\n"
    "v_lshl_add_u32 v13, v14, 4, v13\n"
    "v_add_u32_e32 v13, s61, v13\n"
    "s_waitcnt vmcnt(5)\n"
    "ds_write_b128 v13, v[72:75]\n"
    "s_waitcnt vmcnt(4)\n"
    "ds_write_b128 v13, v[76:79] offset:32\n"
    "s_waitcnt vmcnt(3)\n"
    "ds_write_b128 v13, v[80:83] offset:64\n"
    "s_waitcnt vmcnt(2)\n"
    "ds_write_b128 v13, v[84:87] offset:96\n"
    "s_waitcnt vmcnt(1)\n"
    "ds_write_b128 v13, v[88:91] offset:128\n"
    "v_lshrrev_b32_e32 v13, 4, v5\n"
    "v_lshlrev_b32_e32 v13, 3, v13\n"
    "v_bfe_u32 v14, v5, 2, 2\n"
    "v_add_u32_e32 v13, v14, v13\n"
    "v_mul_u32_u24_e32 v13, 160, v13\n"
    "v_and_b32_e32 v14, 3, v5\n"
    "v_lshl_add_u32 v13, v14, 3, v13\n"
    "v_add_u32_e32 v13, s61, v13\n"
    "ds_read_b64_tr_b16 v[96:97], v13 offset:0\n"
    "ds_read_b64_tr_b16 v[98:99], v13 offset:640\n"
    "ds_read_b64_tr_b16 v[100:101], v13 offset:32\n"
    "ds_read_b64_tr_b16 v[102:103], v13 offset:672\n"
    "ds_read_b64_tr_b16 v[104:105], v13 offset:64\n"
    "ds_read_b64_tr_b16 v[106:107], v13 offset:704\n"
    "ds_read_b64_tr_b16 v[108:109], v13 offset:96\n"
    "ds_read_b64_tr_b16 v[110:111], v13 offset:736\n"
    "ds_read_b64_tr_b16 v[112:113], v13 offset:128\n"
    "ds_read_b64_tr_b16 v[114:115], v13 offset:768\n"
    "s_waitcnt lgkmcnt(8)\n"
    "buffer_store_dwordx4 v[96:99], v0, s[16:19], s57 offen sc1\n"
    "s_waitcnt lgkmcnt(6)\n"
    "buffer_store_dwordx4 v[100:103], v0, s[16:19], s57 offen offset:1024 sc1\n"
    "s_waitcnt lgkmcnt(4)\n"
    "buffer_store_dwordx4 v[104:107], v0, s[16:19], s57 offen offset:2048 sc1\n"
    "s_waitcnt lgkmcnt(2)\n"
    "buffer_store_dwordx4 v[108:111], v0, s[16:19], s57 offen offset:3072 sc1\n"
    "s_add_u32 s57, s57, 0x1000\n"
    "s_waitcnt lgkmcnt(0)\n"
    "buffer_store_dwordx4 v[112:115], v0, s[16:19], s57 offen sc1\n"
    "s_cmp_eq_u32 s59, 0\n"
    "s_cbranch_scc1 .Lmk_z14_12\n"
    "v_mov_b32_e32 v140, 0\n"
    "v_mov_b32_e32 v141, 0\n"
    "v_mov_b32_e32 v142, 0\n"
    "v_mov_b32_e32 v143, 0\n"
    "buffer_store_dwordx4 v[140:143], v0, s[16:19], s58 offen sc1\n"
    "buffer_store_dwordx4 v[140:143], v0, s[16:19], s58 offen offset:1024 sc1\n"
    "buffer_store_dwordx4 v[140:143], v0, s[16:19], s58 offen offset:2048 sc1\n"
    "buffer_store_dwordx4 v[140:143], v0, s[16:19], s58 offen offset:3072 sc1\n"
    "s_add_u32 s58, s58, 0x1000\n"
    "buffer_store_dwordx4 v[140:143], v0, s[16:19], s58 offen sc1\n"
    ".Lmk_z14_12:\n"
    ".Lmk_pj_10:\n"
    "s_mov_b32 s20, s40\n"
    "s_and_b32 s21, s41, 0xffff\n"
    "s_mov_b32 s22, 0x280000\n"
    "v_and_b32_e32 v10, 0xf0, v0\n"
    "v_lshrrev_b32_e32 v10, 4, v10\n"
    "v_mul_u32_u24_e32 v10, 1280, v10\n"
    "v_lshrrev_b32_e32 v11, 8, v0\n"
    "v_lshl_add_u32 v10, v11, 3, v10\n"
    "s_mul_i32 s61, s33, 20480\n"
    "s_mul_i32 s62, s31, 160\n"
    "s_add_u32 s61, s61, s62\n"
    "buffer_load_dwordx2 a[0:1], v10, s[20:23], s61 offen\n"
    "buffer_load_dwordx2 a[2:3], v10, s[20:23], s61 offen offset:32\n"
    "buffer_load_dwordx2 a[4:5], v10, s[20:23], s61 offen offset:64\n"
    "buffer_load_dwordx2 a[6:7], v10, s[20:23], s61 offen offset:96\n"
    "buffer_load_dwordx2 a[8:9], v10, s[20:23], s61 offen offset:128\n"
    "v_lshrrev_b32_e32 v12, 1, v0\n"
    "v_and_b32_e32 v12, 0x78, v12\n"
    "s_lshl_b32 s50, s28, 7\n"
    "s_lshl_b32 s51, s27, 7\n"
    "v_mov_b32_e32 v13, s50\n"
    "v_mov_b32_e32 v14, s51\n"
    "v_and_b32_e32 v15, 0x100, v0\n"
    "v_cmp_eq_u32_e32 vcc, 0, v15\n"
    "s_nop 1\n"
    "v_cndmask_b32_e32 v13, v14, v13, vcc\n"
    "v_add_u32_e32 v12, v12, v13\n"
    "v_and_b32_e32 v9, 0x1f0, v0\n"
    "v_lshrrev_b32_e32 v9, 2, v9\n"
    "v_and_b32_e32 v10, 0x200, v0\n"
    "v_or_b32_e32 v9, v9, v10\n"
    "s_mul_i32 s51, s28, 0x1000\n"
    "s_add_u32 s51, s51, 0x2fc000\n"
    "s_mov_b32 s50, 1024\n"
    "buffer_load_dwordx2 v[254:255], v12, s[16:19], s50 offen sc1\n"
    "s_waitcnt vmcnt(0)\n"
    "v_cmp_ne_u64_e32 vcc, s[24:25], v[254:255]\n"
    "s_cbranch_vccnz .Lmk_slow_13\n"
    ".Lmk_back_14:\n"
    "buffer_load_dword v16, v9, s[16:19], s51 offen sc1\n"
    "buffer_load_dword v17, v9, s[16:19], s51 offen offset:128 sc1\n"
    "buffer_load_dword v18, v9, s[16:19], s51 offen offset:256 sc1\n"
    "buffer_load_dword v19, v9, s[16:19], s51 offen offset:384 sc1\n"
    "buffer_load_dword v20, v9, s[16:19], s51 offen offset:1024 sc1\n"
    "buffer_load_dword v21, v9, s[16:19], s51 offen offset:1152 sc1\n"
    "buffer_load_dword v22, v9, s[16:19], s51 offen offset:1280 sc1\n"
    "buffer_load_dword v23, v9, s[16:19], s51 offen offset:1408 sc1\n"
    "buffer_load_dword v24, v9, s[16:19], s51 offen offset:2048 sc1\n"
    "buffer_load_dword v25, v9, s[16:19], s51 offen offset:2176 sc1\n"
    "buffer_load_dword v26, v9, s[16:19], s51 offen offset:2304 sc1\n"
    "buffer_load_dword v27, v9, s[16:19], s51 offen offset:2432 sc1\n"
    "buffer_load_dword v28, v9, s[16:19], s51 offen offset:3072 sc1\n"
    "buffer_load_dword v29, v9, s[16:19], s51 offen offset:3200 sc1\n"
    "buffer_load_dword v30, v9, s[16:19], s51 offen offset:3328 sc1\n"
    "buffer_load_dword v31, v9, s[16:19], s51 offen offset:3456 sc1\n"
    "s_mul_i32 s70, s28, 0x25800\n"
    "s_add_u32 s70, s70, 0xae000\n"
    "s_mul_i32 s71, s26, 10240\n"
    "s_add_u32 s70, s70, s71\n"
    "s_add_u32 s71, s71, 0xa000\n"
    "s_mov_b32 m0, s71\n"
    "s_nop 0\n"
    "buffer_load_dwordx4 v0, s[16:19], s70 offen sc1 lds\n"
    "buffer_load_dwordx4 v0, s[16:19], s70 offen offset:1024 sc1 lds\n"
    "buffer_load_dwordx4 v0, s[16:19], s70 offen offset:2048 sc1 lds\n"
    "buffer_load_dwordx4 v0, s[16:19], s70 offen offset:3072 sc1 lds\n"
    "s_add_u32 m0, m0, 0x1000\n"
    "s_add_u32 s70, s70, 0x1000\n"
    "buffer_load_dwordx4 v0, s[16:19], s70 offen sc1 lds\n"
    "buffer_load_dwordx4 v0, s[16:19], s70 offen offset:1024 sc1 lds\n"
    "buffer_load_dwordx4 v0, s[16:19], s70 offen offset:2048 sc1 lds\n"
    "buffer_load_dwordx4 v0, s[16:19], s70 offen offset:3072 sc1 lds\n"
    "s_add_u32 m0, m0, 0x1000\n"
    "s_add_u32 s70, s70, 0x1000\n"
    "buffer_load_dwordx4 v0, s[16:19], s70 offen sc1 lds\n"
    "buffer_load_dwordx4 v0, s[16:19], s70 offen offset:1024 sc1 lds\n"
    "s_mul_i32 s70, s28, 0x25800\n"
    "s_add_u32 s70, s70, 0xb8000\n"
    "s_mul_i32 s71, s26, 10240\n"
    "s_add_u32 s70, s70, s71\n"
    "s_add_u32 s71, s71, 0x14000\n"
    "s_mov_b32 m0, s71\n"
    "s_nop 0\n"
    "buffer_load_dwordx4 v0, s[16:19], s70 offen sc1 lds\n"
    "buffer_load_dwordx4 v0, s[16:19], s70 offen offset:1024 sc1 lds\n"
    "buffer_load_dwordx4 v0, s[16:19], s70 offen offset:2048 sc1 lds\n"
    "buffer_load_dwordx4 v0, s[16:19], s70 offen offset:3072 sc1 lds\n"
    "s_add_u32 m0, m0, 0x1000\n"
    "s_add_u32 s70, s70, 0x1000\n"
    "buffer_load_dwordx4 v0, s[16:19], s70 offen sc1 lds\n"
    "buffer_load_dwordx4 v0, s[16:19], s70 offen offset:1024 sc1 lds\n"
    "buffer_load_dwordx4 v0, s[16:19], s70 offen offset:2048 sc1 lds\n"
    "buffer_load_dwordx4 v0, s[16:19], s70 offen offset:3072 sc1 lds\n"
    "s_add_u32 m0, m0, 0x1000\n"
    "s_add_u32 s70, s70, 0x1000\n"
    "buffer_load_dwordx4 v0, s[16:19], s70 offen sc1 lds\n"
    "buffer_load_dwordx4 v0, s[16:19], s70 offen offset:1024 sc1 lds\n"
    "s_mul_i32 s70, s28, 0x25800\n"
    "s_add_u32 s70, s70, 0xc2000\n"
    "s_mul_i32 s71, s26, 7680\n"
    "s_add_u32 s70, s70, s71\n"
    "s_add_u32 s71, s71, 0x1e000\n"
    "s_mov_b32 m0, s71\n"
    "s_nop 0\n"
    "buffer_load_dwordx4 v0, s[16:19], s70 offen sc1 lds\n"
    "buffer_load_dwordx4 v0, s[16:19], s70 offen offset:1024 sc1 lds\n"
    "buffer_load_dwordx4 v0, s[16:19], s70 offen offset:2048 sc1 lds\n"
    "buffer_load_dwordx4 v0, s[16:19], s70 offen offset:3072 sc1 lds\n"
    "s_add_u32 m0, m0, 0x1000\n"
    "s_add_u32 s70, s70, 0x1000\n"
    "buffer_load_dwordx4 v0, s[16:19], s70 offen sc1 lds\n"
    "buffer_load_dwordx4 v0, s[16:19], s70 offen offset:1024 sc1 lds\n"
    "buffer_load_dwordx4 v0, s[16:19], s70 offen offset:2048 sc1 lds\n"
    "s_mov_b32 exec_hi, 0\n"
    "buffer_load_dwordx4 v0, s[16:19], s70 offen offset:3072 sc1 lds\n"
    "s_mov_b32 exec_hi, -1\n"
    "s_waitcnt vmcnt(42)\n"
    "v_add_f32_e32 v16, v16, v17\n"
    "s_waitcnt vmcnt(40)\n"
    "v_add_f32_e32 v18, v18, v19\n"
    "v_add_f32_e32 v16, v16, v18\n"
    "v_mov_b32_e32 v17, v16\n"
    "s_nop 1\n"
    "v_permlane32_swap_b32 v17, v16\n"
    "v_add_f32_e32 v16, v17, v16\n"
    "v_sub_f32_e32 v16, 0, v16\n"
    "v_cvt_pk_bf16_f32 v17, v16, v16\n"
    "v_lshlrev_b32_e32 v17, 16, v17\n"
    "v_sub_f32_e32 v17, v16, v17\n"
    "v_cvt_pk_bf16_f32 v16, v16, v17\n"
    "v_cndmask_b32_e64 v16, v16, 0, s[48:49]\n"
    "v_accvgpr_write_b32 a10, v16\n"
    "s_waitcnt vmcnt(38)\n"
    "v_add_f32_e32 v20, v20, v21\n"
    "s_waitcnt vmcnt(36)\n"
    "v_add_f32_e32 v22, v22, v23\n"
    "v_add_f32_e32 v20, v20, v22\n"
    "v_mov_b32_e32 v21, v20\n"
    "s_nop 1\n"
    "v_permlane32_swap_b32 v21, v20\n"
    "v_add_f32_e32 v20, v21, v20\n"
    "v_sub_f32_e32 v20, 0, v20\n"
    "v_cvt_pk_bf16_f32 v21, v20, v20\n"
    "v_lshlrev_b32_e32 v21, 16, v21\n"
    "v_sub_f32_e32 v21, v20, v21\n"
    "v_cvt_pk_bf16_f32 v20, v20, v21\n"
    "v_cndmask_b32_e64 v20, v20, 0, s[48:49]\n"
    "v_accvgpr_write_b32 a11, v20\n"
    "s_waitcnt vmcnt(34)\n"
    "v_add_f32_e32 v24, v24, v25\n"
    "s_waitcnt vmcnt(32)\n"
    "v_add_f32_e32 v26, v26, v27\n"
    "v_add_f32_e32 v24, v24, v26\n"
    "v_mov_b32_e32 v25, v24\n"
    "s_nop 1\n"
    "v_permlane32_swap_b32 v25, v24\n"
    "v_add_f32_e32 v24, v25, v24\n"
    "v_sub_f32_e32 v24, 0, v24\n"
    "v_cvt_pk_bf16_f32 v25, v24, v24\n"
    "v_lshlrev_b32_e32 v25, 16, v25\n"
    "v_sub_f32_e32 v25, v24, v25\n"
    "v_cvt_pk_bf16_f32 v24, v24, v25\n"
    "v_cndmask_b32_e64 v24, v24, 0, s[48:49]\n"
    "v_accvgpr_write_b32 a12, v24\n"
    "s_waitcnt vmcnt(30)\n"
    "v_add_f32_e32 v28, v28, v29\n"
    "s_waitcnt vmcnt(28)\n"
    "v_add_f32_e32 v30, v30, v31\n"
    "v_add_f32_e32 v28, v28, v30\n"
    "v_mov_b32_e32 v29, v28\n"
    "s_nop 1\n"
    "v_permlane32_swap_b32 v29, v28\n"
    "v_add_f32_e32 v28, v29, v28\n"
    "v_sub_f32_e32 v28, 0, v28\n"
    "v_cvt_pk_bf16_f32 v29, v28, v28\n"
    "v_lshlrev_b32_e32 v29, 16, v29\n"
    "v_sub_f32_e32 v29, v28, v29\n"
    "v_cvt_pk_bf16_f32 v28, v28, v29\n"
    "v_cndmask_b32_e64 v28, v28, 0, s[48:49]\n"
    "v_accvgpr_write_b32 a13, v28\n"
    ".Lmk_layer:\n"
    "s_mul_i32 s50, s29, 0x500000\n"
    "s_mul_i32 s52, s32, 40960\n"
    "s_add_u32 s50, s50, s52\n"
    "s_add_u32 s50, s50, 0x82d800\n"
    "s_mul_i32 s51, s29, 0x40000\n"
    "s_lshl_b32 s52, s32, 11\n"
    "s_add_u32 s51, s51, s52\n"
    "s_add_u32 s51, s51, 0x122d800\n"
    "v_and_b32_e32 v10, 0xf0, v0\n"
    "v_lshrrev_b32_e32 v10, 1, v10\n"
    "v_and_b32_e32 v11, 0x100, v0\n"
    "v_lshl_or_b32 v10, v11, 2, v10\n"
    "v_and_b32_e32 v11, 0x200, v0\n"
    "v_or_b32_e32 v10, v10, v11\n"
    "s_mov_b32 s73, s50\n"
    "buffer_load_dwordx2 v[20:21], v10, s[16:19], s51 offen sc1\n"
    "buffer_load_dwordx2 v[22:23], v10, s[16:19], s51 offen offset:128 sc1\n"
    "buffer_load_dwordx2 v[24:25], v10, s[16:19], s51 offen offset:256 sc1\n"
    "buffer_load_dwordx2 v[26:27], v10, s[16:19], s51 offen offset:384 sc1\n"
    "buffer_load_dwordx4 v[96:99], v0, s[16:19], s50 offen sc1\n"
    "buffer_load_dwordx4 v[100:103], v0, s[16:19], s50 offen offset:1024 sc1\n"
    "buffer_load_dwordx4 v[104:107], v0, s[16:19], s50 offen offset:2048 sc1\n"
    "buffer_load_dwordx4 v[108:111], v0, s[16:19], s50 offen offset:3072 sc1\n"
    "s_add_u32 s50, s50, 0x1000\n"
    "buffer_load_dwordx4 v[112:115], v0, s[16:19], s50 offen sc1\n"
    "buffer_load_dwordx4 v[116:119], v0, s[16:19], s50 offen offset:1024 sc1\n"
    "buffer_load_dwordx4 v[120:123], v0, s[16:19], s50 offen offset:2048 sc1\n"
    "buffer_load_dwordx4 v[124:127], v0, s[16:19], s50 offen offset:3072 sc1\n"
    "s_add_u32 s50, s50, 0x1000\n"
    "buffer_load_dwordx4 v[128:131], v0, s[16:19], s50 offen sc1\n"
    "buffer_load_dwordx4 v[132:135], v0, s[16:19], s50 offen offset:1024 sc1\n"
    "buffer_load_dwordx4 v[136:139], v0, s[16:19], s50 offen offset:2048 sc1\n"
    "buffer_load_dwordx4 v[140:143], v0, s[16:19], s50 offen offset:3072 sc1\n"
    "s_add_u32 s50, s50, 0x1000\n"
    "buffer_load_dwordx4 v[144:147], v0, s[16:19], s50 offen sc1\n"
    "s_mul_i32 s62, s32, 108\n"
    "s_add_u32 s62, s62, s37\n"
    "s_lshl_b32 s62, s62, 10\n"
    "s_mul_i32 s59, s29, 0x6c0000\n"
    "s_add_u32 s62, s62, s59\n"
    "s_add_u32 s62, s62, 0x24ad800\n"
    "v_and_b32_e32 v80, 0x2f0, v0\n"
    "v_bfe_u32 v81, v0, 8, 1\n"
    "v_mul_u32_u24_e32 v81, 55296, v81\n"
    "v_add_u32_e32 v80, v80, v81\n"
    "s_cmp_lt_u32 s38, 4\n"
    "s_cselect_b32 s59, 0x7c000000, 0\n"
    "v_add_u32_e32 v81, s59, v80\n"
    "v_accvgpr_write_b32 a60, -1\n"
    "v_accvgpr_write_b32 a61, -1\n"
    "v_accvgpr_write_b32 a62, -1\n"
    "v_accvgpr_write_b32 a63, -1\n"
    "s_mul_i32 s63, s29, 0x500000\n"
    "s_lshr_b32 s59, s33, 1\n"
    "s_mul_i32 s59, s59, 40960\n"
    "s_add_u32 s63, s63, s59\n"
    "s_mul_i32 s59, s31, 5120\n"
    "s_add_u32 s63, s63, s59\n"
    "s_add_u32 s63, s63, 0xaad800\n"
    "s_mul_i32 s64, s29, 0x40000\n"
    "s_lshl_b32 s59, s33, 3\n"
    "s_add_u32 s59, s59, s31\n"
    "s_lshl_b32 s59, s59, 7\n"
    "s_add_u32 s64, s64, s59\n"
    "s_add_u32 s64, s64, 0x124d800\n"
    "s_and_b32 s59, s33, 1\n"
    "s_lshl_b32 s59, s59, 8\n"
    "v_and_b32_e32 v82, 0xf0, v0\n"
    "v_and_b32_e32 v84, 0x200, v0\n"
    "v_or_b32_e32 v82, v82, v84\n"
    "v_bfe_u32 v84, v0, 8, 1\n"
    "v_lshl_add_u32 v82, v84, 3, v82\n"
    "v_add_u32_e32 v82, s59, v82\n"
    "v_lshrrev_b32_e32 v83, 1, v0\n"
    "v_mov_b32_e32 v84, 0x7c000000\n"
    "v_cmp_gt_u32_e32 vcc, 0x100, v0\n"
    "s_nop 1\n"
    "v_cndmask_b32_e32 v83, v84, v83, vcc\n"
    "s_mul_i32 s65, s29, 0x0\n"
    "s_lshr_b32 s59, s33, 1\n"
    "s_mul_i32 s59, s59, 40960\n"
    "s_add_u32 s65, s65, s59\n"
    "s_mul_i32 s59, s31, 5120\n"
    "s_add_u32 s65, s65, s59\n"
    "s_add_u32 s65, s65, 0xd2d800\n"
    "s_mul_i32 s66, s29, 0x0\n"
    "s_lshl_b32 s59, s33, 3\n"
    "s_add_u32 s59, s59, s31\n"
    "s_lshl_b32 s59, s59, 7\n"
    "s_add_u32 s66, s66, s59\n"
    "s_add_u32 s66, s66, 0x126d800\n"
    "s_cmp_eq_u32 s29, 0\n"
    "s_cselect_b32 s67, 0, 0x7c000000\n"
    "s_waitcnt vmcnt(13)\n"
    "v_max3_u32 v31, v20, v21, v22\n"
    "v_max3_u32 v31, v31, v23, v24\n"
    "v_max3_u32 v31, v31, v25, v26\n"
    "v_max_u32_e32 v31, v31, v27\n"
    "v_cmp_eq_u32_e32 vcc, -1, v31\n"
    "s_cbranch_vccnz .Lmk_again_15\n"
    ".Lmk_there_16:\n"
    "v_add_u32_e32 v28, 0xa000, v0\n"
    "v_and_b32_e32 v30, 0x1f0, v0\n"
    "v_min_u32_e32 v30, 368, v30\n"
    "v_lshrrev_b32_e32 v31, 9, v0\n"
    "v_mul_u32_u24_e32 v31, 384, v31\n"
    "v_add_u32_e32 v29, v30, v31\n"
    "v_add_u32_e32 v29, 0x1e000, v29\n"
    "v_add_u32_e32 v30, 0x14000, v0\n"
    "s_mul_i32 s90, s28, 0x25800\n"
    "s_add_u32 s90, s90, 0xa4000\n"
    "s_mul_i32 s91, s26, 10240\n"
    "s_add_u32 s90, s90, s91\n"
    "s_add_u32 s91, s91, 0x0\n"
    "s_barrier\n"
    "s_lshl_b32 s55, s78, 3\n"
    "s_add_u32 s55, s55, 4096\n"
    "buffer_store_dwordx2 v[2:3], v4, s[16:19], s55 offen sc1\n"
    "ds_read_b128 a[24:27], v28\n"
    "ds_read_b128 a[28:31], v30\n"
    "ds_read_b128 a[32:35], v29\n"
    "ds_read_b128 a[36:39], v28 offset:1024\n"
    "ds_read_b128 a[40:43], v30 offset:1024\n"
    "ds_read_b128 a[44:47], v29 offset:768\n"
    "s_waitcnt vmcnt(13)\n"
    "v_max3_u32 v31, v96, v97, v98\n"
    "v_max_u32_e32 v31, v31, v99\n"
    "s_waitcnt vmcnt(12)\n"
    "v_max3_u32 v31, v31, v100, v101\n"
    "v_max3_u32 v31, v31, v102, v103\n"
    "s_waitcnt vmcnt(11)\n"
    "v_max3_u32 v31, v31, v104, v105\n"
    "v_max3_u32 v31, v31, v106, v107\n"
    "s_waitcnt vmcnt(10)\n"
    "v_max3_u32 v31, v31, v108, v109\n"
    "v_max3_u32 v31, v31, v110, v111\n"
    "v_cmp_eq_u32_e32 vcc, -1, v31\n"
    "s_cbranch_vccnz .Lmk_again_17\n"
    ".Lmk_there_18:\n"
    "s_waitcnt lgkmcnt(3)\n"
    "v_mfma_f32_32x32x16_bf16 v[32:47], a[24:27], v[96:99], 0\n"
    "ds_read_b128 a[48:51], v28 offset:2048\n"
    "buffer_load_dwordx4 v[148:151], v0, s[16:19], s50 offen offset:1024 sc1\n"
    "v_mfma_f32_32x32x16_bf16 v[48:63], v[96:99], a[28:31], 0\n"
    "ds_read_b128 a[52:55], v30 offset:2048\n"
    "v_mfma_f32_32x32x16_bf16 v[64:79], a[32:35], v[96:99], 0\n"
    "ds_read_b128 a[56:59], v29 offset:1536\n"
    "s_waitcnt vmcnt(10)\n"
    "v_max3_u32 v31, v112, v113, v114\n"
    "v_max_u32_e32 v31, v31, v115\n"
    "s_waitcnt lgkmcnt(3)\n"
    "v_mfma_f32_32x32x16_bf16 v[32:47], a[36:39], v[100:103], v[32:47]\n"
    "ds_read_b128 a[24:27], v28 offset:3072\n"
    "buffer_load_dwordx4 v[152:155], v0, s[16:19], s50 offen offset:2048 sc1\n"
    "v_mfma_f32_32x32x16_bf16 v[48:63], v[100:103], a[40:43], v[48:63]\n"
    "ds_read_b128 a[28:31], v30 offset:3072\n"
    "v_mfma_f32_32x32x16_bf16 v[64:79], a[44:47], v[100:103], v[64:79]\n"
    "ds_read_b128 a[32:35], v29 offset:2304\n"
    "s_waitcnt vmcnt(10)\n"
    "v_max3_u32 v31, v31, v116, v117\n"
    "v_max3_u32 v31, v31, v118, v119\n"
    "s_waitcnt lgkmcnt(3)\n"
    "v_mfma_f32_32x32x16_bf16 v[32:47], a[48:51], v[104:107], v[32:47]\n"
    "ds_read_b128 a[36:39], v28 offset:4096\n"
    "buffer_load_dwordx4 v[156:159], v0, s[16:19], s50 offen offset:3072 sc1\n"
    "v_mfma_f32_32x32x16_bf16 v[48:63], v[104:107], a[52:55], v[48:63]\n"
    "ds_read_b128 a[40:43], v30 offset:4096\n"
    "v_mfma_f32_32x32x16_bf16 v[64:79], a[56:59], v[104:107], v[64:79]\n"
    "ds_read_b128 a[44:47], v29 offset:3072\n"
    "s_waitcnt vmcnt(10)\n"
    "v_max3_u32 v31, v31, v120, v121\n"
    "v_max3_u32 v31, v31, v122, v123\n"
    "s_waitcnt lgkmcnt(3)\n"
    "v_mfma_f32_32x32x16_bf16 v[32:47], a[24:27], v[108:111], v[32:47]\n"
    "ds_read_b128 a[48:51], v28 offset:5120\n"
    "s_add_u32 s50, s50, 0x1000\n"
    "buffer_load_dwordx4 v[160:163], v0, s[16:19], s50 offen sc1\n"
    "v_mfma_f32_32x32x16_bf16 v[48:63], v[108:111], a[28:31], v[48:63]\n"
    "ds_read_b128 a[52:55], v30 offset:5120\n"
    "v_add_f32_e32 v5, v20, v22\n"
    "v_add_f32_e32 v9, v24, v26\n"
    "v_add_f32_e32 v6, v21, v23\n"
    "v_add_f32_e32 v5, v5, v9\n"
    "v_mfma_f32_32x32x16_bf16 v[64:79], a[32:35], v[108:111], v[64:79]\n"
    "ds_read_b128 a[56:59], v29 offset:3840\n"
    "s_waitcnt vmcnt(10)\n"
    "v_max3_u32 v31, v31, v124, v125\n"
    "v_max3_u32 v31, v31, v126, v127\n"
    "v_cmp_eq_u32_e32 vcc, -1, v31\n"
    "s_cbranch_vccnz .Lmk_again_19\n"
    ".Lmk_there_20:\n"
    "v_add_f32_e32 v9, v25, v27\n"
    "v_add_f32_e32 v6, v6, v9\n"
    "v_mov_b32_e32 v9, v5\n"
    "s_waitcnt lgkmcnt(3)\n"
    "v_mfma_f32_32x32x16_bf16 v[32:47], a[36:39], v[112:115], v[32:47]\n"
    "ds_read_b128 a[24:27], v28 offset:6144\n"
    "buffer_load_dwordx4 v[164:167], v0, s[16:19], s50 offen offset:1024 sc1\n"
    "v_mfma_f32_32x32x16_bf16 v[48:63], v[112:115], a[40:43], v[48:63]\n"
    "ds_read_b128 a[28:31], v30 offset:6144\n"
    "v_permlane32_swap_b32 v9, v5\n"
    "v_add_f32_e32 v5, v9, v5\n"
    "v_mul_f32_e32 v5, 0x3e000000, v5\n"
    "v_sub_f32_e32 v20, v20, v5\n"
    "v_mfma_f32_32x32x16_bf16 v[64:79], a[44:47], v[112:115], v[64:79]\n"
    "ds_read_b128 a[32:35], v29 offset:4608\n"
    "s_mov_b32 m0, s91\n"
    "s_nop 0\n"
    "buffer_load_dwordx4 v0, s[16:19], s90 offen sc1 lds\n"
    "s_waitcnt vmcnt(11)\n"
    "v_max3_u32 v31, v128, v129, v130\n"
    "v_max_u32_e32 v31, v31, v131\n"
    "v_sub_f32_e32 v22, v22, v5\n"
    "v_sub_f32_e32 v24, v24, v5\n"
    "v_sub_f32_e32 v26, v26, v5\n"
    "s_waitcnt lgkmcnt(3)\n"
    "v_mfma_f32_32x32x16_bf16 v[32:47], a[48:51], v[116:119], v[32:47]\n"
    "ds_read_b128 a[36:39], v28 offset:7168\n"
    "buffer_load_dwordx4 v[168:171], v0, s[16:19], s50 offen offset:2048 sc1\n"
    "v_mfma_f32_32x32x16_bf16 v[48:63], v[116:119], a[52:55], v[48:63]\n"
    "ds_read_b128 a[40:43], v30 offset:7168\n"
    "v_mul_f32_e32 v9, v20, v20\n"
    "v_fmac_f32_e32 v9, v22, v22\n"
    "v_fmac_f32_e32 v9, v24, v24\n"
    "v_fmac_f32_e32 v9, v26, v26\n"
    "v_mfma_f32_32x32x16_bf16 v[64:79], a[56:59], v[116:119], v[64:79]\n"
    "ds_read_b128 a[44:47], v29 offset:5376\n"
    "s_waitcnt vmcnt(11)\n"
    "v_max3_u32 v31, v31, v132, v133\n"
    "v_max3_u32 v31, v31, v134, v135\n"
    "v_fmac_f32_e32 v6, 0x42a00000, v9\n"
    "v_mov_b32_e32 v9, v6\n"
    "s_nop 1\n"
    "v_permlane32_swap_b32 v9, v6\n"
    "s_waitcnt lgkmcnt(3)\n"
    "v_mfma_f32_32x32x16_bf16 v[32:47], a[24:27], v[120:123], v[32:47]\n"
    "ds_read_b128 a[48:51], v28 offset:8192\n"
    "buffer_load_dwordx4 v[172:175], v0, s[16:19], s50 offen offset:3072 sc1\n"
    "v_mfma_f32_32x32x16_bf16 v[48:63], v[120:123], a[28:31], v[48:63]\n"
    "ds_read_b128 a[52:55], v30 offset:8192\n"
    "v_add_f32_e32 v6, v9, v6\n"
    "v_mul_f32_e32 v6, 0x3acccccd, v6\n"
    "v_add_f32_e32 v6, 0x3727c5ac, v6\n"
    "v_mfma_f32_32x32x16_bf16 v[64:79], a[32:35], v[120:123], v[64:79]\n"
    "ds_read_b128 a[56:59], v29 offset:6144\n"
    "buffer_load_dwordx4 v0, s[16:19], s90 offen offset:1024 sc1 lds\n"
    "s_waitcnt vmcnt(12)\n"
    "v_max3_u32 v31, v31, v136, v137\n"
    "v_max3_u32 v31, v31, v138, v139\n"
    "v_rsq_f32_e32 v7, v6\n"
    "s_nop 0\n"
    "v_mul_f32_e32 v8, 0x3ea52b5c, v7\n"
    "s_waitcnt lgkmcnt(3)\n"
    "v_mfma_f32_32x32x16_bf16 v[32:47], a[36:39], v[124:127], v[32:47]\n"
    "ds_read_b128 a[24:27], v28 offset:9216\n"
    "s_add_u32 s50, s50, 0x1000\n"
    "buffer_load_dwordx4 v[176:179], v0, s[16:19], s50 offen sc1\n"
    "v_mfma_f32_32x32x16_bf16 v[48:63], v[124:127], a[40:43], v[48:63]\n"
    "ds_read_b128 a[28:31], v30 offset:9216\n"
    "v_cvt_pk_bf16_f32 v12, v5, v5\n"
    "v_lshlrev_b32_e32 v9, 16, v12\n"
    "v_sub_f32_e32 v9, v5, v9\n"
    "v_cvt_pk_bf16_f32 v13, v9, v9\n"
    "v_mfma_f32_32x32x16_bf16 v[64:79], a[44:47], v[124:127], v[64:79]\n"
    "ds_read_b128 a[32:35], v29 offset:6912\n"
    "s_waitcnt vmcnt(12)\n"
    "v_max3_u32 v31, v31, v140, v141\n"
    "v_max3_u32 v31, v31, v142, v143\n"
    "v_cmp_eq_u32_e32 vcc, -1, v31\n"
    "s_cbranch_vccnz .Lmk_again_21\n"
    ".Lmk_there_22:\n"
    "v_mov_b32_e32 v14, 0\n"
    "v_mov_b32_e32 v15, 0\n"
    "v_cndmask_b32_e64 v12, v12, 0, s[48:49]\n"
    "s_waitcnt lgkmcnt(3)\n"
    "v_mfma_f32_32x32x16_bf16 v[32:47], a[48:51], v[128:131], v[32:47]\n"
    "ds_read_b128 a[36:39], v28 offset:10240\n"
    "buffer_load_dwordx4 v[180:183], v0, s[16:19], s50 offen offset:1024 sc1\n"
    "v_mfma_f32_32x32x16_bf16 v[48:63], v[128:131], a[52:55], v[48:63]\n"
    "ds_read_b128 a[40:43], v30 offset:10240\n"
    "v_cndmask_b32_e64 v13, v13, 0, s[48:49]\n"
    "s_mul_i32 s52, s26, 2560\n"
    "s_add_u32 s52, s52, 0x25800\n"
    "v_and_b32_e32 v9, 0x1f0, v0\n"
    "v_mfma_f32_32x32x16_bf16 v[64:79], a[56:59], v[128:131], v[64:79]\n"
    "ds_read_b128 a[44:47], v29 offset:7680\n"
    "buffer_load_dwordx4 v0, s[16:19], s90 offen offset:2048 sc1 lds\n"
    "s_waitcnt vmcnt(13)\n"
    "v_max3_u32 v31, v144, v145, v146\n"
    "v_max_u32_e32 v31, v31, v147\n"
    "v_lshrrev_b32_e32 v9, 2, v9\n"
    "v_add_u32_e32 v9, s52, v9\n"
    "ds_write_b32 v9, v7\n"
    "s_waitcnt lgkmcnt(3)\n"
    "v_mfma_f32_32x32x16_bf16 v[32:47], a[24:27], v[132:135], v[32:47]\n"
    "ds_read_b128 a[48:51], v28 offset:11264\n"
    "buffer_load_dwordx4 v[184:187], v0, s[16:19], s50 offen offset:2048 sc1\n"
    "v_mfma_f32_32x32x16_bf16 v[48:63], v[132:135], a[28:31], v[48:63]\n"
    "ds_read_b128 a[52:55], v30 offset:11264\n"
    "v_mov_b32_e32 v18, 0\n"
    "v_mov_b32_e32 v19, 0\n"
    "s_mul_i32 s53, s29, 0x680000\n"
    "s_lshl_b32 s54, s28, 6\n"
    "v_mfma_f32_32x32x16_bf16 v[64:79], a[32:35], v[132:135], v[64:79]\n"
    "ds_read_b128 a[56:59], v29 offset:8448\n"
    "s_waitcnt vmcnt(12)\n"
    "v_max3_u32 v31, v31, v148, v149\n"
    "v_max3_u32 v31, v31, v150, v151\n"
    "s_add_u32 s54, s54, s32\n"
    "s_mul_i32 s54, s54, 6656\n"
    "s_add_u32 s53, s53, s54\n"
    "s_waitcnt lgkmcnt(3)\n"
    "v_mfma_f32_32x32x16_bf16 v[32:47], a[36:39], v[136:139], v[32:47]\n"
    "ds_read_b128 a[24:27], v28 offset:12288\n"
    "buffer_load_dwordx4 v[188:191], v0, s[16:19], s50 offen offset:3072 sc1\n"
    "v_mfma_f32_32x32x16_bf16 v[48:63], v[136:139], a[40:43], v[48:63]\n"
    "ds_read_b128 a[28:31], v30 offset:12288\n"
    "s_add_u32 s53, s53, 0x12ad800\n"
    "v_and_b32_e32 v20, 0x2f0, v0\n"
    "v_and_b32_e32 v9, 0x100, v0\n"
    "v_lshl_add_u32 v20, v9, 3, v20\n"
    "v_mfma_f32_32x32x16_bf16 v[64:79], a[44:47], v[136:139], v[64:79]\n"
    "ds_read_b128 a[32:35], v29 offset:9216\n"
    "buffer_load_dwordx4 v0, s[16:19], s90 offen offset:3072 sc1 lds\n"
    "s_waitcnt vmcnt(13)\n"
    "v_max3_u32 v31, v31, v152, v153\n"
    "v_max3_u32 v31, v31, v154, v155\n"
    "v_add_u32_e32 v20, 2560, v20\n"
    "v_lshrrev_b32_e32 v21, 1, v0\n"
    "v_lshrrev_b32_e32 v9, 8, v0\n"
    "s_waitcnt lgkmcnt(3)\n"
    "v_mfma_f32_32x32x16_bf16 v[32:47], a[48:51], v[140:143], v[32:47]\n"
    "ds_read_b128 a[36:39], v28 offset:13312\n"
    "s_add_u32 s50, s50, 0x1000\n"
    "buffer_load_dwordx4 v[192:195], v0, s[16:19], s50 offen sc1\n"
    "v_mfma_f32_32x32x16_bf16 v[48:63], v[140:143], a[52:55], v[48:63]\n"
    "ds_read_b128 a[40:43], v30 offset:13312\n"
    "v_lshlrev_b32_e32 v22, 7, v9\n"
    "v_bfe_u32 v9, v0, 6, 1\n"
    "v_lshl_add_u32 v22, v9, 6, v22\n"
    "v_bfe_u32 v9, v0, 4, 2\n"
    "v_mfma_f32_32x32x16_bf16 v[64:79], a[56:59], v[140:143], v[64:79]\n"
    "ds_read_b128 a[44:47], v29 offset:9984\n"
    "s_waitcnt vmcnt(13)\n"
    "v_max3_u32 v31, v31, v156, v157\n"
    "v_max3_u32 v31, v31, v158, v159\n"
    "v_cmp_eq_u32_e32 vcc, -1, v31\n"
    "s_cbranch_vccnz .Lmk_again_23\n"
    ".Lmk_there_24:\n"
    "v_lshl_add_u32 v22, v9, 1, v22\n"
    "v_bfe_u32 v9, v0, 7, 1\n"
    "v_lshl_add_u32 v22, v9, 3, v22\n"
    "s_waitcnt lgkmcnt(3)\n"
    "v_mfma_f32_32x32x16_bf16 v[32:47], a[24:27], v[144:147], v[32:47]\n"
    "ds_read_b128 a[48:51], v28 offset:14336\n"
    "buffer_load_dwordx4 v[196:199], v0, s[16:19], s50 offen offset:1024 sc1\n"
    "v_mfma_f32_32x32x16_bf16 v[48:63], v[144:147], a[28:31], v[48:63]\n"
    "ds_read_b128 a[52:55], v30 offset:14336\n"
    "v_add_u32_e32 v22, s52, v22\n"
    "v_mov_b32_e32 v23, s52\n"
    "v_lshrrev_b32_e32 v24, 9, v0\n"
    "v_lshl_add_u32 v24, v24, 4, v23\n"
    "v_mfma_f32_32x32x16_bf16 v[64:79], a[32:35], v[144:147], v[64:79]\n"
    "ds_read_b128 a[56:59], v29 offset:10752\n"
    "s_add_u32 m0, m0, 0x1000\n"
    "s_add_u32 s90, s90, 0x1000\n"
    "buffer_load_dwordx4 v0, s[16:19], s90 offen sc1 lds\n"
    "s_waitcnt vmcnt(14)\n"
    "v_max3_u32 v31, v160, v161, v162\n"
    "v_max_u32_e32 v31, v31, v163\n"
    "v_bfe_u32 v9, v0, 7, 2\n"
    "v_lshlrev_b32_e32 v25, 10, v9\n"
    "v_bfe_u32 v9, v0, 6, 1\n"
    "s_waitcnt lgkmcnt(3)\n"
    "v_mfma_f32_32x32x16_bf16 v[32:47], a[36:39], v[148:151], v[32:47]\n"
    "ds_read_b128 a[24:27], v28 offset:15360\n"
    "buffer_load_dwordx4 v[200:203], v0, s[16:19], s50 offen offset:2048 sc1\n"
    "v_mfma_f32_32x32x16_bf16 v[48:63], v[148:151], a[40:43], v[48:63]\n"
    "ds_read_b128 a[28:31], v30 offset:15360\n"
    "v_lshl_add_u32 v25, v9, 9, v25\n"
    "v_and_b32_e32 v9, 0x30, v0\n"
    "v_add_u32_e32 v25, v9, v25\n"
    "v_add_u32_e32 v25, 2816, v25\n"
    "v_mfma_f32_32x32x16_bf16 v[64:79], a[44:47], v[148:151], v[64:79]\n"
    "ds_read_b128 a[32:35], v29 offset:11520\n"
    "s_waitcnt vmcnt(14)\n"
    "v_max3_u32 v31, v31, v164, v165\n"
    "v_max3_u32 v31, v31, v166, v167\n"
    "s_waitcnt lgkmcnt(3)\n"
    "v_mfma_f32_32x32x16_bf16 v[32:47], a[48:51], v[152:155], v[32:47]\n"
    "ds_read_b128 a[36:39], v28 offset:16384\n"
    "buffer_load_dwordx4 v[204:207], v0, s[16:19], s50 offen offset:3072 sc1\n"
    "v_mfma_f32_32x32x16_bf16 v[48:63], v[152:155], a[52:55], v[48:63]\n"
    "ds_read_b128 a[40:43], v30 offset:16384\n"
    "v_mfma_f32_32x32x16_bf16 v[64:79], a[56:59], v[152:155], v[64:79]\n"
    "ds_read_b128 a[44:47], v29 offset:12288\n"
    "buffer_load_dwordx4 v0, s[16:19], s90 offen offset:1024 sc1 lds\n"
    "s_waitcnt vmcnt(14)\n"
    "v_max3_u32 v31, v31, v168, v169\n"
    "v_max3_u32 v31, v31, v170, v171\n"
    "s_waitcnt lgkmcnt(3)\n"
    "v_mfma_f32_32x32x16_bf16 v[32:47], a[24:27], v[156:159], v[32:47]\n"
    "ds_read_b128 a[48:51], v28 offset:17408\n"
    "s_add_u32 s50, s50, 0x1000\n"
    "buffer_load_dwordx4 v[208:211], v0, s[16:19], s50 offen sc1\n"
    "v_mfma_f32_32x32x16_bf16 v[48:63], v[156:159], a[28:31], v[48:63]\n"
    "ds_read_b128 a[52:55], v30 offset:17408\n"
    "v_mfma_f32_32x32x16_bf16 v[64:79], a[32:35], v[156:159], v[64:79]\n"
    "ds_read_b128 a[56:59], v29 offset:13056\n"
    "s_waitcnt vmcnt(14)\n"
    "v_max3_u32 v31, v31, v172, v173\n"
    "v_max3_u32 v31, v31, v174, v175\n"
    "v_cmp_eq_u32_e32 vcc, -1, v31\n"
    "s_cbranch_vccnz .Lmk_again_25\n"
    ".Lmk_there_26:\n"
    "s_waitcnt lgkmcnt(3)\n"
    "v_mfma_f32_32x32x16_bf16 v[32:47], a[36:39], v[160:163], v[32:47]\n"
    "ds_read_b128 a[24:27], v28 offset:18432\n"
    "buffer_load_dwordx4 v[212:215], v0, s[16:19], s50 offen offset:1024 sc1\n"
    "v_mfma_f32_32x32x16_bf16 v[48:63], v[160:163], a[40:43], v[48:63]\n"
    "ds_read_b128 a[28:31], v30 offset:18432\n"
    "v_mfma_f32_32x32x16_bf16 v[64:79], a[44:47], v[160:163], v[64:79]\n"
    "ds_read_b128 a[32:35], v29 offset:13824\n"
    "buffer_load_dwordx4 v0, s[16:19], s90 offen offset:2048 sc1 lds\n"
    "s_waitcnt vmcnt(14)\n"
    "v_max3_u32 v31, v176, v177, v178\n"
    "v_max_u32_e32 v31, v31, v179\n"
    "s_waitcnt lgkmcnt(3)\n"
    "v_mfma_f32_32x32x16_bf16 v[32:47], a[48:51], v[164:167], v[32:47]\n"
    "ds_read_b128 a[36:39], v28 offset:19456\n"
    "buffer_load_dwordx4 v[216:219], v0, s[16:19], s50 offen offset:2048 sc1\n"
    "v_mfma_f32_32x32x16_bf16 v[48:63], v[164:167], a[52:55], v[48:63]\n"
    "ds_read_b128 a[40:43], v30 offset:19456\n"
    "v_mfma_f32_32x32x16_bf16 v[64:79], a[56:59], v[164:167], v[64:79]\n"
    "ds_read_b128 a[44:47], v29 offset:14592\n"
    "s_waitcnt vmcnt(14)\n"
    "v_max3_u32 v31, v31, v180, v181\n"
    "v_max3_u32 v31, v31, v182, v183\n"
    "s_waitcnt lgkmcnt(3)\n"
    "v_mfma_f32_32x32x16_bf16 v[32:47], a[24:27], v[168:171], v[32:47]\n"
    "ds_read_b128 a[48:51], v28 offset:20480\n"
    "buffer_load_dwordx4 v[220:223], v0, s[16:19], s50 offen offset:3072 sc1\n"
    "v_mfma_f32_32x32x16_bf16 v[48:63], v[168:171], a[28:31], v[48:63]\n"
    "ds_read_b128 a[52:55], v30 offset:20480\n"
    "v_mfma_f32_32x32x16_bf16 v[64:79], a[32:35], v[168:171], v[64:79]\n"
    "ds_read_b128 a[56:59], v29 offset:15360\n"
    "buffer_load_dwordx4 v0, s[16:19], s90 offen offset:3072 sc1 lds\n"
    "s_waitcnt vmcnt(14)\n"
    "v_max3_u32 v31, v31, v184, v185\n"
    "v_max3_u32 v31, v31, v186, v187\n"
    "s_waitcnt lgkmcnt(3)\n"
    "v_mfma_f32_32x32x16_bf16 v[32:47], a[36:39], v[172:175], v[32:47]\n"
    "ds_read_b128 a[24:27], v28 offset:21504\n"
    "s_add_u32 s50, s50, 0x1000\n"
    "buffer_load_dwordx4 v[224:227], v0, s[16:19], s50 offen sc1\n"
    "v_mfma_f32_32x32x16_bf16 v[48:63], v[172:175], a[40:43], v[48:63]\n"
    "ds_read_b128 a[28:31], v30 offset:21504\n"
    "v_mfma_f32_32x32x16_bf16 v[64:79], a[44:47], v[172:175], v[64:79]\n"
    "ds_read_b128 a[32:35], v29 offset:16128\n"
    "s_waitcnt vmcnt(14)\n"
    "v_max3_u32 v31, v31, v188, v189\n"
    "v_max3_u32 v31, v31, v190, v191\n"
    "v_cmp_eq_u32_e32 vcc, -1, v31\n"
    "s_cbranch_vccnz .Lmk_again_27\n"
    ".Lmk_there_28:\n"
    "s_waitcnt lgkmcnt(3)\n"
    "v_mfma_f32_32x32x16_bf16 v[32:47], a[48:51], v[176:179], v[32:47]\n"
    "ds_read_b128 a[36:39], v28 offset:22528\n"
    "buffer_load_dwordx4 v[228:231], v0, s[16:19], s50 offen offset:1024 sc1\n"
    "v_mfma_f32_32x32x16_bf16 v[48:63], v[176:179], a[52:55], v[48:63]\n"
    "ds_read_b128 a[40:43], v30 offset:22528\n"
    "v_mfma_f32_32x32x16_bf16 v[64:79], a[56:59], v[176:179], v[64:79]\n"
    "ds_read_b128 a[44:47], v29 offset:16896\n"
    "s_add_u32 m0, m0, 0x1000\n"
    "s_add_u32 s90, s90, 0x1000\n"
    "buffer_load_dwordx4 v0, s[16:19], s90 offen sc1 lds\n"
    "s_waitcnt vmcnt(14)\n"
    "v_max3_u32 v31, v192, v193, v194\n"
    "v_max_u32_e32 v31, v31, v195\n"
    "s_waitcnt lgkmcnt(3)\n"
    "v_mfma_f32_32x32x16_bf16 v[32:47], a[24:27], v[180:183], v[32:47]\n"
    "ds_read_b128 a[48:51], v28 offset:23552\n"
    "buffer_load_dwordx4 v[232:235], v0, s[16:19], s50 offen offset:2048 sc1\n"
    "v_mfma_f32_32x32x16_bf16 v[48:63], v[180:183], a[28:31], v[48:63]\n"
    "ds_read_b128 a[52:55], v30 offset:23552\n"
    "v_mfma_f32_32x32x16_bf16 v[64:79], a[32:35], v[180:183], v[64:79]\n"
    "ds_read_b128 a[56:59], v29 offset:17664\n"
    "s_waitcnt vmcnt(14)\n"
    "v_max3_u32 v31, v31, v196, v197\n"
    "v_max3_u32 v31, v31, v198, v199\n"
    "s_waitcnt lgkmcnt(3)\n"
    "v_mfma_f32_32x32x16_bf16 v[32:47], a[36:39], v[184:187], v[32:47]\n"
    "ds_read_b128 a[24:27], v28 offset:24576\n"
    "buffer_load_dwordx4 v[236:239], v0, s[16:19], s50 offen offset:3072 sc1\n"
    "v_mfma_f32_32x32x16_bf16 v[48:63], v[184:187], a[40:43], v[48:63]\n"
    "ds_read_b128 a[28:31], v30 offset:24576\n"
    "v_mfma_f32_32x32x16_bf16 v[64:79], a[44:47], v[184:187], v[64:79]\n"
    "ds_read_b128 a[32:35], v29 offset:18432\n"
    "buffer_load_dwordx4 v0, s[16:19], s90 offen offset:1024 sc1 lds\n"
    "s_waitcnt vmcnt(14)\n"
    "v_max3_u32 v31, v31, v200, v201\n"
    "v_max3_u32 v31, v31, v202, v203\n"
    "s_waitcnt lgkmcnt(3)\n"
    "v_mfma_f32_32x32x16_bf16 v[32:47], a[48:51], v[188:191], v[32:47]\n"
    "ds_read_b128 a[36:39], v28 offset:25600\n"
    "s_add_u32 s50, s50, 0x1000\n"
    "buffer_load_dwordx4 v[240:243], v0, s[16:19], s50 offen sc1\n"
    "v_mfma_f32_32x32x16_bf16 v[48:63], v[188:191], a[52:55], v[48:63]\n"
    "ds_read_b128 a[40:43], v30 offset:25600\n"
    "v_mfma_f32_32x32x16_bf16 v[64:79], a[56:59], v[188:191], v[64:79]\n"
    "ds_read_b128 a[44:47], v29 offset:19200\n"
    "s_waitcnt vmcnt(14)\n"
    "v_max3_u32 v31, v31, v204, v205\n"
    "v_max3_u32 v31, v31, v206, v207\n"
    "v_cmp_eq_u32_e32 vcc, -1, v31\n"
    "s_cbranch_vccnz .Lmk_again_29\n"
    ".Lmk_there_30:\n"
    "s_waitcnt lgkmcnt(3)\n"
    "v_mfma_f32_32x32x16_bf16 v[32:47], a[24:27], v[192:195], v[32:47]\n"
    "ds_read_b128 a[48:51], v28 offset:26624\n"
    "buffer_load_dwordx4 v[244:247], v0, s[16:19], s50 offen offset:1024 sc1\n"
    "v_mfma_f32_32x32x16_bf16 v[48:63], v[192:195], a[28:31], v[48:63]\n"
    "ds_read_b128 a[52:55], v30 offset:26624\n"
    "v_mfma_f32_32x32x16_bf16 v[64:79], a[32:35], v[192:195], v[64:79]\n"
    "ds_read_b128 a[56:59], v29 offset:19968\n"
    "s_waitcnt vmcnt(13)\n"
    "v_max3_u32 v31, v208, v209, v210\n"
    "v_max_u32_e32 v31, v31, v211\n"
    "s_waitcnt lgkmcnt(3)\n"
    "v_mfma_f32_32x32x16_bf16 v[32:47], a[36:39], v[196:199], v[32:47]\n"
    "ds_read_b128 a[24:27], v28 offset:27648\n"
    "buffer_load_dwordx4 v[248:251], v0, s[16:19], s50 offen offset:2048 sc1\n"
    "v_mfma_f32_32x32x16_bf16 v[48:63], v[196:199], a[40:43], v[48:63]\n"
    "ds_read_b128 a[28:31], v30 offset:27648\n"
    "v_mfma_f32_32x32x16_bf16 v[64:79], a[44:47], v[196:199], v[64:79]\n"
    "ds_read_b128 a[32:35], v29 offset:20736\n"
    "s_waitcnt vmcnt(13)\n"
    "v_max3_u32 v31, v31, v212, v213\n"
    "v_max3_u32 v31, v31, v214, v215\n"
    "s_waitcnt lgkmcnt(3)\n"
    "v_mfma_f32_32x32x16_bf16 v[32:47], a[48:51], v[200:203], v[32:47]\n"
    "ds_read_b128 a[36:39], v28 offset:28672\n"
    "buffer_load_dwordx4 v[252:255], v0, s[16:19], s50 offen offset:3072 sc1\n"
    "v_mfma_f32_32x32x16_bf16 v[48:63], v[200:203], a[52:55], v[48:63]\n"
    "ds_read_b128 a[40:43], v30 offset:28672\n"
    "v_mfma_f32_32x32x16_bf16 v[64:79], a[56:59], v[200:203], v[64:79]\n"
    "ds_read_b128 a[44:47], v29 offset:21504\n"
    "s_waitcnt vmcnt(12)\n"
    "v_max3_u32 v31, v31, v216, v217\n"
    "v_max3_u32 v31, v31, v218, v219\n"
    "s_waitcnt lgkmcnt(3)\n"
    "v_mfma_f32_32x32x16_bf16 v[32:47], a[24:27], v[204:207], v[32:47]\n"
    "ds_read_b128 a[48:51], v28 offset:29696\n"
    "v_mfma_f32_32x32x16_bf16 v[48:63], v[204:207], a[28:31], v[48:63]\n"
    "ds_read_b128 a[52:55], v30 offset:29696\n"
    "v_mfma_f32_32x32x16_bf16 v[64:79], a[32:35], v[204:207], v[64:79]\n"
    "ds_read_b128 a[56:59], v29 offset:22272\n"
    "s_waitcnt vmcnt(11)\n"
    "v_max3_u32 v31, v31, v220, v221\n"
    "v_max3_u32 v31, v31, v222, v223\n"
    "v_cmp_eq_u32_e32 vcc, -1, v31\n"
    "s_cbranch_vccnz .Lmk_again_31\n"
    ".Lmk_there_32:\n"
    "s_waitcnt lgkmcnt(3)\n"
    "v_mfma_f32_32x32x16_bf16 v[32:47], a[36:39], v[208:211], v[32:47]\n"
    "ds_read_b128 a[24:27], v28 offset:30720\n"
    "v_mfma_f32_32x32x16_bf16 v[48:63], v[208:211], a[40:43], v[48:63]\n"
    "ds_read_b128 a[28:31], v30 offset:30720\n"
    "v_mfma_f32_32x32x16_bf16 v[64:79], a[44:47], v[208:211], v[64:79]\n"
    "ds_read_b128 a[32:35], v29 offset:23040\n"
    "buffer_store_dwordx4 a[60:63], v80, s[16:19], s62 offen sc1\n"
    "s_waitcnt vmcnt(10)\n"
    "v_max3_u32 v31, v224, v225, v226\n"
    "v_max_u32_e32 v31, v31, v227\n"
    "s_waitcnt lgkmcnt(3)\n"
    "v_mfma_f32_32x32x16_bf16 v[32:47], a[48:51], v[212:215], v[32:47]\n"
    "ds_read_b128 a[36:39], v28 offset:31744\n"
    "v_mfma_f32_32x32x16_bf16 v[48:63], v[212:215], a[52:55], v[48:63]\n"
    "ds_read_b128 a[40:43], v30 offset:31744\n"
    "v_mfma_f32_32x32x16_bf16 v[64:79], a[56:59], v[212:215], v[64:79]\n"
    "ds_read_b128 a[44:47], v29 offset:23808\n"
    "buffer_store_dwordx4 a[60:63], v80, s[16:19], s62 offen offset:256 sc1\n"
    "s_waitcnt vmcnt(10)\n"
    "v_max3_u32 v31, v31, v228, v229\n"
    "v_max3_u32 v31, v31, v230, v231\n"
    "s_waitcnt lgkmcnt(3)\n"
    "v_mfma_f32_32x32x16_bf16 v[32:47], a[24:27], v[216:219], v[32:47]\n"
    "ds_read_b128 a[48:51], v28 offset:32768\n"
    "v_mfma_f32_32x32x16_bf16 v[48:63], v[216:219], a[28:31], v[48:63]\n"
    "ds_read_b128 a[52:55], v30 offset:32768\n"
    "v_mfma_f32_32x32x16_bf16 v[64:79], a[32:35], v[216:219], v[64:79]\n"
    "ds_read_b128 a[56:59], v29 offset:24576\n"
    "s_waitcnt vmcnt(8)\n"
    "v_max3_u32 v31, v31, v232, v233\n"
    "v_max3_u32 v31, v31, v234, v235\n"
    "s_waitcnt lgkmcnt(3)\n"
    "v_mfma_f32_32x32x16_bf16 v[32:47], a[36:39], v[220:223], v[32:47]\n"
    "ds_read_b128 a[24:27], v28 offset:33792\n"
    "v_mfma_f32_32x32x16_bf16 v[48:63], v[220:223], a[40:43], v[48:63]\n"
    "ds_read_b128 a[28:31], v30 offset:33792\n"
    "v_mfma_f32_32x32x16_bf16 v[64:79], a[44:47], v[220:223], v[64:79]\n"
    "ds_read_b128 a[32:35], v29 offset:25344\n"
    "buffer_store_dwordx4 a[60:63], v80, s[16:19], s62 offen offset:1024 sc1\n"
    "s_waitcnt vmcnt(8)\n"
    "v_max3_u32 v31, v31, v236, v237\n"
    "v_max3_u32 v31, v31, v238, v239\n"
    "v_cmp_eq_u32_e32 vcc, -1, v31\n"
    "s_cbranch_vccnz .Lmk_again_33\n"
    ".Lmk_there_34:\n"
    "s_waitcnt lgkmcnt(3)\n"
    "v_mfma_f32_32x32x16_bf16 v[32:47], a[48:51], v[224:227], v[32:47]\n"
    "ds_read_b128 a[36:39], v28 offset:34816\n"
    "v_mfma_f32_32x32x16_bf16 v[48:63], v[224:227], a[52:55], v[48:63]\n"
    "ds_read_b128 a[40:43], v30 offset:34816\n"
    "v_mfma_f32_32x32x16_bf16 v[64:79], a[56:59], v[224:227], v[64:79]\n"
    "ds_read_b128 a[44:47], v29 offset:26112\n"
    "buffer_store_dwordx4 a[60:63], v80, s[16:19], s62 offen offset:1280 sc1\n"
    "s_waitcnt vmcnt(7)\n"
    "v_max3_u32 v31, v240, v241, v242\n"
    "v_max_u32_e32 v31, v31, v243\n"
    "s_waitcnt lgkmcnt(3)\n"
    "v_mfma_f32_32x32x16_bf16 v[32:47], a[24:27], v[228:231], v[32:47]\n"
    "ds_read_b128 a[48:51], v28 offset:35840\n"
    "v_mfma_f32_32x32x16_bf16 v[48:63], v[228:231], a[28:31], v[48:63]\n"
    "ds_read_b128 a[52:55], v30 offset:35840\n"
    "v_mfma_f32_32x32x16_bf16 v[64:79], a[32:35], v[228:231], v[64:79]\n"
    "ds_read_b128 a[56:59], v29 offset:26880\n"
    "s_waitcnt vmcnt(6)\n"
    "v_max3_u32 v31, v31, v244, v245\n"
    "v_max3_u32 v31, v31, v246, v247\n"
    "s_waitcnt lgkmcnt(3)\n"
    "v_mfma_f32_32x32x16_bf16 v[32:47], a[36:39], v[232:235], v[32:47]\n"
    "ds_read_b128 a[24:27], v28 offset:36864\n"
    "v_mfma_f32_32x32x16_bf16 v[48:63], v[232:235], a[40:43], v[48:63]\n"
    "ds_read_b128 a[28:31], v30 offset:36864\n"
    "v_mfma_f32_32x32x16_bf16 v[64:79], a[44:47], v[232:235], v[64:79]\n"
    "ds_read_b128 a[32:35], v29 offset:27648\n"
    "buffer_store_dwordx4 a[60:63], v80, s[16:19], s62 offen offset:2048 sc1\n"
    "s_waitcnt vmcnt(6)\n"
    "v_max3_u32 v31, v31, v248, v249\n"
    "v_max3_u32 v31, v31, v250, v251\n"
    "s_waitcnt lgkmcnt(3)\n"
    "v_mfma_f32_32x32x16_bf16 v[32:47], a[48:51], v[236:239], v[32:47]\n"
    "ds_read_b128 a[36:39], v28 offset:37888\n"
    "v_mfma_f32_32x32x16_bf16 v[48:63], v[236:239], a[52:55], v[48:63]\n"
    "ds_read_b128 a[40:43], v30 offset:37888\n"
    "v_mfma_f32_32x32x16_bf16 v[64:79], a[56:59], v[236:239], v[64:79]\n"
    "ds_read_b128 a[44:47], v29 offset:28416\n"
    "buffer_store_dwordx4 a[60:63], v80, s[16:19], s62 offen offset:2304 sc1\n"
    "s_waitcnt vmcnt(6)\n"
    "v_max3_u32 v31, v31, v252, v253\n"
    "v_max3_u32 v31, v31, v254, v255\n"
    "v_cmp_eq_u32_e32 vcc, -1, v31\n"
    "s_cbranch_vccnz .Lmk_again_35\n"
    ".Lmk_there_36:\n"
    "s_waitcnt lgkmcnt(3)\n"
    "v_mfma_f32_32x32x16_bf16 v[32:47], a[24:27], v[240:243], v[32:47]\n"
    "ds_read_b128 a[48:51], v28 offset:38912\n"
    "v_mfma_f32_32x32x16_bf16 v[48:63], v[240:243], a[28:31], v[48:63]\n"
    "ds_read_b128 a[52:55], v30 offset:38912\n"
    "v_mfma_f32_32x32x16_bf16 v[64:79], a[32:35], v[240:243], v[64:79]\n"
    "ds_read_b128 a[56:59], v29 offset:29184\n"
    "s_waitcnt lgkmcnt(3)\n"
    "v_mfma_f32_32x32x16_bf16 v[32:47], a[36:39], v[244:247], v[32:47]\n"
    "ds_read_b128 a[24:27], v28 offset:39936\n"
    "v_mfma_f32_32x32x16_bf16 v[48:63], v[244:247], a[40:43], v[48:63]\n"
    "ds_read_b128 a[28:31], v30 offset:39936\n"
    "v_mfma_f32_32x32x16_bf16 v[64:79], a[44:47], v[244:247], v[64:79]\n"
    "ds_read_b128 a[32:35], v29 offset:29952\n"
    "buffer_store_dwordx4 a[60:63], v81, s[16:19], s62 offen offset:3072 sc1\n"
    "s_waitcnt lgkmcnt(3)\n"
    "v_mfma_f32_32x32x16_bf16 v[32:47], a[48:51], v[248:251], v[32:47]\n"
    "v_mfma_f32_32x32x16_bf16 v[48:63], v[248:251], a[52:55], v[48:63]\n"
    "v_mfma_f32_32x32x16_bf16 v[64:79], a[56:59], v[248:251], v[64:79]\n"
    "buffer_store_dwordx4 a[60:63], v81, s[16:19], s62 offen offset:3328 sc1\n"
    "s_waitcnt lgkmcnt(0)\n"
    "v_mfma_f32_32x32x16_bf16 v[32:47], a[24:27], v[252:255], v[32:47]\n"
    "v_mfma_f32_32x32x16_bf16 v[48:63], v[252:255], a[28:31], v[48:63]\n"
    "v_mfma_f32_32x32x16_bf16 v[64:79], a[32:35], v[252:255], v[64:79]\n"
    "v_accvgpr_read_b32 v16, a10\n"
    "v_accvgpr_read_b32 v17, a10\n"
    "s_nop 1\n"
    "v_mfma_f32_32x32x16_bf16 v[32:47], v[16:19], v[12:15], v[32:47]\n"
    "s_barrier\n"
    "ds_read_b128 a[24:27], v0\n"
    "ds_read_b128 a[28:31], v0 offset:1024\n"
    "ds_read_b128 a[32:35], v0 offset:2048\n"
    "ds_read_b128 a[36:39], v0 offset:3072\n"
    "ds_read_b128 a[40:43], v0 offset:4096\n"
    "v_accvgpr_read_b32 v16, a12\n"
    "v_accvgpr_read_b32 v17, a12\n"
    "s_nop 1\n"
    "v_mfma_f32_32x32x16_bf16 v[64:79], v[16:19], v[12:15], v[64:79]\n"
    "v_accvgpr_read_b32 v16, a11\n"
    "v_accvgpr_read_b32 v17, a11\n"
    "s_nop 1\n"
    "v_mfma_f32_32x32x16_bf16 v[48:63], v[12:15], v[16:19], v[48:63]\n"
    "v_lshrrev_b32_e32 v26, 1, v0\n"
    "s_lshl_b32 s57, s26, 9\n"
    "s_add_u32 s57, s57, 4096\n"
    "buffer_load_dwordx2 v[30:31], v26, s[16:19], s57 offen sc1\n"
    "v_mov_b32_e32 v5, v82\n"
    "v_mov_b32_e32 v6, v83\n"
    "v_add_u32_e32 v28, s67, v82\n"
    "v_add_u32_e32 v29, s67, v83\n"
    "s_waitcnt lgkmcnt(4)\n"
    "v_mfma_f32_32x32x16_bf16 v[80:95], a[24:27], v[96:99], 0\n"
    "ds_read_b128 a[24:27], v0 offset:5120\n"
    "s_waitcnt lgkmcnt(4)\n"
    "v_mfma_f32_32x32x16_bf16 v[80:95], a[28:31], v[100:103], v[80:95]\n"
    "ds_read_b128 a[28:31], v0 offset:6144\n"
    "buffer_store_dwordx2 a[60:61], v6, s[16:19], s64 offen sc1\n"
    "v_mul_f32_e32 v32, v7, v32\n"
    "v_mul_f32_e32 v33, v7, v33\n"
    "v_mul_f32_e32 v34, v7, v34\n"
    "v_mul_f32_e32 v35, v7, v35\n"
    "v_mul_f32_e32 v36, v7, v36\n"
    "s_waitcnt lgkmcnt(4)\n"
    "v_mfma_f32_32x32x16_bf16 v[80:95], a[32:35], v[104:107], v[80:95]\n"
    "ds_read_b128 a[32:35], v0 offset:7168\n"
    "v_mul_f32_e32 v37, v7, v37\n"
    "v_mul_f32_e32 v38, v7, v38\n"
    "v_mul_f32_e32 v39, v7, v39\n"
    "v_mul_f32_e32 v40, v7, v40\n"
    "v_mul_f32_e32 v41, v7, v41\n"
    "s_waitcnt lgkmcnt(4)\n"
    "v_mfma_f32_32x32x16_bf16 v[80:95], a[36:39], v[108:111], v[80:95]\n"
    "ds_read_b128 a[36:39], v0 offset:8192\n"
    "v_mul_f32_e32 v42, v7, v42\n"
    "v_mul_f32_e32 v43, v7, v43\n"
    "v_mul_f32_e32 v44, v7, v44\n"
    "v_mul_f32_e32 v45, v7, v45\n"
    "v_mul_f32_e32 v46, v7, v46\n"
    "s_waitcnt lgkmcnt(4)\n"
    "v_mfma_f32_32x32x16_bf16 v[80:95], a[40:43], v[112:115], v[80:95]\n"
    "ds_read_b128 a[40:43], v0 offset:9216\n"
    "buffer_store_dwordx2 a[60:61], v5, s[16:19], s63 offen sc1\n"
    "v_mul_f32_e32 v47, v7, v47\n"
    "v_cvt_pk_bf16_f32 v32, v32, v33\n"
    "v_cvt_pk_bf16_f32 v33, v34, v35\n"
    "v_cvt_pk_bf16_f32 v34, v36, v37\n"
    "v_cvt_pk_bf16_f32 v35, v38, v39\n"
    "s_waitcnt lgkmcnt(4)\n"
    "v_mfma_f32_32x32x16_bf16 v[80:95], a[24:27], v[116:119], v[80:95]\n"
    "ds_read_b128 a[24:27], v0 offset:10240\n"
    "buffer_store_dwordx4 v[32:35], v0, s[16:19], s53 offen sc1\n"
    "v_cvt_pk_bf16_f32 v36, v40, v41\n"
    "v_cvt_pk_bf16_f32 v37, v42, v43\n"
    "v_cvt_pk_bf16_f32 v38, v44, v45\n"
    "s_waitcnt lgkmcnt(4)\n"
    "v_mfma_f32_32x32x16_bf16 v[80:95], a[28:31], v[120:123], v[80:95]\n"
    "ds_read_b128 a[28:31], v0 offset:11264\n"
    "v_cvt_pk_bf16_f32 v39, v46, v47\n"
    "buffer_store_dwordx4 v[36:39], v0, s[16:19], s53 offen offset:1024 sc1\n"
    "v_mul_f32_e32 v72, v7, v72\n"
    "v_mul_f32_e32 v73, v7, v73\n"
    "s_waitcnt lgkmcnt(4)\n"
    "v_mfma_f32_32x32x16_bf16 v[80:95], a[32:35], v[124:127], v[80:95]\n"
    "ds_read_b128 a[32:35], v0 offset:12288\n"
    "buffer_store_dwordx2 a[60:61], v5, s[16:19], s63 offen offset:1024 sc1\n"
    "v_mul_f32_e32 v74, v7, v74\n"
    "v_mul_f32_e32 v75, v7, v75\n"
    "v_mul_f32_e32 v64, v7, v64\n"
    "v_mul_f32_e32 v65, v7, v65\n"
    "v_mul_f32_e32 v66, v7, v66\n"
    "s_waitcnt lgkmcnt(4)\n"
    "v_mfma_f32_32x32x16_bf16 v[80:95], a[36:39], v[128:131], v[80:95]\n"
    "ds_read_b128 a[36:39], v0 offset:13312\n"
    "v_mul_f32_e32 v67, v7, v67\n"
    "v_mul_f32_e32 v68, v8, v68\n"
    "v_mul_f32_e32 v69, v8, v69\n"
    "v_mul_f32_e32 v70, v8, v70\n"
    "v_mul_f32_e32 v71, v8, v71\n"
    "s_waitcnt lgkmcnt(4)\n"
    "v_mfma_f32_32x32x16_bf16 v[80:95], a[40:43], v[132:135], v[80:95]\n"
    "ds_read_b128 a[40:43], v0 offset:14336\n"
    "v_cvt_pk_bf16_f32 v64, v64, v65\n"
    "v_cvt_pk_bf16_f32 v65, v66, v67\n"
    "v_cvt_pk_bf16_f32 v66, v68, v69\n"
    "v_cvt_pk_bf16_f32 v67, v70, v71\n"
    "v_cvt_pk_bf16_f32 v68, v72, v73\n"
    "s_waitcnt lgkmcnt(4)\n"
    "v_mfma_f32_32x32x16_bf16 v[80:95], a[24:27], v[136:139], v[80:95]\n"
    "ds_read_b128 a[24:27], v0 offset:15360\n"
    "buffer_store_dwordx2 a[60:61], v5, s[16:19], s63 offen offset:2048 sc1\n"
    "v_cvt_pk_bf16_f32 v69, v74, v75\n"
    "ds_write_b16 v22, v68 offset:128\n"
    "ds_write_b16_d16_hi v22, v68 offset:144\n"
    "ds_write_b16 v22, v69 offset:160\n"
    "ds_write_b16_d16_hi v22, v69 offset:176\n"
    "s_waitcnt lgkmcnt(8)\n"
    "v_mfma_f32_32x32x16_bf16 v[80:95], a[28:31], v[140:143], v[80:95]\n"
    "ds_read_b128 a[28:31], v0 offset:16384\n"
    "buffer_store_dwordx2 v[64:65], v21, s[16:19], s53 offen offset:2048 sc1\n"
    "ds_read_b128 v[72:75], v24\n"
    "ds_read_b128 v[76:79], v24 offset:32\n"
    "v_add_u32_e32 v9, v23, v0\n"
    "s_waitcnt lgkmcnt(10)\n"
    "v_mfma_f32_32x32x16_bf16 v[80:95], a[32:35], v[144:147], v[80:95]\n"
    "ds_read_b128 a[32:35], v0 offset:17408\n"
    "ds_read_b128 v[40:43], v9 offset:128\n"
    "v_mov_b32_e32 v9, 0x7c000000\n"
    "v_cndmask_b32_e64 v27, v25, v9, s[48:49]\n"
    "s_waitcnt lgkmcnt(3)\n"
    "v_mul_f32_e32 v48, v72, v48\n"
    "v_mul_f32_e32 v49, v73, v49\n"
    "v_mfma_f32_32x32x16_bf16 v[80:95], a[36:39], v[148:151], v[80:95]\n"
    "ds_read_b128 a[36:39], v0 offset:18432\n"
    "buffer_store_dwordx2 a[60:61], v5, s[16:19], s63 offen offset:3072 sc1\n"
    "v_mul_f32_e32 v50, v74, v50\n"
    "v_mul_f32_e32 v51, v75, v51\n"
    "s_waitcnt lgkmcnt(3)\n"
    "v_mul_f32_e32 v52, v76, v52\n"
    "v_mul_f32_e32 v53, v77, v53\n"
    "v_mul_f32_e32 v54, v78, v54\n"
    "v_mfma_f32_32x32x16_bf16 v[80:95], a[40:43], v[152:155], v[80:95]\n"
    "ds_read_b128 a[40:43], v0 offset:19456\n"
    "v_mul_f32_e32 v55, v79, v55\n"
    "ds_read_b128 v[72:75], v24 offset:64\n"
    "ds_read_b128 v[76:79], v24 offset:96\n"
    "v_cvt_pk_bf16_f32 v48, v48, v49\n"
    "v_cvt_pk_bf16_f32 v49, v50, v51\n"
    "v_mfma_f32_32x32x16_bf16 v[80:95], a[24:27], v[156:159], v[80:95]\n"
    "ds_read_b128 a[24:27], v0 offset:20480\n"
    "v_cvt_pk_bf16_f32 v50, v52, v53\n"
    "v_cvt_pk_bf16_f32 v51, v54, v55\n"
    "buffer_store_dwordx4 v[48:51], v20, s[16:19], s53 offen sc1\n"
    "v_mfma_f32_32x32x16_bf16 v[80:95], a[28:31], v[160:163], v[80:95]\n"
    "ds_read_b128 a[28:31], v0 offset:21504\n"
    "s_add_u32 s63, s63, 0x1000\n"
    "buffer_store_dwordx2 a[60:61], v5, s[16:19], s63 offen sc1\n"
    "s_waitcnt lgkmcnt(6)\n"
    "buffer_store_dwordx4 v[40:43], v27, s[16:19], s53 offen sc1\n"
    "s_waitcnt lgkmcnt(3)\n"
    "v_mul_f32_e32 v56, v72, v56\n"
    "v_mul_f32_e32 v57, v73, v57\n"
    "v_mul_f32_e32 v58, v74, v58\n"
    "v_mfma_f32_32x32x16_bf16 v[80:95], a[32:35], v[164:167], v[80:95]\n"
    "ds_read_b128 a[32:35], v0 offset:22528\n"
    "v_mul_f32_e32 v59, v75, v59\n"
    "s_waitcnt lgkmcnt(3)\n"
    "v_mul_f32_e32 v60, v76, v60\n"
    "v_mul_f32_e32 v61, v77, v61\n"
    "v_mul_f32_e32 v62, v78, v62\n"
    "v_mul_f32_e32 v63, v79, v63\n"
    "v_mfma_f32_32x32x16_bf16 v[80:95], a[36:39], v[168:171], v[80:95]\n"
    "ds_read_b128 a[36:39], v0 offset:23552\n"
    "v_cvt_pk_bf16_f32 v52, v56, v57\n"
    "v_cvt_pk_bf16_f32 v53, v58, v59\n"
    "v_cvt_pk_bf16_f32 v54, v60, v61\n"
    "v_cvt_pk_bf16_f32 v55, v62, v63\n"
    "v_mfma_f32_32x32x16_bf16 v[80:95], a[40:43], v[172:175], v[80:95]\n"
    "ds_read_b128 a[40:43], v0 offset:24576\n"
    "buffer_store_dwordx2 a[60:61], v29, s[16:19], s66 offen sc1\n"
    "buffer_store_dwordx4 v[52:55], v20, s[16:19], s53 offen offset:1024 sc1\n"
    "s_mul_i32 s56, s26, 6656\n"
    "s_add_u32 s56, s56, 0x1f000\n"
    "v_add_u32_e32 v9, s56, v0\n"
    "s_waitcnt lgkmcnt(4)\n"
    "v_mfma_f32_32x32x16_bf16 v[80:95], a[24:27], v[176:179], v[80:95]\n"
    "ds_read_b128 a[24:27], v0 offset:25600\n"
    "ds_write_b128 v9, v[32:35]\n"
    "ds_write_b128 v9, v[36:39] offset:1024\n"
    "v_add_u32_e32 v9, s56, v21\n"
    "ds_write_b64 v9, v[64:65] offset:2048\n"
    "v_add_u32_e32 v9, s56, v20\n"
    "s_waitcnt lgkmcnt(7)\n"
    "v_mfma_f32_32x32x16_bf16 v[80:95], a[28:31], v[180:183], v[80:95]\n"
    "ds_read_b128 a[28:31], v0 offset:26624\n"
    "ds_write_b128 v9, v[48:51]\n"
    "ds_write_b128 v9, v[52:55] offset:1024\n"
    "v_add_u32_e32 v9, s56, v25\n"
    "s_mov_b32 exec_hi, 0\n"
    "ds_write_b128 v9, v[40:43]\n"
    "s_mov_b32 exec_hi, -1\n"
    "v_bfe_u32 v10, v0, 6, 2\n"
    "s_waitcnt lgkmcnt(10)\n"
    "v_mfma_f32_32x32x16_bf16 v[80:95], a[32:35], v[184:187], v[80:95]\n"
    "ds_read_b128 a[32:35], v0 offset:27648\n"
    "buffer_store_dwordx2 a[60:61], v28, s[16:19], s65 offen sc1\n"
    "v_lshlrev_b32_e32 v10, 10, v10\n"
    "v_bfe_u32 v11, v0, 4, 1\n"
    "v_lshl_add_u32 v10, v11, 9, v10\n"
    "v_bfe_u32 v11, v0, 5, 1\n"
    "v_lshl_add_u32 v10, v11, 6, v10\n"
    "s_waitcnt lgkmcnt(10)\n"
    "v_mfma_f32_32x32x16_bf16 v[80:95], a[36:39], v[188:191], v[80:95]\n"
    "ds_read_b128 a[36:39], v0 offset:28672\n"
    "v_add_u32_e32 v10, s56, v10\n"
    "v_mov_b32_e32 v44, 0x3f803f80\n"
    "v_mov_b32_e32 v45, 0x3f803f80\n"
    "v_mov_b32_e32 v46, 0x3f803f80\n"
    "v_mov_b32_e32 v47, 0x3f803f80\n"
    "s_mov_b64 exec, 0xffff\n"
    "ds_write_b128 v10, v[44:47] offset:2880\n"
    "s_mov_b64 exec, -1\n"
    "s_waitcnt lgkmcnt(11)\n"
    "v_mfma_f32_32x32x16_bf16 v[80:95], a[40:43], v[192:195], v[80:95]\n"
    "ds_read_b128 a[40:43], v0 offset:29696\n"
    "s_waitcnt lgkmcnt(11)\n"
    "v_mfma_f32_32x32x16_bf16 v[80:95], a[24:27], v[196:199], v[80:95]\n"
    "ds_read_b128 a[24:27], v0 offset:30720\n"
    "buffer_store_dwordx2 a[60:61], v28, s[16:19], s65 offen offset:1024 sc1\n"
    "s_waitcnt lgkmcnt(8)\n"
    "v_mfma_f32_32x32x16_bf16 v[80:95], a[28:31], v[200:203], v[80:95]\n"
    "ds_read_b128 a[28:31], v0 offset:31744\n"
    "s_waitcnt lgkmcnt(5)\n"
    "v_mfma_f32_32x32x16_bf16 v[80:95], a[32:35], v[204:207], v[80:95]\n"
    "ds_read_b128 a[32:35], v0 offset:32768\n"
    "s_waitcnt lgkmcnt(5)\n"
    "v_mfma_f32_32x32x16_bf16 v[80:95], a[36:39], v[208:211], v[80:95]\n"
    "ds_read_b128 a[36:39], v0 offset:33792\n"
    "buffer_store_dwordx2 a[60:61], v28, s[16:19], s65 offen offset:2048 sc1\n"
    "s_waitcnt lgkmcnt(4)\n"
    "v_mfma_f32_32x32x16_bf16 v[80:95], a[40:43], v[212:215], v[80:95]\n"
    "ds_read_b128 a[40:43], v0 offset:34816\n"
    "s_waitcnt lgkmcnt(4)\n"
    "v_mfma_f32_32x32x16_bf16 v[80:95], a[24:27], v[216:219], v[80:95]\n"
    "ds_read_b128 a[24:27], v0 offset:35840\n"
    "s_waitcnt lgkmcnt(4)\n"
    "v_mfma_f32_32x32x16_bf16 v[80:95], a[28:31], v[220:223], v[80:95]\n"
    "ds_read_b128 a[28:31], v0 offset:36864\n"
    "buffer_store_dwordx2 a[60:61], v28, s[16:19], s65 offen offset:3072 sc1\n"
    "s_waitcnt lgkmcnt(4)\n"
    "v_mfma_f32_32x32x16_bf16 v[80:95], a[32:35], v[224:227], v[80:95]\n"
    "ds_read_b128 a[32:35], v0 offset:37888\n"
    "s_waitcnt lgkmcnt(4)\n"
    "v_mfma_f32_32x32x16_bf16 v[80:95], a[36:39], v[228:231], v[80:95]\n"
    "ds_read_b128 a[36:39], v0 offset:38912\n"
    "s_waitcnt lgkmcnt(4)\n"
    "v_mfma_f32_32x32x16_bf16 v[80:95], a[40:43], v[232:235], v[80:95]\n"
    "ds_read_b128 a[40:43], v0 offset:39936\n"
    "s_add_u32 s65, s65, 0x1000\n"
    "buffer_store_dwordx2 a[60:61], v28, s[16:19], s65 offen sc1\n"
    "s_waitcnt lgkmcnt(4)\n"
    "v_mfma_f32_32x32x16_bf16 v[80:95], a[24:27], v[236:239], v[80:95]\n"
    "s_waitcnt lgkmcnt(3)\n"
    "v_mfma_f32_32x32x16_bf16 v[80:95], a[28:31], v[240:243], v[80:95]\n"
    "s_waitcnt lgkmcnt(2)\n"
    "v_mfma_f32_32x32x16_bf16 v[80:95], a[32:35], v[244:247], v[80:95]\n"
    "s_waitcnt lgkmcnt(1)\n"
    "v_mfma_f32_32x32x16_bf16 v[80:95], a[36:39], v[248:251], v[80:95]\n"
    "s_waitcnt lgkmcnt(0)\n"
    "v_mfma_f32_32x32x16_bf16 v[80:95], a[40:43], v[252:255], v[80:95]\n"
    "v_accvgpr_read_b32 v16, a13\n"
    "v_accvgpr_read_b32 v17, a13\n"
    "s_nop 1\n"
    "v_mfma_f32_32x32x16_bf16 v[80:95], v[16:19], v[12:15], v[80:95]\n"
    "s_waitcnt vmcnt(18)\n"
    "v_cmp_ne_u64_e32 vcc, s[24:25], v[30:31]\n"
    "s_cbranch_vccnz .Lmk_slow_37\n"
    ".Lmk_back_38:\n"
    "s_nop 8\n"
    "v_mul_f32_e32 v80, v8, v80\n"
    "v_mul_f32_e32 v81, v8, v81\n"
    "v_mul_f32_e32 v82, v8, v82\n"
    "v_mul_f32_e32 v83, v8, v83\n"
    "v_mul_f32_e32 v84, v8, v84\n"
    "v_mul_f32_e32 v85, v8, v85\n"
    "v_mul_f32_e32 v86, v8, v86\n"
    "v_mul_f32_e32 v87, v8, v87\n"
    "v_mul_f32_e32 v88, v8, v88\n"
    "v_mul_f32_e32 v89, v8, v89\n"
    "v_mul_f32_e32 v90, v8, v90\n"
    "v_mul_f32_e32 v91, v8, v91\n"
    "v_mul_f32_e32 v92, v8, v92\n"
    "v_mul_f32_e32 v93, v8, v93\n"
    "v_mul_f32_e32 v94, v8, v94\n"
    "v_mul_f32_e32 v95, v8, v95\n"
    "v_cvt_pk_bf16_f32 v80, v80, v81\n"
    "v_cvt_pk_bf16_f32 v81, v82, v83\n"
    "v_cvt_pk_bf16_f32 v82, v84, v85\n"
    "v_cvt_pk_bf16_f32 v83, v86, v87\n"
    "v_cvt_pk_bf16_f32 v84, v88, v89\n"
    "v_cvt_pk_bf16_f32 v85, v90, v91\n"
    "v_cvt_pk_bf16_f32 v86, v92, v93\n"
    "v_cvt_pk_bf16_f32 v87, v94, v95\n"
    "v_add_u32_e32 v9, v23, v0\n"
    "ds_write_b128 v9, v[80:83]\n"
    "ds_write_b128 v9, v[84:87] offset:1024\n"
    "v_add_u32_e32 v9, v23, v21\n"
    "ds_write_b64 v9, v[66:67] offset:2048\n"
    "s_lshl_b32 s60, s29, 4\n"
    "s_add_u32 s60, s60, s28\n"
    "s_lshl_b32 s60, s60, 9\n"
    "s_add_u32 s60, s60, 0x2000\n"
    "s_lshl_b32 s61, s32, 3\n"
    "s_add_u32 s61, s61, s60\n"
    "s_waitcnt vmcnt(5)\n"
    "buffer_store_dwordx2 v[2:3], v4, s[16:19], s61 offen sc1\n"
    "s_waitcnt lgkmcnt(0)\n"
    "s_barrier\n"
    "s_mul_i32 s50, s35, 2560\n"
    "s_add_u32 s50, s50, 0x25800\n"
    "s_sub_u32 s51, 3, s35\n"
    "s_mul_i32 s51, s51, 2560\n"
    "s_add_u32 s51, s51, 0x25800\n"
    "s_lshl_b32 s52, s34, 10\n"
    "v_add_u32_e32 v9, s52, v0\n"
    "v_lshrrev_b32_e32 v10, 1, v0\n"
    "v_add_u32_e32 v11, s50, v9\n"
    "ds_read_b128 v[96:99], v11\n"
    "v_add_u32_e32 v11, s51, v9\n"
    "ds_read_b128 v[104:107], v11\n"
    "v_add_u32_e32 v11, s50, v10\n"
    "ds_read_b64 v[100:101], v11 offset:2048\n"
    "v_add_u32_e32 v11, s51, v10\n"
    "ds_read_b64 v[108:109], v11 offset:2048\n"
    "v_add_u32_e32 v12, 0x12000, v9\n"
    "v_add_u32_e32 v13, 0x12800, v10\n"
    "s_lshl_b32 s52, s34, 11\n"
    "s_add_u32 s52, s52, 0x12a00\n"
    "v_add_u32_e32 v14, s52, v0\n"
    "v_add_u32_e32 v18, 0xd000, v12\n"
    "v_add_u32_e32 v19, 0xd000, v13\n"
    "v_add_u32_e32 v20, 0xd000, v14\n"
    "s_cmp_eq_u32 s34, 0\n"
    "s_cselect_b32 s80, -1, 0\n"
    "s_cselect_b32 s81, 0, -1\n"
    "v_mov_b32_e32 v102, 0\n"
    "v_mov_b32_e32 v103, 0\n"
    "v_mov_b32_e32 v110, 0\n"
    "v_mov_b32_e32 v111, 0\n"
    "v_mov_b32_e32 v190, 0x3f803f80\n"
    "v_mov_b32_e32 v191, 0x3f80\n"
    "v_mov_b32_e32 v198, 0x3f803f80\n"
    "v_mov_b32_e32 v199, 0x3f80\n"
    "v_bfe_u32 v9, v0, 4, 5\n"
    "v_lshrrev_b32_e32 v10, 9, v0\n"
    "v_lshlrev_b32_e32 v10, 2, v10\n"
    "v_sub_u32_e32 v9, v9, v10\n"
    "v_mov_b32_e32 v10, 0xf1800000\n"
    "v_cmp_le_i32_e64 s[82:83], 0, v9\n"
    "v_cmp_le_i32_e64 s[84:85], 1, v9\n"
    "v_cmp_le_i32_e64 s[86:87], 2, v9\n"
    "v_cmp_le_i32_e64 s[88:89], 3, v9\n"
    "v_cndmask_b32_e64 v216, v10, 0, s[82:83]\n"
    "v_cndmask_b32_e64 v217, v10, 0, s[84:85]\n"
    "v_cndmask_b32_e64 v218, v10, 0, s[86:87]\n"
    "v_cndmask_b32_e64 v219, v10, 0, s[88:89]\n"
    "v_cmp_le_i32_e64 s[82:83], 8, v9\n"
    "v_cmp_le_i32_e64 s[84:85], 9, v9\n"
    "v_cmp_le_i32_e64 s[86:87], 10, v9\n"
    "v_cmp_le_i32_e64 s[88:89], 11, v9\n"
    "v_cndmask_b32_e64 v220, v10, 0, s[82:83]\n"
    "v_cndmask_b32_e64 v221, v10, 0, s[84:85]\n"
    "v_cndmask_b32_e64 v222, v10, 0, s[86:87]\n"
    "v_cndmask_b32_e64 v223, v10, 0, s[88:89]\n"
    "v_cmp_le_i32_e64 s[82:83], 16, v9\n"
    "v_cmp_le_i32_e64 s[84:85], 17, v9\n"
    "v_cmp_le_i32_e64 s[86:87], 18, v9\n"
    "v_cmp_le_i32_e64 s[88:89], 19, v9\n"
    "v_cndmask_b32_e64 v224, v10, 0, s[82:83]\n"
    "v_cndmask_b32_e64 v225, v10, 0, s[84:85]\n"
    "v_cndmask_b32_e64 v226, v10, 0, s[86:87]\n"
    "v_cndmask_b32_e64 v227, v10, 0, s[88:89]\n"
    "v_cmp_le_i32_e64 s[82:83], 24, v9\n"
    "v_cmp_le_i32_e64 s[84:85], 25, v9\n"
    "v_cmp_le_i32_e64 s[86:87], 26, v9\n"
    "v_cmp_le_i32_e64 s[88:89], 27, v9\n"
    "v_cndmask_b32_e64 v228, v10, 0, s[82:83]\n"
    "v_cndmask_b32_e64 v229, v10, 0, s[84:85]\n"
    "v_cndmask_b32_e64 v230, v10, 0, s[86:87]\n"
    "v_cndmask_b32_e64 v231, v10, 0, s[88:89]\n"
    "s_waitcnt lgkmcnt(1)\n"
    "v_cndmask_b32_e64 v100, 0, v100, s[80:81]\n"
    "v_cndmask_b32_e64 v101, 0, v101, s[80:81]\n"
    "s_waitcnt lgkmcnt(0)\n"
    "v_cndmask_b32_e64 v108, 0, v108, s[80:81]\n"
    "v_cndmask_b32_e64 v109, 0, v109, s[80:81]\n"
    "s_lshl_b32 s62, s27, 2\n"
    "s_add_u32 s62, s62, s26\n"
    "s_add_i32 s62, s62, -8\n"
    "s_add_i32 s63, s62, 4\n"
    "s_cmp_lt_i32 s62, 0\n"
    "s_cselect_b32 s64, 0, 0x3f803f80\n"
    "s_cselect_b32 s66, 0, s18\n"
    "s_cselect_b32 s62, s32, s62\n"
    "s_cmp_lt_i32 s63, 0\n"
    "s_cselect_b32 s65, 0, 0x3f803f80\n"
    "s_cselect_b32 s67, 0, s18\n"
    "s_cselect_b32 s63, s32, s63\n"
    "s_lshl_b32 s68, s62, 3\n"
    "s_lshl_b32 s69, s63, 3\n"
    "v_mov_b32_e32 v21, s68\n"
    "v_mov_b32_e32 v22, s69\n"
    "v_cndmask_b32_e64 v21, v21, v22, s[48:49]\n"
    "s_lshl_b32 s68, s28, 6\n"
    "s_add_u32 s68, s68, s62\n"
    "s_mul_i32 s62, s68, 6656\n"
    "s_mul_i32 s68, s29, 0x680000\n"
    "s_add_u32 s62, s62, s68\n"
    "s_add_u32 s62, s62, 0x12ad800\n"
    "s_add_u32 s68, s26, 0\n"
    "s_mul_i32 s68, s68, 6656\n"
    "s_add_u32 s68, s68, 0x12000\n"
    "s_mov_b32 s70, s68\n"
    "s_lshl_b32 s68, s28, 6\n"
    "s_add_u32 s68, s68, s63\n"
    "s_mul_i32 s63, s68, 6656\n"
    "s_mul_i32 s68, s29, 0x680000\n"
    "s_add_u32 s63, s63, s68\n"
    "s_add_u32 s63, s63, 0x12ad800\n"
    "s_add_u32 s69, s26, 4\n"
    "s_mul_i32 s69, s69, 6656\n"
    "s_add_u32 s69, s69, 0x12000\n"
    "s_add_u32 s90, s37, 0\n"
    "s_mul_i32 s90, s90, 40960\n"
    "s_mul_i32 s91, s26, 10240\n"
    "s_add_u32 s90, s90, s91\n"
    "s_add_u32 s90, s90, 0x30c000\n"
    "s_add_u32 s91, s91, 0x0\n"
    "s_add_u32 s92, s37, 1\n"
    "s_mul_i32 s92, s92, 40960\n"
    "s_mul_i32 s93, s26, 8192\n"
    "s_add_u32 s92, s92, s93\n"
    "s_add_u32 s92, s92, 0x30c000\n"
    "s_add_u32 s93, s93, 0xa000\n"
    "s_cmp_eq_u32 s35, 0\n"
    "s_cbranch_scc0 .Lmk_pr1_39\n"
    "ds_read_b128 v[184:187], v18\n"
    "ds_read_b64 v[188:189], v19\n"
    "ds_read_b128 v[200:203], v20\n"
    "ds_read_b128 v[204:207], v20 offset:1024\n"
    "ds_read_b128 v[192:195], v18\n"
    "ds_read_b64 v[196:197], v19\n"
    "s_waitcnt lgkmcnt(5)\n"
    "v_mfma_f32_32x32x16_bf16 v[144:159], v[184:187], v[96:99], v[216:231]\n"
    "s_waitcnt lgkmcnt(4)\n"
    "v_mfma_f32_32x32x16_bf16 v[144:159], v[188:191], v[100:103], v[144:159]\n"
    "s_nop 11\n"
    "v_exp_f32_e32 v144, v144\n"
    "v_exp_f32_e32 v145, v145\n"
    "v_exp_f32_e32 v146, v146\n"
    "v_exp_f32_e32 v147, v147\n"
    "s_waitcnt lgkmcnt(1)\n"
    "v_mfma_f32_32x32x16_bf16 v[160:175], v[192:195], v[104:107], 0\n"
    "ds_read_b128 v[208:211], v20\n"
    "v_exp_f32_e32 v148, v148\n"
    "v_exp_f32_e32 v149, v149\n"
    "v_exp_f32_e32 v150, v150\n"
    "v_exp_f32_e32 v151, v151\n"
    "s_mov_b32 m0, s91\n"
    "s_nop 0\n"
    "buffer_load_dwordx4 v0, s[16:19], s90 offen sc1 lds\n"
    "s_waitcnt lgkmcnt(1)\n"
    "v_mfma_f32_32x32x16_bf16 v[160:175], v[196:199], v[108:111], v[160:175]\n"
    "ds_read_b128 v[184:187], v18 offset:6656\n"
    "ds_read_b64 v[188:189], v19 offset:6656\n"
    "v_cvt_pk_bf16_f32 v176, v144, v145\n"
    "v_cvt_pk_bf16_f32 v177, v146, v147\n"
    "v_cvt_pk_bf16_f32 v178, v148, v149\n"
    "v_cvt_pk_bf16_f32 v179, v150, v151\n"
    "buffer_load_dwordx4 v0, s[16:19], s90 offen offset:1024 sc1 lds\n"
    "v_exp_f32_e32 v152, v152\n"
    "v_exp_f32_e32 v153, v153\n"
    "v_mfma_f32_32x32x16_bf16 v[112:127], v[200:203], v[176:179], 0\n"
    "v_exp_f32_e32 v154, v154\n"
    "v_exp_f32_e32 v155, v155\n"
    "v_exp_f32_e32 v156, v156\n"
    "buffer_load_dwordx4 v0, s[16:19], s90 offen offset:2048 sc1 lds\n"
    "v_exp_f32_e32 v157, v157\n"
    "v_exp_f32_e32 v158, v158\n"
    "v_exp_f32_e32 v159, v159\n"
    "buffer_load_dwordx4 v0, s[16:19], s90 offen offset:3072 sc1 lds\n"
    "v_cvt_pk_bf16_f32 v180, v152, v153\n"
    "v_cvt_pk_bf16_f32 v181, v154, v155\n"
    "v_cvt_pk_bf16_f32 v182, v156, v157\n"
    "v_cvt_pk_bf16_f32 v183, v158, v159\n"
    "ds_read_b128 v[212:215], v20 offset:1024\n"
    "s_nop 0\n"
    "v_mfma_f32_32x32x16_bf16 v[112:127], v[204:207], v[180:183], v[112:127]\n"
    "v_exp_f32_e32 v160, v160\n"
    "v_exp_f32_e32 v161, v161\n"
    "v_exp_f32_e32 v162, v162\n"
    "v_exp_f32_e32 v163, v163\n"
    "s_waitcnt lgkmcnt(1)\n"
    "v_mfma_f32_32x32x16_bf16 v[144:159], v[184:187], v[104:107], 0\n"
    "ds_read_b128 v[200:203], v20 offset:6656\n"
    "v_exp_f32_e32 v164, v164\n"
    "v_exp_f32_e32 v165, v165\n"
    "v_exp_f32_e32 v166, v166\n"
    "v_exp_f32_e32 v167, v167\n"
    "s_add_u32 m0, m0, 0x1000\n"
    "s_add_u32 s90, s90, 0x1000\n"
    "buffer_load_dwordx4 v0, s[16:19], s90 offen sc1 lds\n"
    "v_mfma_f32_32x32x16_bf16 v[144:159], v[188:191], v[108:111], v[144:159]\n"
    "ds_read_b128 v[192:195], v18 offset:13312\n"
    "ds_read_b64 v[196:197], v19 offset:13312\n"
    "v_cvt_pk_bf16_f32 v176, v160, v161\n"
    "v_cvt_pk_bf16_f32 v177, v162, v163\n"
    "v_cvt_pk_bf16_f32 v178, v164, v165\n"
    "v_cvt_pk_bf16_f32 v179, v166, v167\n"
    "buffer_load_dwordx4 v0, s[16:19], s90 offen offset:1024 sc1 lds\n"
    "v_exp_f32_e32 v168, v168\n"
    "v_exp_f32_e32 v169, v169\n"
    "v_mfma_f32_32x32x16_bf16 v[128:143], v[208:211], v[176:179], 0\n"
    "v_exp_f32_e32 v170, v170\n"
    "v_exp_f32_e32 v171, v171\n"
    "v_exp_f32_e32 v172, v172\n"
    "buffer_load_dwordx4 v0, s[16:19], s90 offen offset:2048 sc1 lds\n"
    "v_exp_f32_e32 v173, v173\n"
    "v_exp_f32_e32 v174, v174\n"
    "v_exp_f32_e32 v175, v175\n"
    "buffer_load_dwordx4 v0, s[16:19], s90 offen offset:3072 sc1 lds\n"
    "v_cvt_pk_bf16_f32 v180, v168, v169\n"
    "v_cvt_pk_bf16_f32 v181, v170, v171\n"
    "v_cvt_pk_bf16_f32 v182, v172, v173\n"
    "v_cvt_pk_bf16_f32 v183, v174, v175\n"
    "s_waitcnt lgkmcnt(3)\n"
    "ds_read_b128 v[204:207], v20 offset:7680\n"
    "v_mfma_f32_32x32x16_bf16 v[128:143], v[212:215], v[180:183], v[128:143]\n"
    "v_exp_f32_e32 v144, v144\n"
    "v_exp_f32_e32 v145, v145\n"
    "v_exp_f32_e32 v146, v146\n"
    "v_exp_f32_e32 v147, v147\n"
    "s_waitcnt lgkmcnt(1)\n"
    "v_mfma_f32_32x32x16_bf16 v[160:175], v[192:195], v[104:107], 0\n"
    "ds_read_b128 v[208:211], v20 offset:13312\n"
    "v_exp_f32_e32 v148, v148\n"
    "v_exp_f32_e32 v149, v149\n"
    "v_exp_f32_e32 v150, v150\n"
    "v_exp_f32_e32 v151, v151\n"
    "s_add_u32 m0, m0, 0x1000\n"
    "s_add_u32 s90, s90, 0x1000\n"
    "buffer_load_dwordx4 v0, s[16:19], s90 offen sc1 lds\n"
    "v_mfma_f32_32x32x16_bf16 v[160:175], v[196:199], v[108:111], v[160:175]\n"
    "ds_read_b128 v[184:187], v18 offset:19968\n"
    "ds_read_b64 v[188:189], v19 offset:19968\n"
    "v_cvt_pk_bf16_f32 v176, v144, v145\n"
    "v_cvt_pk_bf16_f32 v177, v146, v147\n"
    "v_cvt_pk_bf16_f32 v178, v148, v149\n"
    "v_cvt_pk_bf16_f32 v179, v150, v151\n"
    "buffer_load_dwordx4 v0, s[16:19], s90 offen offset:1024 sc1 lds\n"
    "v_exp_f32_e32 v152, v152\n"
    "v_exp_f32_e32 v153, v153\n"
    "v_mfma_f32_32x32x16_bf16 v[128:143], v[200:203], v[176:179], v[128:143]\n"
    "v_exp_f32_e32 v154, v154\n"
    "v_exp_f32_e32 v155, v155\n"
    "v_exp_f32_e32 v156, v156\n"
    "s_mov_b32 m0, s93\n"
    "s_nop 0\n"
    "buffer_load_dwordx4 v0, s[16:19], s92 offen sc1 lds\n"
    "v_exp_f32_e32 v157, v157\n"
    "v_exp_f32_e32 v158, v158\n"
    "v_exp_f32_e32 v159, v159\n"
    "buffer_load_dwordx4 v0, s[16:19], s92 offen offset:1024 sc1 lds\n"
    "v_cvt_pk_bf16_f32 v180, v152, v153\n"
    "v_cvt_pk_bf16_f32 v181, v154, v155\n"
    "v_cvt_pk_bf16_f32 v182, v156, v157\n"
    "v_cvt_pk_bf16_f32 v183, v158, v159\n"
    "s_waitcnt lgkmcnt(3)\n"
    "ds_read_b128 v[212:215], v20 offset:14336\n"
    "v_mfma_f32_32x32x16_bf16 v[128:143], v[204:207], v[180:183], v[128:143]\n"
    "v_exp_f32_e32 v160, v160\n"
    "v_exp_f32_e32 v161, v161\n"
    "v_exp_f32_e32 v162, v162\n"
    "v_exp_f32_e32 v163, v163\n"
    "s_waitcnt lgkmcnt(1)\n"
    "v_mfma_f32_32x32x16_bf16 v[144:159], v[184:187], v[104:107], v[216:231]\n"
    "ds_read_b128 v[200:203], v20 offset:19968\n"
    "v_exp_f32_e32 v164, v164\n"
    "v_exp_f32_e32 v165, v165\n"
    "v_exp_f32_e32 v166, v166\n"
    "v_exp_f32_e32 v167, v167\n"
    "buffer_load_dwordx4 v0, s[16:19], s92 offen offset:2048 sc1 lds\n"
    "v_mfma_f32_32x32x16_bf16 v[144:159], v[188:191], v[108:111], v[144:159]\n"
    "v_cvt_pk_bf16_f32 v176, v160, v161\n"
    "v_cvt_pk_bf16_f32 v177, v162, v163\n"
    "v_cvt_pk_bf16_f32 v178, v164, v165\n"
    "v_cvt_pk_bf16_f32 v179, v166, v167\n"
    "buffer_load_dwordx4 v0, s[16:19], s92 offen offset:3072 sc1 lds\n"
    "v_exp_f32_e32 v168, v168\n"
    "v_exp_f32_e32 v169, v169\n"
    "v_mfma_f32_32x32x16_bf16 v[128:143], v[208:211], v[176:179], v[128:143]\n"
    "v_exp_f32_e32 v170, v170\n"
    "v_exp_f32_e32 v171, v171\n"
    "v_exp_f32_e32 v172, v172\n"
    "s_add_u32 m0, m0, 0x1000\n"
    "s_add_u32 s92, s92, 0x1000\n"
    "buffer_load_dwordx4 v0, s[16:19], s92 offen sc1 lds\n"
    "v_exp_f32_e32 v173, v173\n"
    "v_exp_f32_e32 v174, v174\n"
    "v_exp_f32_e32 v175, v175\n"
    "buffer_load_dwordx4 v0, s[16:19], s92 offen offset:1024 sc1 lds\n"
    "v_cvt_pk_bf16_f32 v180, v168, v169\n"
    "v_cvt_pk_bf16_f32 v181, v170, v171\n"
    "v_cvt_pk_bf16_f32 v182, v172, v173\n"
    "v_cvt_pk_bf16_f32 v183, v174, v175\n"
    "buffer_load_dwordx2 v[22:23], v21, s[16:19], s60 offen sc1\n"
    "s_waitcnt lgkmcnt(1)\n"
    "ds_read_b128 v[204:207], v20 offset:20992\n"
    "v_mfma_f32_32x32x16_bf16 v[128:143], v[212:215], v[180:183], v[128:143]\n"
    "v_exp_f32_e32 v144, v144\n"
    "v_exp_f32_e32 v145, v145\n"
    "v_exp_f32_e32 v146, v146\n"
    "v_exp_f32_e32 v147, v147\n"
    "v_exp_f32_e32 v148, v148\n"
    "v_exp_f32_e32 v149, v149\n"
    "v_exp_f32_e32 v150, v150\n"
    "v_exp_f32_e32 v151, v151\n"
    "buffer_load_dwordx4 v0, s[16:19], s92 offen offset:2048 sc1 lds\n"
    "v_cvt_pk_bf16_f32 v176, v144, v145\n"
    "v_cvt_pk_bf16_f32 v177, v146, v147\n"
    "v_cvt_pk_bf16_f32 v178, v148, v149\n"
    "v_cvt_pk_bf16_f32 v179, v150, v151\n"
    "buffer_load_dwordx4 v0, s[16:19], s92 offen offset:3072 sc1 lds\n"
    "v_exp_f32_e32 v152, v152\n"
    "v_exp_f32_e32 v153, v153\n"
    "s_waitcnt lgkmcnt(1)\n"
    "v_mfma_f32_32x32x16_bf16 v[128:143], v[200:203], v[176:179], v[128:143]\n"
    "v_exp_f32_e32 v154, v154\n"
    "v_exp_f32_e32 v155, v155\n"
    "v_exp_f32_e32 v156, v156\n"
    "v_exp_f32_e32 v157, v157\n"
    "v_exp_f32_e32 v158, v158\n"
    "v_exp_f32_e32 v159, v159\n"
    "v_cvt_pk_bf16_f32 v180, v152, v153\n"
    "v_cvt_pk_bf16_f32 v181, v154, v155\n"
    "v_cvt_pk_bf16_f32 v182, v156, v157\n"
    "v_cvt_pk_bf16_f32 v183, v158, v159\n"
    "s_waitcnt lgkmcnt(0)\n"
    "s_nop 0\n"
    "v_mfma_f32_32x32x16_bf16 v[128:143], v[204:207], v[180:183], v[128:143]\n"
    "s_branch .Lmk_ownj_40\n"
    ".Lmk_pr1_39:\n"
    "ds_read_b128 v[184:187], v18\n"
    "ds_read_b64 v[188:189], v19\n"
    "ds_read_b128 v[200:203], v20\n"
    "ds_read_b128 v[204:207], v20 offset:1024\n"
    "ds_read_b128 v[192:195], v18 offset:6656\n"
    "ds_read_b64 v[196:197], v19 offset:6656\n"
    "s_waitcnt lgkmcnt(5)\n"
    "v_mfma_f32_32x32x16_bf16 v[144:159], v[184:187], v[96:99], 0\n"
    "s_waitcnt lgkmcnt(4)\n"
    "v_mfma_f32_32x32x16_bf16 v[144:159], v[188:191], v[100:103], v[144:159]\n"
    "s_nop 11\n"
    "v_exp_f32_e32 v144, v144\n"
    "v_exp_f32_e32 v145, v145\n"
    "v_exp_f32_e32 v146, v146\n"
    "v_exp_f32_e32 v147, v147\n"
    "s_waitcnt lgkmcnt(1)\n"
    "v_mfma_f32_32x32x16_bf16 v[160:175], v[192:195], v[96:99], v[216:231]\n"
    "ds_read_b128 v[208:211], v20 offset:6656\n"
    "v_exp_f32_e32 v148, v148\n"
    "v_exp_f32_e32 v149, v149\n"
    "v_exp_f32_e32 v150, v150\n"
    "v_exp_f32_e32 v151, v151\n"
    "s_mov_b32 m0, s91\n"
    "s_nop 0\n"
    "buffer_load_dwordx4 v0, s[16:19], s90 offen sc1 lds\n"
    "s_waitcnt lgkmcnt(1)\n"
    "v_mfma_f32_32x32x16_bf16 v[160:175], v[196:199], v[100:103], v[160:175]\n"
    "ds_read_b128 v[184:187], v18\n"
    "ds_read_b64 v[188:189], v19\n"
    "v_cvt_pk_bf16_f32 v176, v144, v145\n"
    "v_cvt_pk_bf16_f32 v177, v146, v147\n"
    "v_cvt_pk_bf16_f32 v178, v148, v149\n"
    "v_cvt_pk_bf16_f32 v179, v150, v151\n"
    "buffer_load_dwordx4 v0, s[16:19], s90 offen offset:1024 sc1 lds\n"
    "v_exp_f32_e32 v152, v152\n"
    "v_exp_f32_e32 v153, v153\n"
    "v_mfma_f32_32x32x16_bf16 v[112:127], v[200:203], v[176:179], 0\n"
    "v_exp_f32_e32 v154, v154\n"
    "v_exp_f32_e32 v155, v155\n"
    "v_exp_f32_e32 v156, v156\n"
    "buffer_load_dwordx4 v0, s[16:19], s90 offen offset:2048 sc1 lds\n"
    "v_exp_f32_e32 v157, v157\n"
    "v_exp_f32_e32 v158, v158\n"
    "v_exp_f32_e32 v159, v159\n"
    "buffer_load_dwordx4 v0, s[16:19], s90 offen offset:3072 sc1 lds\n"
    "v_cvt_pk_bf16_f32 v180, v152, v153\n"
    "v_cvt_pk_bf16_f32 v181, v154, v155\n"
    "v_cvt_pk_bf16_f32 v182, v156, v157\n"
    "v_cvt_pk_bf16_f32 v183, v158, v159\n"
    "ds_read_b128 v[212:215], v20 offset:7680\n"
    "s_nop 0\n"
    "v_mfma_f32_32x32x16_bf16 v[112:127], v[204:207], v[180:183], v[112:127]\n"
    "v_exp_f32_e32 v160, v160\n"
    "v_exp_f32_e32 v161, v161\n"
    "v_exp_f32_e32 v162, v162\n"
    "v_exp_f32_e32 v163, v163\n"
    "s_waitcnt lgkmcnt(1)\n"
    "v_mfma_f32_32x32x16_bf16 v[144:159], v[184:187], v[104:107], 0\n"
    "ds_read_b128 v[200:203], v20\n"
    "v_exp_f32_e32 v164, v164\n"
    "v_exp_f32_e32 v165, v165\n"
    "v_exp_f32_e32 v166, v166\n"
    "v_exp_f32_e32 v167, v167\n"
    "s_add_u32 m0, m0, 0x1000\n"
    "s_add_u32 s90, s90, 0x1000\n"
    "buffer_load_dwordx4 v0, s[16:19], s90 offen sc1 lds\n"
    "v_mfma_f32_32x32x16_bf16 v[144:159], v[188:191], v[108:111], v[144:159]\n"
    "ds_read_b128 v[192:195], v18 offset:6656\n"
    "ds_read_b64 v[196:197], v19 offset:6656\n"
    "v_cvt_pk_bf16_f32 v176, v160, v161\n"
    "v_cvt_pk_bf16_f32 v177, v162, v163\n"
    "v_cvt_pk_bf16_f32 v178, v164, v165\n"
    "v_cvt_pk_bf16_f32 v179, v166, v167\n"
    "buffer_load_dwordx4 v0, s[16:19], s90 offen offset:1024 sc1 lds\n"
    "v_exp_f32_e32 v168, v168\n"
    "v_exp_f32_e32 v169, v169\n"
    "v_mfma_f32_32x32x16_bf16 v[112:127], v[208:211], v[176:179], v[112:127]\n"
    "v_exp_f32_e32 v170, v170\n"
    "v_exp_f32_e32 v171, v171\n"
    "v_exp_f32_e32 v172, v172\n"
    "buffer_load_dwordx4 v0, s[16:19], s90 offen offset:2048 sc1 lds\n"
    "v_exp_f32_e32 v173, v173\n"
    "v_exp_f32_e32 v174, v174\n"
    "v_exp_f32_e32 v175, v175\n"
    "buffer_load_dwordx4 v0, s[16:19], s90 offen offset:3072 sc1 lds\n"
    "v_cvt_pk_bf16_f32 v180, v168, v169\n"
    "v_cvt_pk_bf16_f32 v181, v170, v171\n"
    "v_cvt_pk_bf16_f32 v182, v172, v173\n"
    "v_cvt_pk_bf16_f32 v183, v174, v175\n"
    "s_waitcnt lgkmcnt(3)\n"
    "ds_read_b128 v[204:207], v20 offset:1024\n"
    "v_mfma_f32_32x32x16_bf16 v[112:127], v[212:215], v[180:183], v[112:127]\n"
    "v_exp_f32_e32 v144, v144\n"
    "v_exp_f32_e32 v145, v145\n"
    "v_exp_f32_e32 v146, v146\n"
    "v_exp_f32_e32 v147, v147\n"
    "s_waitcnt lgkmcnt(1)\n"
    "v_mfma_f32_32x32x16_bf16 v[160:175], v[192:195], v[104:107], 0\n"
    "ds_read_b128 v[208:211], v20 offset:6656\n"
    "v_exp_f32_e32 v148, v148\n"
    "v_exp_f32_e32 v149, v149\n"
    "v_exp_f32_e32 v150, v150\n"
    "v_exp_f32_e32 v151, v151\n"
    "s_add_u32 m0, m0, 0x1000\n"
    "s_add_u32 s90, s90, 0x1000\n"
    "buffer_load_dwordx4 v0, s[16:19], s90 offen sc1 lds\n"
    "v_mfma_f32_32x32x16_bf16 v[160:175], v[196:199], v[108:111], v[160:175]\n"
    "ds_read_b128 v[184:187], v18 offset:13312\n"
    "ds_read_b64 v[188:189], v19 offset:13312\n"
    "v_cvt_pk_bf16_f32 v176, v144, v145\n"
    "v_cvt_pk_bf16_f32 v177, v146, v147\n"
    "v_cvt_pk_bf16_f32 v178, v148, v149\n"
    "v_cvt_pk_bf16_f32 v179, v150, v151\n"
    "buffer_load_dwordx4 v0, s[16:19], s90 offen offset:1024 sc1 lds\n"
    "v_exp_f32_e32 v152, v152\n"
    "v_exp_f32_e32 v153, v153\n"
    "v_mfma_f32_32x32x16_bf16 v[128:143], v[200:203], v[176:179], 0\n"
    "v_exp_f32_e32 v154, v154\n"
    "v_exp_f32_e32 v155, v155\n"
    "v_exp_f32_e32 v156, v156\n"
    "s_mov_b32 m0, s93\n"
    "s_nop 0\n"
    "buffer_load_dwordx4 v0, s[16:19], s92 offen sc1 lds\n"
    "v_exp_f32_e32 v157, v157\n"
    "v_exp_f32_e32 v158, v158\n"
    "v_exp_f32_e32 v159, v159\n"
    "buffer_load_dwordx4 v0, s[16:19], s92 offen offset:1024 sc1 lds\n"
    "v_cvt_pk_bf16_f32 v180, v152, v153\n"
    "v_cvt_pk_bf16_f32 v181, v154, v155\n"
    "v_cvt_pk_bf16_f32 v182, v156, v157\n"
    "v_cvt_pk_bf16_f32 v183, v158, v159\n"
    "s_waitcnt lgkmcnt(3)\n"
    "ds_read_b128 v[212:215], v20 offset:7680\n"
    "v_mfma_f32_32x32x16_bf16 v[128:143], v[204:207], v[180:183], v[128:143]\n"
    "v_exp_f32_e32 v160, v160\n"
    "v_exp_f32_e32 v161, v161\n"
    "v_exp_f32_e32 v162, v162\n"
    "v_exp_f32_e32 v163, v163\n"
    "s_waitcnt lgkmcnt(1)\n"
    "v_mfma_f32_32x32x16_bf16 v[144:159], v[184:187], v[104:107], v[216:231]\n"
    "ds_read_b128 v[200:203], v20 offset:13312\n"
    "v_exp_f32_e32 v164, v164\n"
    "v_exp_f32_e32 v165, v165\n"
    "v_exp_f32_e32 v166, v166\n"
    "v_exp_f32_e32 v167, v167\n"
    "buffer_load_dwordx4 v0, s[16:19], s92 offen offset:2048 sc1 lds\n"
    "v_mfma_f32_32x32x16_bf16 v[144:159], v[188:191], v[108:111], v[144:159]\n"
    "v_cvt_pk_bf16_f32 v176, v160, v161\n"
    "v_cvt_pk_bf16_f32 v177, v162, v163\n"
    "v_cvt_pk_bf16_f32 v178, v164, v165\n"
    "v_cvt_pk_bf16_f32 v179, v166, v167\n"
    "buffer_load_dwordx4 v0, s[16:19], s92 offen offset:3072 sc1 lds\n"
    "v_exp_f32_e32 v168, v168\n"
    "v_exp_f32_e32 v169, v169\n"
    "v_mfma_f32_32x32x16_bf16 v[128:143], v[208:211], v[176:179], v[128:143]\n"
    "v_exp_f32_e32 v170, v170\n"
    "v_exp_f32_e32 v171, v171\n"
    "v_exp_f32_e32 v172, v172\n"
    "s_add_u32 m0, m0, 0x1000\n"
    "s_add_u32 s92, s92, 0x1000\n"
    "buffer_load_dwordx4 v0, s[16:19], s92 offen sc1 lds\n"
    "v_exp_f32_e32 v173, v173\n"
    "v_exp_f32_e32 v174, v174\n"
    "v_exp_f32_e32 v175, v175\n"
    "buffer_load_dwordx4 v0, s[16:19], s92 offen offset:1024 sc1 lds\n"
    "v_cvt_pk_bf16_f32 v180, v168, v169\n"
    "v_cvt_pk_bf16_f32 v181, v170, v171\n"
    "v_cvt_pk_bf16_f32 v182, v172, v173\n"
    "v_cvt_pk_bf16_f32 v183, v174, v175\n"
    "buffer_load_dwordx2 v[22:23], v21, s[16:19], s60 offen sc1\n"
    "s_waitcnt lgkmcnt(1)\n"
    "ds_read_b128 v[204:207], v20 offset:14336\n"
    "v_mfma_f32_32x32x16_bf16 v[128:143], v[212:215], v[180:183], v[128:143]\n"
    "v_exp_f32_e32 v144, v144\n"
    "v_exp_f32_e32 v145, v145\n"
    "v_exp_f32_e32 v146, v146\n"
    "v_exp_f32_e32 v147, v147\n"
    "v_exp_f32_e32 v148, v148\n"
    "v_exp_f32_e32 v149, v149\n"
    "v_exp_f32_e32 v150, v150\n"
    "v_exp_f32_e32 v151, v151\n"
    "buffer_load_dwordx4 v0, s[16:19], s92 offen offset:2048 sc1 lds\n"
    "v_cvt_pk_bf16_f32 v176, v144, v145\n"
    "v_cvt_pk_bf16_f32 v177, v146, v147\n"
    "v_cvt_pk_bf16_f32 v178, v148, v149\n"
    "v_cvt_pk_bf16_f32 v179, v150, v151\n"
    "buffer_load_dwordx4 v0, s[16:19], s92 offen offset:3072 sc1 lds\n"
    "v_exp_f32_e32 v152, v152\n"
    "v_exp_f32_e32 v153, v153\n"
    "s_waitcnt lgkmcnt(1)\n"
    "v_mfma_f32_32x32x16_bf16 v[128:143], v[200:203], v[176:179], v[128:143]\n"
    "v_exp_f32_e32 v154, v154\n"
    "v_exp_f32_e32 v155, v155\n"
    "v_exp_f32_e32 v156, v156\n"
    "v_exp_f32_e32 v157, v157\n"
    "v_exp_f32_e32 v158, v158\n"
    "v_exp_f32_e32 v159, v159\n"
    "v_cvt_pk_bf16_f32 v180, v152, v153\n"
    "v_cvt_pk_bf16_f32 v181, v154, v155\n"
    "v_cvt_pk_bf16_f32 v182, v156, v157\n"
    "v_cvt_pk_bf16_f32 v183, v158, v159\n"
    "s_waitcnt lgkmcnt(0)\n"
    "s_nop 0\n"
    "v_mfma_f32_32x32x16_bf16 v[128:143], v[204:207], v[180:183], v[128:143]\n"
    ".Lmk_ownj_40:\n"
    "s_mov_b32 s20, s16\n"
    "s_mov_b32 s21, s17\n"
    "s_waitcnt vmcnt(2)\n"
    "v_cmp_ne_u64_e32 vcc, s[24:25], v[22:23]\n"
    "s_cbranch_vccnz .Lmk_slow_41\n"
    ".Lmk_back_42:\n"
    "s_mov_b32 s22, s66\n"
    "s_mov_b32 m0, s70\n"
    "s_nop 0\n"
    "buffer_load_dwordx4 v0, s[20:23], s62 offen sc1 lds\n"
    "buffer_load_dwordx4 v0, s[20:23], s62 offen offset:1024 sc1 lds\n"
    "s_mov_b32 exec_hi, 0\n"
    "buffer_load_dwordx4 v0, s[20:23], s62 offen offset:2048 sc1 lds\n"
    "s_mov_b32 exec_lo, 0xfffff\n"
    "s_mov_b32 exec_hi, 0xfffff\n"
    "s_add_u32 s62, s62, 2560\n"
    "s_add_u32 m0, m0, 2560\n"
    "s_nop 0\n"
    "buffer_load_dwordx4 v0, s[20:23], s62 offen sc1 lds\n"
    "buffer_load_dwordx4 v0, s[20:23], s62 offen offset:1024 sc1 lds\n"
    "buffer_load_dwordx4 v0, s[20:23], s62 offen offset:2048 sc1 lds\n"
    "buffer_load_dwordx4 v0, s[20:23], s62 offen offset:3072 sc1 lds\n"
    "s_mov_b64 exec, -1\n"
    "s_mov_b32 s22, s67\n"
    "s_mov_b32 m0, s69\n"
    "s_nop 0\n"
    "buffer_load_dwordx4 v0, s[20:23], s63 offen sc1 lds\n"
    "buffer_load_dwordx4 v0, s[20:23], s63 offen offset:1024 sc1 lds\n"
    "s_mov_b32 exec_hi, 0\n"
    "buffer_load_dwordx4 v0, s[20:23], s63 offen offset:2048 sc1 lds\n"
    "s_mov_b32 exec_lo, 0xfffff\n"
    "s_mov_b32 exec_hi, 0xfffff\n"
    "s_add_u32 s63, s63, 2560\n"
    "s_add_u32 m0, m0, 2560\n"
    "s_nop 0\n"
    "buffer_load_dwordx4 v0, s[20:23], s63 offen sc1 lds\n"
    "buffer_load_dwordx4 v0, s[20:23], s63 offen offset:1024 sc1 lds\n"
    "buffer_load_dwordx4 v0, s[20:23], s63 offen offset:2048 sc1 lds\n"
    "buffer_load_dwordx4 v0, s[20:23], s63 offen offset:3072 sc1 lds\n"
    "s_mov_b64 exec, -1\n"
    "v_bfe_u32 v10, v0, 6, 2\n"
    "v_lshlrev_b32_e32 v10, 10, v10\n"
    "v_bfe_u32 v11, v0, 4, 1\n"
    "v_lshl_add_u32 v10, v11, 9, v10\n"
    "v_bfe_u32 v11, v0, 5, 1\n"
    "v_lshl_add_u32 v10, v11, 6, v10\n"
    "v_add_u32_e32 v10, s70, v10\n"
    "v_mov_b32_e32 v24, s64\n"
    "v_mov_b32_e32 v25, s64\n"
    "v_mov_b32_e32 v26, s64\n"
    "v_mov_b32_e32 v27, s64\n"
    "s_mov_b64 exec, 0xffff\n"
    "ds_write_b128 v10, v[24:27] offset:2880\n"
    "s_mov_b64 exec, -1\n"
    "v_bfe_u32 v10, v0, 6, 2\n"
    "v_lshlrev_b32_e32 v10, 10, v10\n"
    "v_bfe_u32 v11, v0, 4, 1\n"
    "v_lshl_add_u32 v10, v11, 9, v10\n"
    "v_bfe_u32 v11, v0, 5, 1\n"
    "v_lshl_add_u32 v10, v11, 6, v10\n"
    "v_add_u32_e32 v10, s69, v10\n"
    "v_mov_b32_e32 v24, s65\n"
    "v_mov_b32_e32 v25, s65\n"
    "v_mov_b32_e32 v26, s65\n"
    "v_mov_b32_e32 v27, s65\n"
    "s_mov_b64 exec, 0xffff\n"
    "ds_write_b128 v10, v[24:27] offset:2880\n"
    "s_mov_b64 exec, -1\n"
    "v_bfe_u32 v9, v0, 4, 5\n"
    "v_lshrrev_b32_e32 v10, 9, v0\n"
    "v_lshlrev_b32_e32 v10, 2, v10\n"
    "v_sub_u32_e32 v9, v9, v10\n"
    "v_mov_b32_e32 v10, 0xf1800000\n"
    "v_cmp_le_i32_e64 s[82:83], 0, v9\n"
    "v_cmp_le_i32_e64 s[84:85], 1, v9\n"
    "v_cmp_le_i32_e64 s[86:87], 2, v9\n"
    "v_cmp_le_i32_e64 s[88:89], 3, v9\n"
    "v_cndmask_b32_e64 v232, 0, v10, s[82:83]\n"
    "v_cndmask_b32_e64 v233, 0, v10, s[84:85]\n"
    "v_cndmask_b32_e64 v234, 0, v10, s[86:87]\n"
    "v_cndmask_b32_e64 v235, 0, v10, s[88:89]\n"
    "v_cmp_le_i32_e64 s[82:83], 8, v9\n"
    "v_cmp_le_i32_e64 s[84:85], 9, v9\n"
    "v_cmp_le_i32_e64 s[86:87], 10, v9\n"
    "v_cmp_le_i32_e64 s[88:89], 11, v9\n"
    "v_cndmask_b32_e64 v236, 0, v10, s[82:83]\n"
    "v_cndmask_b32_e64 v237, 0, v10, s[84:85]\n"
    "v_cndmask_b32_e64 v238, 0, v10, s[86:87]\n"
    "v_cndmask_b32_e64 v239, 0, v10, s[88:89]\n"
    "v_cmp_le_i32_e64 s[82:83], 16, v9\n"
    "v_cmp_le_i32_e64 s[84:85], 17, v9\n"
    "v_cmp_le_i32_e64 s[86:87], 18, v9\n"
    "v_cmp_le_i32_e64 s[88:89], 19, v9\n"
    "v_cndmask_b32_e64 v240, 0, v10, s[82:83]\n"
    "v_cndmask_b32_e64 v241, 0, v10, s[84:85]\n"
    "v_cndmask_b32_e64 v242, 0, v10, s[86:87]\n"
    "v_cndmask_b32_e64 v243, 0, v10, s[88:89]\n"
    "v_cmp_le_i32_e64 s[82:83], 24, v9\n"
    "v_cmp_le_i32_e64 s[84:85], 25, v9\n"
    "v_cmp_le_i32_e64 s[86:87], 26, v9\n"
    "v_cmp_le_i32_e64 s[88:89], 27, v9\n"
    "v_cndmask_b32_e64 v244, 0, v10, s[82:83]\n"
    "v_cndmask_b32_e64 v245, 0, v10, s[84:85]\n"
    "v_cndmask_b32_e64 v246, 0, v10, s[86:87]\n"
    "v_cndmask_b32_e64 v247, 0, v10, s[88:89]\n"
    "s_cmp_eq_u32 s29, 0\n"
    "s_cbranch_scc0 .Lmk_l0a_43\n"
    "s_mov_b32 s20, s42\n"
    "s_and_b32 s21, s43, 0xffff\n"
    "s_mov_b32 s22, 0xd80\n"
    "v_and_b32_e32 v52, 0x1f0, v0\n"
    "v_lshrrev_b32_e32 v52, 2, v52\n"
    "v_and_b32_e32 v53, 0x200, v0\n"
    "v_or_b32_e32 v52, v52, v53\n"
    "v_and_b32_e32 v53, 0x1f0, v0\n"
    "v_lshrrev_b32_e32 v53, 3, v53\n"
    "s_add_u32 s55, s37, 0\n"
    "s_min_u32 s55, s55, 53\n"
    "s_lshl_b32 s56, s55, 10\n"
    "s_add_u32 s56, s56, 0x528000\n"
    "buffer_load_dword v32, v52, s[16:19], s56 offen sc1\n"
    "buffer_load_dword v33, v52, s[16:19], s56 offen offset:128 sc1\n"
    "buffer_load_dword v34, v52, s[16:19], s56 offen offset:256 sc1\n"
    "buffer_load_dword v35, v52, s[16:19], s56 offen offset:384 sc1\n"
    "s_lshl_b32 s56, s55, 6\n"
    "buffer_load_ushort v48, v53, s[20:23], s56 offen\n"
    "s_add_u32 s55, s37, 1\n"
    "s_min_u32 s55, s55, 53\n"
    "s_lshl_b32 s56, s55, 10\n"
    "s_add_u32 s56, s56, 0x528000\n"
    "buffer_load_dword v36, v52, s[16:19], s56 offen sc1\n"
    "buffer_load_dword v37, v52, s[16:19], s56 offen offset:128 sc1\n"
    "buffer_load_dword v38, v52, s[16:19], s56 offen offset:256 sc1\n"
    "buffer_load_dword v39, v52, s[16:19], s56 offen offset:384 sc1\n"
    "s_lshl_b32 s56, s55, 6\n"
    "buffer_load_ushort v49, v53, s[20:23], s56 offen\n"
    "s_add_u32 s55, s37, 2\n"
    "s_min_u32 s55, s55, 53\n"
    "s_lshl_b32 s56, s55, 10\n"
    "s_add_u32 s56, s56, 0x528000\n"
    "buffer_load_dword v40, v52, s[16:19], s56 offen sc1\n"
    "buffer_load_dword v41, v52, s[16:19], s56 offen offset:128 sc1\n"
    "buffer_load_dword v42, v52, s[16:19], s56 offen offset:256 sc1\n"
    "buffer_load_dword v43, v52, s[16:19], s56 offen offset:384 sc1\n"
    "s_lshl_b32 s56, s55, 6\n"
    "buffer_load_ushort v50, v53, s[20:23], s56 offen\n"
    "s_add_u32 s55, s37, 3\n"
    "s_min_u32 s55, s55, 53\n"
    "s_lshl_b32 s56, s55, 10\n"
    "s_add_u32 s56, s56, 0x528000\n"
    "buffer_load_dword v44, v52, s[16:19], s56 offen sc1\n"
    "buffer_load_dword v45, v52, s[16:19], s56 offen offset:128 sc1\n"
    "buffer_load_dword v46, v52, s[16:19], s56 offen offset:256 sc1\n"
    "buffer_load_dword v47, v52, s[16:19], s56 offen offset:384 sc1\n"
    "s_lshl_b32 s56, s55, 6\n"
    "buffer_load_ushort v51, v53, s[20:23], s56 offen\n"
    ".Lmk_l0a_43:\n"
    "s_mul_i32 s92, s31, 0x19000\n"
    "s_add_u32 s92, s92, 0x535800\n"
    "s_mul_i32 s71, s26, 25600\n"
    "s_add_u32 s92, s92, s71\n"
    "s_mul_i32 s93, s31, 0x46000\n"
    "s_add_u32 s93, s93, 0x5fd800\n"
    "s_mul_i32 s71, s26, 71680\n"
    "s_add_u32 s93, s93, s71\n"
    "s_waitcnt vmcnt(0)\n"
    "s_waitcnt lgkmcnt(0)\n"
    "s_barrier\n"
    "s_cmp_eq_u32 s35, 0\n"
    "s_cbranch_scc0 .Lmk_hpr1_44\n"
    "ds_read_b128 v[184:187], v12\n"
    "ds_read_b64 v[188:189], v13\n"
    "ds_read_b128 v[200:203], v14\n"
    "ds_read_b128 v[204:207], v14 offset:1024\n"
    "ds_read_b128 v[192:195], v12 offset:6656\n"
    "ds_read_b64 v[196:197], v13 offset:6656\n"
    "s_waitcnt lgkmcnt(5)\n"
    "v_mfma_f32_32x32x16_bf16 v[144:159], v[184:187], v[96:99], v[232:247]\n"
    "s_waitcnt lgkmcnt(4)\n"
    "v_mfma_f32_32x32x16_bf16 v[144:159], v[188:191], v[100:103], v[144:159]\n"
    "s_nop 11\n"
    "v_exp_f32_e32 v144, v144\n"
    "v_exp_f32_e32 v145, v145\n"
    "v_exp_f32_e32 v146, v146\n"
    "v_exp_f32_e32 v147, v147\n"
    "s_waitcnt lgkmcnt(1)\n"
    "v_mfma_f32_32x32x16_bf16 v[160:175], v[192:195], v[96:99], 0\n"
    "ds_read_b128 v[208:211], v14 offset:6656\n"
    "v_exp_f32_e32 v148, v148\n"
    "v_exp_f32_e32 v149, v149\n"
    "v_exp_f32_e32 v150, v150\n"
    "v_exp_f32_e32 v151, v151\n"
    "buffer_load_dwordx4 a[156:159], v0, s[16:19], s92 offen sc1\n"
    "s_waitcnt lgkmcnt(1)\n"
    "v_mfma_f32_32x32x16_bf16 v[160:175], v[196:199], v[100:103], v[160:175]\n"
    "ds_read_b128 v[184:187], v12 offset:13312\n"
    "ds_read_b64 v[188:189], v13 offset:13312\n"
    "v_cvt_pk_bf16_f32 v176, v144, v145\n"
    "v_cvt_pk_bf16_f32 v177, v146, v147\n"
    "v_cvt_pk_bf16_f32 v178, v148, v149\n"
    "v_cvt_pk_bf16_f32 v179, v150, v151\n"
    "buffer_load_dwordx4 a[160:163], v0, s[16:19], s92 offen offset:1024 sc1\n"
    "v_exp_f32_e32 v152, v152\n"
    "v_exp_f32_e32 v153, v153\n"
    "v_mfma_f32_32x32x16_bf16 v[112:127], v[200:203], v[176:179], v[112:127]\n"
    "v_exp_f32_e32 v154, v154\n"
    "v_exp_f32_e32 v155, v155\n"
    "v_exp_f32_e32 v156, v156\n"
    "buffer_load_dwordx4 a[164:167], v0, s[16:19], s92 offen offset:2048 sc1\n"
    "v_exp_f32_e32 v157, v157\n"
    "v_exp_f32_e32 v158, v158\n"
    "v_exp_f32_e32 v159, v159\n"
    "buffer_load_dwordx4 a[168:171], v0, s[16:19], s92 offen offset:3072 sc1\n"
    "v_cvt_pk_bf16_f32 v180, v152, v153\n"
    "v_cvt_pk_bf16_f32 v181, v154, v155\n"
    "v_cvt_pk_bf16_f32 v182, v156, v157\n"
    "v_cvt_pk_bf16_f32 v183, v158, v159\n"
    "ds_read_b128 v[212:215], v14 offset:7680\n"
    "s_nop 0\n"
    "v_mfma_f32_32x32x16_bf16 v[112:127], v[204:207], v[180:183], v[112:127]\n"
    "v_exp_f32_e32 v160, v160\n"
    "v_exp_f32_e32 v161, v161\n"
    "v_exp_f32_e32 v162, v162\n"
    "v_exp_f32_e32 v163, v163\n"
    "s_waitcnt lgkmcnt(1)\n"
    "v_mfma_f32_32x32x16_bf16 v[144:159], v[184:187], v[96:99], 0\n"
    "ds_read_b128 v[200:203], v14 offset:13312\n"
    "v_exp_f32_e32 v164, v164\n"
    "v_exp_f32_e32 v165, v165\n"
    "v_exp_f32_e32 v166, v166\n"
    "v_exp_f32_e32 v167, v167\n"
    "s_add_u32 s92, s92, 0x1000\n"
    "buffer_load_dwordx4 a[172:175], v0, s[16:19], s92 offen sc1\n"
    "v_mfma_f32_32x32x16_bf16 v[144:159], v[188:191], v[100:103], v[144:159]\n"
    "ds_read_b128 v[192:195], v12 offset:19968\n"
    "ds_read_b64 v[196:197], v13 offset:19968\n"
    "v_cvt_pk_bf16_f32 v176, v160, v161\n"
    "v_cvt_pk_bf16_f32 v177, v162, v163\n"
    "v_cvt_pk_bf16_f32 v178, v164, v165\n"
    "v_cvt_pk_bf16_f32 v179, v166, v167\n"
    "buffer_load_dwordx4 a[176:179], v0, s[16:19], s92 offen offset:1024 sc1\n"
    "v_exp_f32_e32 v168, v168\n"
    "v_exp_f32_e32 v169, v169\n"
    "v_mfma_f32_32x32x16_bf16 v[112:127], v[208:211], v[176:179], v[112:127]\n"
    "v_exp_f32_e32 v170, v170\n"
    "v_exp_f32_e32 v171, v171\n"
    "v_exp_f32_e32 v172, v172\n"
    "buffer_load_dwordx4 a[180:183], v0, s[16:19], s92 offen offset:2048 sc1\n"
    "v_exp_f32_e32 v173, v173\n"
    "v_exp_f32_e32 v174, v174\n"
    "v_exp_f32_e32 v175, v175\n"
    "buffer_load_dwordx4 a[184:187], v0, s[16:19], s92 offen offset:3072 sc1\n"
    "v_cvt_pk_bf16_f32 v180, v168, v169\n"
    "v_cvt_pk_bf16_f32 v181, v170, v171\n"
    "v_cvt_pk_bf16_f32 v182, v172, v173\n"
    "v_cvt_pk_bf16_f32 v183, v174, v175\n"
    "s_waitcnt lgkmcnt(3)\n"
    "ds_read_b128 v[204:207], v14 offset:14336\n"
    "v_mfma_f32_32x32x16_bf16 v[112:127], v[212:215], v[180:183], v[112:127]\n"
    "v_exp_f32_e32 v144, v144\n"
    "v_exp_f32_e32 v145, v145\n"
    "v_exp_f32_e32 v146, v146\n"
    "v_exp_f32_e32 v147, v147\n"
    "s_waitcnt lgkmcnt(1)\n"
    "v_mfma_f32_32x32x16_bf16 v[160:175], v[192:195], v[96:99], 0\n"
    "ds_read_b128 v[208:211], v14 offset:19968\n"
    "v_exp_f32_e32 v148, v148\n"
    "v_exp_f32_e32 v149, v149\n"
    "v_exp_f32_e32 v150, v150\n"
    "v_exp_f32_e32 v151, v151\n"
    "s_add_u32 s92, s92, 0x1000\n"
    "buffer_load_dwordx4 a[188:191], v0, s[16:19], s92 offen sc1\n"
    "v_mfma_f32_32x32x16_bf16 v[160:175], v[196:199], v[100:103], v[160:175]\n"
    "ds_read_b128 v[184:187], v12 offset:26624\n"
    "ds_read_b64 v[188:189], v13 offset:26624\n"
    "v_cvt_pk_bf16_f32 v176, v144, v145\n"
    "v_cvt_pk_bf16_f32 v177, v146, v147\n"
    "v_cvt_pk_bf16_f32 v178, v148, v149\n"
    "v_cvt_pk_bf16_f32 v179, v150, v151\n"
    "buffer_load_dwordx4 a[192:195], v0, s[16:19], s92 offen offset:1024 sc1\n"
    "v_exp_f32_e32 v152, v152\n"
    "v_exp_f32_e32 v153, v153\n"
    "v_mfma_f32_32x32x16_bf16 v[112:127], v[200:203], v[176:179], v[112:127]\n"
    "v_exp_f32_e32 v154, v154\n"
    "v_exp_f32_e32 v155, v155\n"
    "v_exp_f32_e32 v156, v156\n"
    "buffer_load_dwordx4 a[196:199], v0, s[16:19], s92 offen offset:2048 sc1\n"
    "v_exp_f32_e32 v157, v157\n"
    "v_exp_f32_e32 v158, v158\n"
    "v_exp_f32_e32 v159, v159\n"
    "buffer_load_dwordx4 a[200:203], v0, s[16:19], s92 offen offset:3072 sc1\n"
    "v_cvt_pk_bf16_f32 v180, v152, v153\n"
    "v_cvt_pk_bf16_f32 v181, v154, v155\n"
    "v_cvt_pk_bf16_f32 v182, v156, v157\n"
    "v_cvt_pk_bf16_f32 v183, v158, v159\n"
    "s_waitcnt lgkmcnt(3)\n"
    "ds_read_b128 v[212:215], v14 offset:20992\n"
    "v_mfma_f32_32x32x16_bf16 v[112:127], v[204:207], v[180:183], v[112:127]\n"
    "v_exp_f32_e32 v160, v160\n"
    "v_exp_f32_e32 v161, v161\n"
    "v_exp_f32_e32 v162, v162\n"
    "v_exp_f32_e32 v163, v163\n"
    "s_waitcnt lgkmcnt(1)\n"
    "v_mfma_f32_32x32x16_bf16 v[144:159], v[184:187], v[96:99], 0\n"
    "ds_read_b128 v[200:203], v14 offset:26624\n"
    "v_exp_f32_e32 v164, v164\n"
    "v_exp_f32_e32 v165, v165\n"
    "v_exp_f32_e32 v166, v166\n"
    "v_exp_f32_e32 v167, v167\n"
    "s_add_u32 s92, s92, 0x1000\n"
    "buffer_load_dwordx4 a[204:207], v0, s[16:19], s92 offen sc1\n"
    "v_mfma_f32_32x32x16_bf16 v[144:159], v[188:191], v[100:103], v[144:159]\n"
    "ds_read_b128 v[192:195], v12 offset:33280\n"
    "ds_read_b64 v[196:197], v13 offset:33280\n"
    "v_cvt_pk_bf16_f32 v176, v160, v161\n"
    "v_cvt_pk_bf16_f32 v177, v162, v163\n"
    "v_cvt_pk_bf16_f32 v178, v164, v165\n"
    "v_cvt_pk_bf16_f32 v179, v166, v167\n"
    "buffer_load_dwordx4 a[208:211], v0, s[16:19], s92 offen offset:1024 sc1\n"
    "v_exp_f32_e32 v168, v168\n"
    "v_exp_f32_e32 v169, v169\n"
    "v_mfma_f32_32x32x16_bf16 v[112:127], v[208:211], v[176:179], v[112:127]\n"
    "v_exp_f32_e32 v170, v170\n"
    "v_exp_f32_e32 v171, v171\n"
    "v_exp_f32_e32 v172, v172\n"
    "buffer_load_dwordx4 a[212:215], v0, s[16:19], s92 offen offset:2048 sc1\n"
    "v_exp_f32_e32 v173, v173\n"
    "v_exp_f32_e32 v174, v174\n"
    "v_exp_f32_e32 v175, v175\n"
    "buffer_load_dwordx4 a[216:219], v0, s[16:19], s92 offen offset:3072 sc1\n"
    "v_cvt_pk_bf16_f32 v180, v168, v169\n"
    "v_cvt_pk_bf16_f32 v181, v170, v171\n"
    "v_cvt_pk_bf16_f32 v182, v172, v173\n"
    "v_cvt_pk_bf16_f32 v183, v174, v175\n"
    "s_waitcnt lgkmcnt(3)\n"
    "ds_read_b128 v[204:207], v14 offset:27648\n"
    "v_mfma_f32_32x32x16_bf16 v[112:127], v[212:215], v[180:183], v[112:127]\n"
    "v_exp_f32_e32 v144, v144\n"
    "v_exp_f32_e32 v145, v145\n"
    "v_exp_f32_e32 v146, v146\n"
    "v_exp_f32_e32 v147, v147\n"
    "s_waitcnt lgkmcnt(1)\n"
    "v_mfma_f32_32x32x16_bf16 v[160:175], v[192:195], v[96:99], 0\n"
    "ds_read_b128 v[208:211], v14 offset:33280\n"
    "v_exp_f32_e32 v148, v148\n"
    "v_exp_f32_e32 v149, v149\n"
    "v_exp_f32_e32 v150, v150\n"
    "v_exp_f32_e32 v151, v151\n"
    "s_add_u32 s92, s92, 0x1000\n"
    "buffer_load_dwordx4 a[220:223], v0, s[16:19], s92 offen sc1\n"
    "v_mfma_f32_32x32x16_bf16 v[160:175], v[196:199], v[100:103], v[160:175]\n"
    "ds_read_b128 v[184:187], v12 offset:39936\n"
    "ds_read_b64 v[188:189], v13 offset:39936\n"
    "v_cvt_pk_bf16_f32 v176, v144, v145\n"
    "v_cvt_pk_bf16_f32 v177, v146, v147\n"
    "v_cvt_pk_bf16_f32 v178, v148, v149\n"
    "v_cvt_pk_bf16_f32 v179, v150, v151\n"
    "buffer_load_dwordx4 a[224:227], v0, s[16:19], s92 offen offset:1024 sc1\n"
    "v_exp_f32_e32 v152, v152\n"
    "v_exp_f32_e32 v153, v153\n"
    "v_mfma_f32_32x32x16_bf16 v[112:127], v[200:203], v[176:179], v[112:127]\n"
    "v_exp_f32_e32 v154, v154\n"
    "v_exp_f32_e32 v155, v155\n"
    "v_exp_f32_e32 v156, v156\n"
    "buffer_load_dwordx4 a[228:231], v0, s[16:19], s92 offen offset:2048 sc1\n"
    "v_exp_f32_e32 v157, v157\n"
    "v_exp_f32_e32 v158, v158\n"
    "v_exp_f32_e32 v159, v159\n"
    "buffer_load_dwordx4 a[232:235], v0, s[16:19], s92 offen offset:3072 sc1\n"
    "v_cvt_pk_bf16_f32 v180, v152, v153\n"
    "v_cvt_pk_bf16_f32 v181, v154, v155\n"
    "v_cvt_pk_bf16_f32 v182, v156, v157\n"
    "v_cvt_pk_bf16_f32 v183, v158, v159\n"
    "s_waitcnt lgkmcnt(3)\n"
    "ds_read_b128 v[212:215], v14 offset:34304\n"
    "v_mfma_f32_32x32x16_bf16 v[112:127], v[204:207], v[180:183], v[112:127]\n"
    "v_exp_f32_e32 v160, v160\n"
    "v_exp_f32_e32 v161, v161\n"
    "v_exp_f32_e32 v162, v162\n"
    "v_exp_f32_e32 v163, v163\n"
    "s_waitcnt lgkmcnt(1)\n"
    "v_mfma_f32_32x32x16_bf16 v[144:159], v[184:187], v[96:99], 0\n"
    "ds_read_b128 v[200:203], v14 offset:39936\n"
    "v_exp_f32_e32 v164, v164\n"
    "v_exp_f32_e32 v165, v165\n"
    "v_exp_f32_e32 v166, v166\n"
    "v_exp_f32_e32 v167, v167\n"
    "s_add_u32 s92, s92, 0x1000\n"
    "buffer_load_dwordx4 a[236:239], v0, s[16:19], s92 offen sc1\n"
    "v_mfma_f32_32x32x16_bf16 v[144:159], v[188:191], v[100:103], v[144:159]\n"
    "ds_read_b128 v[192:195], v12 offset:46592\n"
    "ds_read_b64 v[196:197], v13 offset:46592\n"
    "v_cvt_pk_bf16_f32 v176, v160, v161\n"
    "v_cvt_pk_bf16_f32 v177, v162, v163\n"
    "v_cvt_pk_bf16_f32 v178, v164, v165\n"
    "v_cvt_pk_bf16_f32 v179, v166, v167\n"
    "buffer_load_dwordx4 a[240:243], v0, s[16:19], s92 offen offset:1024 sc1\n"
    "v_exp_f32_e32 v168, v168\n"
    "v_exp_f32_e32 v169, v169\n"
    "v_mfma_f32_32x32x16_bf16 v[112:127], v[208:211], v[176:179], v[112:127]\n"
    "v_exp_f32_e32 v170, v170\n"
    "v_exp_f32_e32 v171, v171\n"
    "v_exp_f32_e32 v172, v172\n"
    "buffer_load_dwordx4 a[244:247], v0, s[16:19], s92 offen offset:2048 sc1\n"
    "v_exp_f32_e32 v173, v173\n"
    "v_exp_f32_e32 v174, v174\n"
    "v_exp_f32_e32 v175, v175\n"
    "buffer_load_dwordx4 a[248:251], v0, s[16:19], s92 offen offset:3072 sc1\n"
    "v_cvt_pk_bf16_f32 v180, v168, v169\n"
    "v_cvt_pk_bf16_f32 v181, v170, v171\n"
    "v_cvt_pk_bf16_f32 v182, v172, v173\n"
    "v_cvt_pk_bf16_f32 v183, v174, v175\n"
    "s_waitcnt lgkmcnt(3)\n"
    "ds_read_b128 v[204:207], v14 offset:40960\n"
    "v_mfma_f32_32x32x16_bf16 v[112:127], v[212:215], v[180:183], v[112:127]\n"
    "v_exp_f32_e32 v144, v144\n"
    "v_exp_f32_e32 v145, v145\n"
    "v_exp_f32_e32 v146, v146\n"
    "v_exp_f32_e32 v147, v147\n"
    "s_waitcnt lgkmcnt(1)\n"
    "v_mfma_f32_32x32x16_bf16 v[160:175], v[192:195], v[96:99], 0\n"
    "ds_read_b128 v[208:211], v14 offset:46592\n"
    "v_exp_f32_e32 v148, v148\n"
    "v_exp_f32_e32 v149, v149\n"
    "v_exp_f32_e32 v150, v150\n"
    "v_exp_f32_e32 v151, v151\n"
    "s_add_u32 s92, s92, 0x1000\n"
    "buffer_load_dwordx4 a[252:255], v0, s[16:19], s92 offen sc1\n"
    "v_mfma_f32_32x32x16_bf16 v[160:175], v[196:199], v[100:103], v[160:175]\n"
    "ds_read_b128 v[184:187], v12 offset:19968\n"
    "ds_read_b64 v[188:189], v13 offset:19968\n"
    "v_cvt_pk_bf16_f32 v176, v144, v145\n"
    "v_cvt_pk_bf16_f32 v177, v146, v147\n"
    "v_cvt_pk_bf16_f32 v178, v148, v149\n"
    "v_cvt_pk_bf16_f32 v179, v150, v151\n"
    "buffer_load_dwordx4 a[60:63], v0, s[16:19], s93 offen sc1\n"
    "v_exp_f32_e32 v152, v152\n"
    "v_exp_f32_e32 v153, v153\n"
    "v_mfma_f32_32x32x16_bf16 v[112:127], v[200:203], v[176:179], v[112:127]\n"
    "v_exp_f32_e32 v154, v154\n"
    "v_exp_f32_e32 v155, v155\n"
    "v_exp_f32_e32 v156, v156\n"
    "buffer_load_dwordx4 a[64:67], v0, s[16:19], s93 offen offset:1024 sc1\n"
    "v_exp_f32_e32 v157, v157\n"
    "v_exp_f32_e32 v158, v158\n"
    "v_exp_f32_e32 v159, v159\n"
    "buffer_load_dwordx4 a[68:71], v0, s[16:19], s93 offen offset:2048 sc1\n"
    "v_cvt_pk_bf16_f32 v180, v152, v153\n"
    "v_cvt_pk_bf16_f32 v181, v154, v155\n"
    "v_cvt_pk_bf16_f32 v182, v156, v157\n"
    "v_cvt_pk_bf16_f32 v183, v158, v159\n"
    "s_waitcnt lgkmcnt(3)\n"
    "ds_read_b128 v[212:215], v14 offset:47616\n"
    "v_mfma_f32_32x32x16_bf16 v[112:127], v[204:207], v[180:183], v[112:127]\n"
    "v_exp_f32_e32 v160, v160\n"
    "v_exp_f32_e32 v161, v161\n"
    "v_exp_f32_e32 v162, v162\n"
    "v_exp_f32_e32 v163, v163\n"
    "s_waitcnt lgkmcnt(1)\n"
    "v_mfma_f32_32x32x16_bf16 v[144:159], v[184:187], v[104:107], v[232:247]\n"
    "ds_read_b128 v[200:203], v14 offset:19968\n"
    "v_exp_f32_e32 v164, v164\n"
    "v_exp_f32_e32 v165, v165\n"
    "v_exp_f32_e32 v166, v166\n"
    "v_exp_f32_e32 v167, v167\n"
    "buffer_load_dwordx4 a[72:75], v0, s[16:19], s93 offen offset:3072 sc1\n"
    "v_mfma_f32_32x32x16_bf16 v[144:159], v[188:191], v[108:111], v[144:159]\n"
    "ds_read_b128 v[192:195], v12 offset:26624\n"
    "ds_read_b64 v[196:197], v13 offset:26624\n"
    "v_cvt_pk_bf16_f32 v176, v160, v161\n"
    "v_cvt_pk_bf16_f32 v177, v162, v163\n"
    "v_cvt_pk_bf16_f32 v178, v164, v165\n"
    "v_cvt_pk_bf16_f32 v179, v166, v167\n"
    "s_add_u32 s93, s93, 0x1000\n"
    "buffer_load_dwordx4 a[76:79], v0, s[16:19], s93 offen sc1\n"
    "v_exp_f32_e32 v168, v168\n"
    "v_exp_f32_e32 v169, v169\n"
    "v_mfma_f32_32x32x16_bf16 v[112:127], v[208:211], v[176:179], v[112:127]\n"
    "v_exp_f32_e32 v170, v170\n"
    "v_exp_f32_e32 v171, v171\n"
    "v_exp_f32_e32 v172, v172\n"
    "buffer_load_dwordx4 a[80:83], v0, s[16:19], s93 offen offset:1024 sc1\n"
    "v_exp_f32_e32 v173, v173\n"
    "v_exp_f32_e32 v174, v174\n"
    "v_exp_f32_e32 v175, v175\n"
    "buffer_load_dwordx4 a[84:87], v0, s[16:19], s93 offen offset:2048 sc1\n"
    "v_cvt_pk_bf16_f32 v180, v168, v169\n"
    "v_cvt_pk_bf16_f32 v181, v170, v171\n"
    "v_cvt_pk_bf16_f32 v182, v172, v173\n"
    "v_cvt_pk_bf16_f32 v183, v174, v175\n"
    "s_waitcnt lgkmcnt(3)\n"
    "ds_read_b128 v[204:207], v14 offset:20992\n"
    "v_mfma_f32_32x32x16_bf16 v[112:127], v[212:215], v[180:183], v[112:127]\n"
    "v_exp_f32_e32 v144, v144\n"
    "v_exp_f32_e32 v145, v145\n"
    "v_exp_f32_e32 v146, v146\n"
    "v_exp_f32_e32 v147, v147\n"
    "s_waitcnt lgkmcnt(1)\n"
    "v_mfma_f32_32x32x16_bf16 v[160:175], v[192:195], v[104:107], 0\n"
    "ds_read_b128 v[208:211], v14 offset:26624\n"
    "v_exp_f32_e32 v148, v148\n"
    "v_exp_f32_e32 v149, v149\n"
    "v_exp_f32_e32 v150, v150\n"
    "v_exp_f32_e32 v151, v151\n"
    "buffer_load_dwordx4 a[88:91], v0, s[16:19], s93 offen offset:3072 sc1\n"
    "v_mfma_f32_32x32x16_bf16 v[160:175], v[196:199], v[108:111], v[160:175]\n"
    "ds_read_b128 v[184:187], v12 offset:33280\n"
    "ds_read_b64 v[188:189], v13 offset:33280\n"
    "v_cvt_pk_bf16_f32 v176, v144, v145\n"
    "v_cvt_pk_bf16_f32 v177, v146, v147\n"
    "v_cvt_pk_bf16_f32 v178, v148, v149\n"
    "v_cvt_pk_bf16_f32 v179, v150, v151\n"
    "s_add_u32 s93, s93, 0x1000\n"
    "buffer_load_dwordx4 a[92:95], v0, s[16:19], s93 offen sc1\n"
    "v_exp_f32_e32 v152, v152\n"
    "v_exp_f32_e32 v153, v153\n"
    "v_mfma_f32_32x32x16_bf16 v[128:143], v[200:203], v[176:179], v[128:143]\n"
    "v_exp_f32_e32 v154, v154\n"
    "v_exp_f32_e32 v155, v155\n"
    "v_exp_f32_e32 v156, v156\n"
    "buffer_load_dwordx4 a[96:99], v0, s[16:19], s93 offen offset:1024 sc1\n"
    "v_exp_f32_e32 v157, v157\n"
    "v_exp_f32_e32 v158, v158\n"
    "v_exp_f32_e32 v159, v159\n"
    "buffer_load_dwordx4 a[100:103], v0, s[16:19], s93 offen offset:2048 sc1\n"
    "v_cvt_pk_bf16_f32 v180, v152, v153\n"
    "v_cvt_pk_bf16_f32 v181, v154, v155\n"
    "v_cvt_pk_bf16_f32 v182, v156, v157\n"
    "v_cvt_pk_bf16_f32 v183, v158, v159\n"
    "s_waitcnt lgkmcnt(3)\n"
    "ds_read_b128 v[212:215], v14 offset:27648\n"
    "v_mfma_f32_32x32x16_bf16 v[128:143], v[204:207], v[180:183], v[128:143]\n"
    "v_exp_f32_e32 v160, v160\n"
    "v_exp_f32_e32 v161, v161\n"
    "v_exp_f32_e32 v162, v162\n"
    "v_exp_f32_e32 v163, v163\n"
    "s_waitcnt lgkmcnt(1)\n"
    "v_mfma_f32_32x32x16_bf16 v[144:159], v[184:187], v[104:107], 0\n"
    "ds_read_b128 v[200:203], v14 offset:33280\n"
    "v_exp_f32_e32 v164, v164\n"
    "v_exp_f32_e32 v165, v165\n"
    "v_exp_f32_e32 v166, v166\n"
    "v_exp_f32_e32 v167, v167\n"
    "buffer_load_dwordx4 a[104:107], v0, s[16:19], s93 offen offset:3072 sc1\n"
    "v_mfma_f32_32x32x16_bf16 v[144:159], v[188:191], v[108:111], v[144:159]\n"
    "ds_read_b128 v[192:195], v12 offset:39936\n"
    "ds_read_b64 v[196:197], v13 offset:39936\n"
    "v_cvt_pk_bf16_f32 v176, v160, v161\n"
    "v_cvt_pk_bf16_f32 v177, v162, v163\n"
    "v_cvt_pk_bf16_f32 v178, v164, v165\n"
    "v_cvt_pk_bf16_f32 v179, v166, v167\n"
    "s_add_u32 s93, s93, 0x1000\n"
    "buffer_load_dwordx4 a[108:111], v0, s[16:19], s93 offen sc1\n"
    "v_exp_f32_e32 v168, v168\n"
    "v_exp_f32_e32 v169, v169\n"
    "v_mfma_f32_32x32x16_bf16 v[128:143], v[208:211], v[176:179], v[128:143]\n"
    "v_exp_f32_e32 v170, v170\n"
    "v_exp_f32_e32 v171, v171\n"
    "v_exp_f32_e32 v172, v172\n"
    "buffer_load_dwordx4 a[112:115], v0, s[16:19], s93 offen offset:1024 sc1\n"
    "v_exp_f32_e32 v173, v173\n"
    "v_exp_f32_e32 v174, v174\n"
    "v_exp_f32_e32 v175, v175\n"
    "buffer_load_dwordx4 a[116:119], v0, s[16:19], s93 offen offset:2048 sc1\n"
    "v_cvt_pk_bf16_f32 v180, v168, v169\n"
    "v_cvt_pk_bf16_f32 v181, v170, v171\n"
    "v_cvt_pk_bf16_f32 v182, v172, v173\n"
    "v_cvt_pk_bf16_f32 v183, v174, v175\n"
    "s_waitcnt lgkmcnt(3)\n"
    "ds_read_b128 v[204:207], v14 offset:34304\n"
    "v_mfma_f32_32x32x16_bf16 v[128:143], v[212:215], v[180:183], v[128:143]\n"
    "v_exp_f32_e32 v144, v144\n"
    "v_exp_f32_e32 v145, v145\n"
    "v_exp_f32_e32 v146, v146\n"
    "v_exp_f32_e32 v147, v147\n"
    "s_waitcnt lgkmcnt(1)\n"
    "v_mfma_f32_32x32x16_bf16 v[160:175], v[192:195], v[104:107], 0\n"
    "ds_read_b128 v[208:211], v14 offset:39936\n"
    "v_exp_f32_e32 v148, v148\n"
    "v_exp_f32_e32 v149, v149\n"
    "v_exp_f32_e32 v150, v150\n"
    "v_exp_f32_e32 v151, v151\n"
    "buffer_load_dwordx4 a[120:123], v0, s[16:19], s93 offen offset:3072 sc1\n"
    "v_mfma_f32_32x32x16_bf16 v[160:175], v[196:199], v[108:111], v[160:175]\n"
    "ds_read_b128 v[184:187], v12 offset:46592\n"
    "ds_read_b64 v[188:189], v13 offset:46592\n"
    "v_cvt_pk_bf16_f32 v176, v144, v145\n"
    "v_cvt_pk_bf16_f32 v177, v146, v147\n"
    "v_cvt_pk_bf16_f32 v178, v148, v149\n"
    "v_cvt_pk_bf16_f32 v179, v150, v151\n"
    "s_add_u32 s93, s93, 0x1000\n"
    "buffer_load_dwordx4 a[124:127], v0, s[16:19], s93 offen sc1\n"
    "v_exp_f32_e32 v152, v152\n"
    "v_exp_f32_e32 v153, v153\n"
    "v_mfma_f32_32x32x16_bf16 v[128:143], v[200:203], v[176:179], v[128:143]\n"
    "v_exp_f32_e32 v154, v154\n"
    "v_exp_f32_e32 v155, v155\n"
    "v_exp_f32_e32 v156, v156\n"
    "buffer_load_dwordx4 a[128:131], v0, s[16:19], s93 offen offset:1024 sc1\n"
    "v_exp_f32_e32 v157, v157\n"
    "v_exp_f32_e32 v158, v158\n"
    "v_exp_f32_e32 v159, v159\n"
    "buffer_load_dwordx4 a[132:135], v0, s[16:19], s93 offen offset:2048 sc1\n"
    "v_cvt_pk_bf16_f32 v180, v152, v153\n"
    "v_cvt_pk_bf16_f32 v181, v154, v155\n"
    "v_cvt_pk_bf16_f32 v182, v156, v157\n"
    "v_cvt_pk_bf16_f32 v183, v158, v159\n"
    "s_waitcnt lgkmcnt(3)\n"
    "ds_read_b128 v[212:215], v14 offset:40960\n"
    "v_mfma_f32_32x32x16_bf16 v[128:143], v[204:207], v[180:183], v[128:143]\n"
    "v_exp_f32_e32 v160, v160\n"
    "v_exp_f32_e32 v161, v161\n"
    "v_exp_f32_e32 v162, v162\n"
    "v_exp_f32_e32 v163, v163\n"
    "s_waitcnt lgkmcnt(1)\n"
    "v_mfma_f32_32x32x16_bf16 v[144:159], v[184:187], v[104:107], 0\n"
    "ds_read_b128 v[200:203], v14 offset:46592\n"
    "v_exp_f32_e32 v164, v164\n"
    "v_exp_f32_e32 v165, v165\n"
    "v_exp_f32_e32 v166, v166\n"
    "v_exp_f32_e32 v167, v167\n"
    "buffer_load_dwordx4 a[136:139], v0, s[16:19], s93 offen offset:3072 sc1\n"
    "v_mfma_f32_32x32x16_bf16 v[144:159], v[188:191], v[108:111], v[144:159]\n"
    "v_cvt_pk_bf16_f32 v176, v160, v161\n"
    "v_cvt_pk_bf16_f32 v177, v162, v163\n"
    "v_cvt_pk_bf16_f32 v178, v164, v165\n"
    "v_cvt_pk_bf16_f32 v179, v166, v167\n"
    "s_add_u32 s93, s93, 0x1000\n"
    "buffer_load_dwordx4 a[140:143], v0, s[16:19], s93 offen sc1\n"
    "v_exp_f32_e32 v168, v168\n"
    "v_exp_f32_e32 v169, v169\n"
    "v_mfma_f32_32x32x16_bf16 v[128:143], v[208:211], v[176:179], v[128:143]\n"
    "v_exp_f32_e32 v170, v170\n"
    "v_exp_f32_e32 v171, v171\n"
    "v_exp_f32_e32 v172, v172\n"
    "buffer_load_dwordx4 a[144:147], v0, s[16:19], s93 offen offset:1024 sc1\n"
    "v_exp_f32_e32 v173, v173\n"
    "v_exp_f32_e32 v174, v174\n"
    "v_exp_f32_e32 v175, v175\n"
    "buffer_load_dwordx4 a[148:151], v0, s[16:19], s93 offen offset:2048 sc1\n"
    "v_cvt_pk_bf16_f32 v180, v168, v169\n"
    "v_cvt_pk_bf16_f32 v181, v170, v171\n"
    "v_cvt_pk_bf16_f32 v182, v172, v173\n"
    "v_cvt_pk_bf16_f32 v183, v174, v175\n"
    "s_waitcnt lgkmcnt(1)\n"
    "ds_read_b128 v[204:207], v14 offset:47616\n"
    "v_mfma_f32_32x32x16_bf16 v[128:143], v[212:215], v[180:183], v[128:143]\n"
    "v_exp_f32_e32 v144, v144\n"
    "v_exp_f32_e32 v145, v145\n"
    "v_exp_f32_e32 v146, v146\n"
    "v_exp_f32_e32 v147, v147\n"
    "v_exp_f32_e32 v148, v148\n"
    "v_exp_f32_e32 v149, v149\n"
    "v_exp_f32_e32 v150, v150\n"
    "v_exp_f32_e32 v151, v151\n"
    "buffer_load_dwordx4 a[152:155], v0, s[16:19], s93 offen offset:3072 sc1\n"
    "v_cvt_pk_bf16_f32 v176, v144, v145\n"
    "v_cvt_pk_bf16_f32 v177, v146, v147\n"
    "v_cvt_pk_bf16_f32 v178, v148, v149\n"
    "v_cvt_pk_bf16_f32 v179, v150, v151\n"
    "v_exp_f32_e32 v152, v152\n"
    "v_exp_f32_e32 v153, v153\n"
    "s_waitcnt lgkmcnt(1)\n"
    "v_mfma_f32_32x32x16_bf16 v[128:143], v[200:203], v[176:179], v[128:143]\n"
    "v_exp_f32_e32 v154, v154\n"
    "v_exp_f32_e32 v155, v155\n"
    "v_exp_f32_e32 v156, v156\n"
    "v_exp_f32_e32 v157, v157\n"
    "v_exp_f32_e32 v158, v158\n"
    "v_exp_f32_e32 v159, v159\n"
    "v_cvt_pk_bf16_f32 v180, v152, v153\n"
    "v_cvt_pk_bf16_f32 v181, v154, v155\n"
    "v_cvt_pk_bf16_f32 v182, v156, v157\n"
    "v_cvt_pk_bf16_f32 v183, v158, v159\n"
    "s_waitcnt lgkmcnt(0)\n"
    "s_nop 0\n"
    "v_mfma_f32_32x32x16_bf16 v[128:143], v[204:207], v[180:183], v[128:143]\n"
    "s_branch .Lmk_haloj_45\n"
    ".Lmk_hpr1_44:\n"
    "ds_read_b128 v[184:187], v12 offset:6656\n"
    "ds_read_b64 v[188:189], v13 offset:6656\n"
    "ds_read_b128 v[200:203], v14 offset:6656\n"
    "ds_read_b128 v[204:207], v14 offset:7680\n"
    "ds_read_b128 v[192:195], v12 offset:13312\n"
    "ds_read_b64 v[196:197], v13 offset:13312\n"
    "s_waitcnt lgkmcnt(5)\n"
    "v_mfma_f32_32x32x16_bf16 v[144:159], v[184:187], v[96:99], v[232:247]\n"
    "s_waitcnt lgkmcnt(4)\n"
    "v_mfma_f32_32x32x16_bf16 v[144:159], v[188:191], v[100:103], v[144:159]\n"
    "s_nop 11\n"
    "v_exp_f32_e32 v144, v144\n"
    "v_exp_f32_e32 v145, v145\n"
    "v_exp_f32_e32 v146, v146\n"
    "v_exp_f32_e32 v147, v147\n"
    "s_waitcnt lgkmcnt(1)\n"
    "v_mfma_f32_32x32x16_bf16 v[160:175], v[192:195], v[96:99], 0\n"
    "ds_read_b128 v[208:211], v14 offset:13312\n"
    "v_exp_f32_e32 v148, v148\n"
    "v_exp_f32_e32 v149, v149\n"
    "v_exp_f32_e32 v150, v150\n"
    "v_exp_f32_e32 v151, v151\n"
    "buffer_load_dwordx4 a[156:159], v0, s[16:19], s92 offen sc1\n"
    "s_waitcnt lgkmcnt(1)\n"
    "v_mfma_f32_32x32x16_bf16 v[160:175], v[196:199], v[100:103], v[160:175]\n"
    "ds_read_b128 v[184:187], v12 offset:19968\n"
    "ds_read_b64 v[188:189], v13 offset:19968\n"
    "v_cvt_pk_bf16_f32 v176, v144, v145\n"
    "v_cvt_pk_bf16_f32 v177, v146, v147\n"
    "v_cvt_pk_bf16_f32 v178, v148, v149\n"
    "v_cvt_pk_bf16_f32 v179, v150, v151\n"
    "buffer_load_dwordx4 a[160:163], v0, s[16:19], s92 offen offset:1024 sc1\n"
    "v_exp_f32_e32 v152, v152\n"
    "v_exp_f32_e32 v153, v153\n"
    "v_mfma_f32_32x32x16_bf16 v[112:127], v[200:203], v[176:179], v[112:127]\n"
    "v_exp_f32_e32 v154, v154\n"
    "v_exp_f32_e32 v155, v155\n"
    "v_exp_f32_e32 v156, v156\n"
    "buffer_load_dwordx4 a[164:167], v0, s[16:19], s92 offen offset:2048 sc1\n"
    "v_exp_f32_e32 v157, v157\n"
    "v_exp_f32_e32 v158, v158\n"
    "v_exp_f32_e32 v159, v159\n"
    "buffer_load_dwordx4 a[168:171], v0, s[16:19], s92 offen offset:3072 sc1\n"
    "v_cvt_pk_bf16_f32 v180, v152, v153\n"
    "v_cvt_pk_bf16_f32 v181, v154, v155\n"
    "v_cvt_pk_bf16_f32 v182, v156, v157\n"
    "v_cvt_pk_bf16_f32 v183, v158, v159\n"
    "ds_read_b128 v[212:215], v14 offset:14336\n"
    "s_nop 0\n"
    "v_mfma_f32_32x32x16_bf16 v[112:127], v[204:207], v[180:183], v[112:127]\n"
    "v_exp_f32_e32 v160, v160\n"
    "v_exp_f32_e32 v161, v161\n"
    "v_exp_f32_e32 v162, v162\n"
    "v_exp_f32_e32 v163, v163\n"
    "s_waitcnt lgkmcnt(1)\n"
    "v_mfma_f32_32x32x16_bf16 v[144:159], v[184:187], v[96:99], 0\n"
    "ds_read_b128 v[200:203], v14 offset:19968\n"
    "v_exp_f32_e32 v164, v164\n"
    "v_exp_f32_e32 v165, v165\n"
    "v_exp_f32_e32 v166, v166\n"
    "v_exp_f32_e32 v167, v167\n"
    "s_add_u32 s92, s92, 0x1000\n"
    "buffer_load_dwordx4 a[172:175], v0, s[16:19], s92 offen sc1\n"
    "v_mfma_f32_32x32x16_bf16 v[144:159], v[188:191], v[100:103], v[144:159]\n"
    "ds_read_b128 v[192:195], v12 offset:26624\n"
    "ds_read_b64 v[196:197], v13 offset:26624\n"
    "v_cvt_pk_bf16_f32 v176, v160, v161\n"
    "v_cvt_pk_bf16_f32 v177, v162, v163\n"
    "v_cvt_pk_bf16_f32 v178, v164, v165\n"
    "v_cvt_pk_bf16_f32 v179, v166, v167\n"
    "buffer_load_dwordx4 a[176:179], v0, s[16:19], s92 offen offset:1024 sc1\n"
    "v_exp_f32_e32 v168, v168\n"
    "v_exp_f32_e32 v169, v169\n"
    "v_mfma_f32_32x32x16_bf16 v[112:127], v[208:211], v[176:179], v[112:127]\n"
    "v_exp_f32_e32 v170, v170\n"
    "v_exp_f32_e32 v171, v171\n"
    "v_exp_f32_e32 v172, v172\n"
    "buffer_load_dwordx4 a[180:183], v0, s[16:19], s92 offen offset:2048 sc1\n"
    "v_exp_f32_e32 v173, v173\n"
    "v_exp_f32_e32 v174, v174\n"
    "v_exp_f32_e32 v175, v175\n"
    "buffer_load_dwordx4 a[184:187], v0, s[16:19], s92 offen offset:3072 sc1\n"
    "v_cvt_pk_bf16_f32 v180, v168, v169\n"
    "v_cvt_pk_bf16_f32 v181, v170, v171\n"
    "v_cvt_pk_bf16_f32 v182, v172, v173\n"
    "v_cvt_pk_bf16_f32 v183, v174, v175\n"
    "s_waitcnt lgkmcnt(3)\n"
    "ds_read_b128 v[204:207], v14 offset:20992\n"
    "v_mfma_f32_32x32x16_bf16 v[112:127], v[212:215], v[180:183], v[112:127]\n"
    "v_exp_f32_e32 v144, v144\n"
    "v_exp_f32_e32 v145, v145\n"
    "v_exp_f32_e32 v146, v146\n"
    "v_exp_f32_e32 v147, v147\n"
    "s_waitcnt lgkmcnt(1)\n"
    "v_mfma_f32_32x32x16_bf16 v[160:175], v[192:195], v[96:99], 0\n"
    "ds_read_b128 v[208:211], v14 offset:26624\n"
    "v_exp_f32_e32 v148, v148\n"
    "v_exp_f32_e32 v149, v149\n"
    "v_exp_f32_e32 v150, v150\n"
    "v_exp_f32_e32 v151, v151\n"
    "s_add_u32 s92, s92, 0x1000\n"
    "buffer_load_dwordx4 a[188:191], v0, s[16:19], s92 offen sc1\n"
    "v_mfma_f32_32x32x16_bf16 v[160:175], v[196:199], v[100:103], v[160:175]\n"
    "ds_read_b128 v[184:187], v12 offset:33280\n"
    "ds_read_b64 v[188:189], v13 offset:33280\n"
    "v_cvt_pk_bf16_f32 v176, v144, v145\n"
    "v_cvt_pk_bf16_f32 v177, v146, v147\n"
    "v_cvt_pk_bf16_f32 v178, v148, v149\n"
    "v_cvt_pk_bf16_f32 v179, v150, v151\n"
    "buffer_load_dwordx4 a[192:195], v0, s[16:19], s92 offen offset:1024 sc1\n"
    "v_exp_f32_e32 v152, v152\n"
    "v_exp_f32_e32 v153, v153\n"
    "v_mfma_f32_32x32x16_bf16 v[112:127], v[200:203], v[176:179], v[112:127]\n"
    "v_exp_f32_e32 v154, v154\n"
    "v_exp_f32_e32 v155, v155\n"
    "v_exp_f32_e32 v156, v156\n"
    "buffer_load_dwordx4 a[196:199], v0, s[16:19], s92 offen offset:2048 sc1\n"
    "v_exp_f32_e32 v157, v157\n"
    "v_exp_f32_e32 v158, v158\n"
    "v_exp_f32_e32 v159, v159\n"
    "buffer_load_dwordx4 a[200:203], v0, s[16:19], s92 offen offset:3072 sc1\n"
    "v_cvt_pk_bf16_f32 v180, v152, v153\n"
    "v_cvt_pk_bf16_f32 v181, v154, v155\n"
    "v_cvt_pk_bf16_f32 v182, v156, v157\n"
    "v_cvt_pk_bf16_f32 v183, v158, v159\n"
    "s_waitcnt lgkmcnt(3)\n"
    "ds_read_b128 v[212:215], v14 offset:27648\n"
    "v_mfma_f32_32x32x16_bf16 v[112:127], v[204:207], v[180:183], v[112:127]\n"
    "v_exp_f32_e32 v160, v160\n"
    "v_exp_f32_e32 v161, v161\n"
    "v_exp_f32_e32 v162, v162\n"
    "v_exp_f32_e32 v163, v163\n"
    "s_waitcnt lgkmcnt(1)\n"
    "v_mfma_f32_32x32x16_bf16 v[144:159], v[184:187], v[96:99], 0\n"
    "ds_read_b128 v[200:203], v14 offset:33280\n"
    "v_exp_f32_e32 v164, v164\n"
    "v_exp_f32_e32 v165, v165\n"
    "v_exp_f32_e32 v166, v166\n"
    "v_exp_f32_e32 v167, v167\n"
    "s_add_u32 s92, s92, 0x1000\n"
    "buffer_load_dwordx4 a[204:207], v0, s[16:19], s92 offen sc1\n"
    "v_mfma_f32_32x32x16_bf16 v[144:159], v[188:191], v[100:103], v[144:159]\n"
    "ds_read_b128 v[192:195], v12 offset:39936\n"
    "ds_read_b64 v[196:197], v13 offset:39936\n"
    "v_cvt_pk_bf16_f32 v176, v160, v161\n"
    "v_cvt_pk_bf16_f32 v177, v162, v163\n"
    "v_cvt_pk_bf16_f32 v178, v164, v165\n"
    "v_cvt_pk_bf16_f32 v179, v166, v167\n"
    "buffer_load_dwordx4 a[208:211], v0, s[16:19], s92 offen offset:1024 sc1\n"
    "v_exp_f32_e32 v168, v168\n"
    "v_exp_f32_e32 v169, v169\n"
    "v_mfma_f32_32x32x16_bf16 v[112:127], v[208:211], v[176:179], v[112:127]\n"
    "v_exp_f32_e32 v170, v170\n"
    "v_exp_f32_e32 v171, v171\n"
    "v_exp_f32_e32 v172, v172\n"
    "buffer_load_dwordx4 a[212:215], v0, s[16:19], s92 offen offset:2048 sc1\n"
    "v_exp_f32_e32 v173, v173\n"
    "v_exp_f32_e32 v174, v174\n"
    "v_exp_f32_e32 v175, v175\n"
    "buffer_load_dwordx4 a[216:219], v0, s[16:19], s92 offen offset:3072 sc1\n"
    "v_cvt_pk_bf16_f32 v180, v168, v169\n"
    "v_cvt_pk_bf16_f32 v181, v170, v171\n"
    "v_cvt_pk_bf16_f32 v182, v172, v173\n"
    "v_cvt_pk_bf16_f32 v183, v174, v175\n"
    "s_waitcnt lgkmcnt(3)\n"
    "ds_read_b128 v[204:207], v14 offset:34304\n"
    "v_mfma_f32_32x32x16_bf16 v[112:127], v[212:215], v[180:183], v[112:127]\n"
    "v_exp_f32_e32 v144, v144\n"
    "v_exp_f32_e32 v145, v145\n"
    "v_exp_f32_e32 v146, v146\n"
    "v_exp_f32_e32 v147, v147\n"
    "s_waitcnt lgkmcnt(1)\n"
    "v_mfma_f32_32x32x16_bf16 v[160:175], v[192:195], v[96:99], 0\n"
    "ds_read_b128 v[208:211], v14 offset:39936\n"
    "v_exp_f32_e32 v148, v148\n"
    "v_exp_f32_e32 v149, v149\n"
    "v_exp_f32_e32 v150, v150\n"
    "v_exp_f32_e32 v151, v151\n"
    "s_add_u32 s92, s92, 0x1000\n"
    "buffer_load_dwordx4 a[220:223], v0, s[16:19], s92 offen sc1\n"
    "v_mfma_f32_32x32x16_bf16 v[160:175], v[196:199], v[100:103], v[160:175]\n"
    "ds_read_b128 v[184:187], v12 offset:46592\n"
    "ds_read_b64 v[188:189], v13 offset:46592\n"
    "v_cvt_pk_bf16_f32 v176, v144, v145\n"
    "v_cvt_pk_bf16_f32 v177, v146, v147\n"
    "v_cvt_pk_bf16_f32 v178, v148, v149\n"
    "v_cvt_pk_bf16_f32 v179, v150, v151\n"
    "buffer_load_dwordx4 a[224:227], v0, s[16:19], s92 offen offset:1024 sc1\n"
    "v_exp_f32_e32 v152, v152\n"
    "v_exp_f32_e32 v153, v153\n"
    "v_mfma_f32_32x32x16_bf16 v[112:127], v[200:203], v[176:179], v[112:127]\n"
    "v_exp_f32_e32 v154, v154\n"
    "v_exp_f32_e32 v155, v155\n"
    "v_exp_f32_e32 v156, v156\n"
    "buffer_load_dwordx4 a[228:231], v0, s[16:19], s92 offen offset:2048 sc1\n"
    "v_exp_f32_e32 v157, v157\n"
    "v_exp_f32_e32 v158, v158\n"
    "v_exp_f32_e32 v159, v159\n"
    "buffer_load_dwordx4 a[232:235], v0, s[16:19], s92 offen offset:3072 sc1\n"
    "v_cvt_pk_bf16_f32 v180, v152, v153\n"
    "v_cvt_pk_bf16_f32 v181, v154, v155\n"
    "v_cvt_pk_bf16_f32 v182, v156, v157\n"
    "v_cvt_pk_bf16_f32 v183, v158, v159\n"
    "s_waitcnt lgkmcnt(3)\n"
    "ds_read_b128 v[212:215], v14 offset:40960\n"
    "v_mfma_f32_32x32x16_bf16 v[112:127], v[204:207], v[180:183], v[112:127]\n"
    "v_exp_f32_e32 v160, v160\n"
    "v_exp_f32_e32 v161, v161\n"
    "v_exp_f32_e32 v162, v162\n"
    "v_exp_f32_e32 v163, v163\n"
    "s_waitcnt lgkmcnt(1)\n"
    "v_mfma_f32_32x32x16_bf16 v[144:159], v[184:187], v[96:99], 0\n"
    "ds_read_b128 v[200:203], v14 offset:46592\n"
    "v_exp_f32_e32 v164, v164\n"
    "v_exp_f32_e32 v165, v165\n"
    "v_exp_f32_e32 v166, v166\n"
    "v_exp_f32_e32 v167, v167\n"
    "s_add_u32 s92, s92, 0x1000\n"
    "buffer_load_dwordx4 a[236:239], v0, s[16:19], s92 offen sc1\n"
    "v_mfma_f32_32x32x16_bf16 v[144:159], v[188:191], v[100:103], v[144:159]\n"
    "ds_read_b128 v[192:195], v12 offset:13312\n"
    "ds_read_b64 v[196:197], v13 offset:13312\n"
    "v_cvt_pk_bf16_f32 v176, v160, v161\n"
    "v_cvt_pk_bf16_f32 v177, v162, v163\n"
    "v_cvt_pk_bf16_f32 v178, v164, v165\n"
    "v_cvt_pk_bf16_f32 v179, v166, v167\n"
    "buffer_load_dwordx4 a[240:243], v0, s[16:19], s92 offen offset:1024 sc1\n"
    "v_exp_f32_e32 v168, v168\n"
    "v_exp_f32_e32 v169, v169\n"
    "v_mfma_f32_32x32x16_bf16 v[112:127], v[208:211], v[176:179], v[112:127]\n"
    "v_exp_f32_e32 v170, v170\n"
    "v_exp_f32_e32 v171, v171\n"
    "v_exp_f32_e32 v172, v172\n"
    "buffer_load_dwordx4 a[244:247], v0, s[16:19], s92 offen offset:2048 sc1\n"
    "v_exp_f32_e32 v173, v173\n"
    "v_exp_f32_e32 v174, v174\n"
    "v_exp_f32_e32 v175, v175\n"
    "buffer_load_dwordx4 a[248:251], v0, s[16:19], s92 offen offset:3072 sc1\n"
    "v_cvt_pk_bf16_f32 v180, v168, v169\n"
    "v_cvt_pk_bf16_f32 v181, v170, v171\n"
    "v_cvt_pk_bf16_f32 v182, v172, v173\n"
    "v_cvt_pk_bf16_f32 v183, v174, v175\n"
    "s_waitcnt lgkmcnt(3)\n"
    "ds_read_b128 v[204:207], v14 offset:47616\n"
    "v_mfma_f32_32x32x16_bf16 v[112:127], v[212:215], v[180:183], v[112:127]\n"
    "v_exp_f32_e32 v144, v144\n"
    "v_exp_f32_e32 v145, v145\n"
    "v_exp_f32_e32 v146, v146\n"
    "v_exp_f32_e32 v147, v147\n"
    "s_waitcnt lgkmcnt(1)\n"
    "v_mfma_f32_32x32x16_bf16 v[160:175], v[192:195], v[104:107], v[232:247]\n"
    "ds_read_b128 v[208:211], v14 offset:13312\n"
    "v_exp_f32_e32 v148, v148\n"
    "v_exp_f32_e32 v149, v149\n"
    "v_exp_f32_e32 v150, v150\n"
    "v_exp_f32_e32 v151, v151\n"
    "s_add_u32 s92, s92, 0x1000\n"
    "buffer_load_dwordx4 a[252:255], v0, s[16:19], s92 offen sc1\n"
    "v_mfma_f32_32x32x16_bf16 v[160:175], v[196:199], v[108:111], v[160:175]\n"
    "ds_read_b128 v[184:187], v12 offset:19968\n"
    "ds_read_b64 v[188:189], v13 offset:19968\n"
    "v_cvt_pk_bf16_f32 v176, v144, v145\n"
    "v_cvt_pk_bf16_f32 v177, v146, v147\n"
    "v_cvt_pk_bf16_f32 v178, v148, v149\n"
    "v_cvt_pk_bf16_f32 v179, v150, v151\n"
    "buffer_load_dwordx4 a[60:63], v0, s[16:19], s93 offen sc1\n"
    "v_exp_f32_e32 v152, v152\n"
    "v_exp_f32_e32 v153, v153\n"
    "v_mfma_f32_32x32x16_bf16 v[112:127], v[200:203], v[176:179], v[112:127]\n"
    "v_exp_f32_e32 v154, v154\n"
    "v_exp_f32_e32 v155, v155\n"
    "v_exp_f32_e32 v156, v156\n"
    "buffer_load_dwordx4 a[64:67], v0, s[16:19], s93 offen offset:1024 sc1\n"
    "v_exp_f32_e32 v157, v157\n"
    "v_exp_f32_e32 v158, v158\n"
    "v_exp_f32_e32 v159, v159\n"
    "buffer_load_dwordx4 a[68:71], v0, s[16:19], s93 offen offset:2048 sc1\n"
    "v_cvt_pk_bf16_f32 v180, v152, v153\n"
    "v_cvt_pk_bf16_f32 v181, v154, v155\n"
    "v_cvt_pk_bf16_f32 v182, v156, v157\n"
    "v_cvt_pk_bf16_f32 v183, v158, v159\n"
    "s_waitcnt lgkmcnt(3)\n"
    "ds_read_b128 v[212:215], v14 offset:14336\n"
    "v_mfma_f32_32x32x16_bf16 v[112:127], v[204:207], v[180:183], v[112:127]\n"
    "v_exp_f32_e32 v160, v160\n"
    "v_exp_f32_e32 v161, v161\n"
    "v_exp_f32_e32 v162, v162\n"
    "v_exp_f32_e32 v163, v163\n"
    "s_waitcnt lgkmcnt(1)\n"
    "v_mfma_f32_32x32x16_bf16 v[144:159], v[184:187], v[104:107], 0\n"
    "ds_read_b128 v[200:203], v14 offset:19968\n"
    "v_exp_f32_e32 v164, v164\n"
    "v_exp_f32_e32 v165, v165\n"
    "v_exp_f32_e32 v166, v166\n"
    "v_exp_f32_e32 v167, v167\n"
    "buffer_load_dwordx4 a[72:75], v0, s[16:19], s93 offen offset:3072 sc1\n"
    "v_mfma_f32_32x32x16_bf16 v[144:159], v[188:191], v[108:111], v[144:159]\n"
    "ds_read_b128 v[192:195], v12 offset:26624\n"
    "ds_read_b64 v[196:197], v13 offset:26624\n"
    "v_cvt_pk_bf16_f32 v176, v160, v161\n"
    "v_cvt_pk_bf16_f32 v177, v162, v163\n"
    "v_cvt_pk_bf16_f32 v178, v164, v165\n"
    "v_cvt_pk_bf16_f32 v179, v166, v167\n"
    "s_add_u32 s93, s93, 0x1000\n"
    "buffer_load_dwordx4 a[76:79], v0, s[16:19], s93 offen sc1\n"
    "v_exp_f32_e32 v168, v168\n"
    "v_exp_f32_e32 v169, v169\n"
    "v_mfma_f32_32x32x16_bf16 v[128:143], v[208:211], v[176:179], v[128:143]\n"
    "v_exp_f32_e32 v170, v170\n"
    "v_exp_f32_e32 v171, v171\n"
    "v_exp_f32_e32 v172, v172\n"
    "buffer_load_dwordx4 a[80:83], v0, s[16:19], s93 offen offset:1024 sc1\n"
    "v_exp_f32_e32 v173, v173\n"
    "v_exp_f32_e32 v174, v174\n"
    "v_exp_f32_e32 v175, v175\n"
    "buffer_load_dwordx4 a[84:87], v0, s[16:19], s93 offen offset:2048 sc1\n"
    "v_cvt_pk_bf16_f32 v180, v168, v169\n"
    "v_cvt_pk_bf16_f32 v181, v170, v171\n"
    "v_cvt_pk_bf16_f32 v182, v172, v173\n"
    "v_cvt_pk_bf16_f32 v183, v174, v175\n"
    "s_waitcnt lgkmcnt(3)\n"
    "ds_read_b128 v[204:207], v14 offset:20992\n"
    "v_mfma_f32_32x32x16_bf16 v[128:143], v[212:215], v[180:183], v[128:143]\n"
    "v_exp_f32_e32 v144, v144\n"
    "v_exp_f32_e32 v145, v145\n"
    "v_exp_f32_e32 v146, v146\n"
    "v_exp_f32_e32 v147, v147\n"
    "s_waitcnt lgkmcnt(1)\n"
    "v_mfma_f32_32x32x16_bf16 v[160:175], v[192:195], v[104:107], 0\n"
    "ds_read_b128 v[208:211], v14 offset:26624\n"
    "v_exp_f32_e32 v148, v148\n"
    "v_exp_f32_e32 v149, v149\n"
    "v_exp_f32_e32 v150, v150\n"
    "v_exp_f32_e32 v151, v151\n"
    "buffer_load_dwordx4 a[88:91], v0, s[16:19], s93 offen offset:3072 sc1\n"
    "v_mfma_f32_32x32x16_bf16 v[160:175], v[196:199], v[108:111], v[160:175]\n"
    "ds_read_b128 v[184:187], v12 offset:33280\n"
    "ds_read_b64 v[188:189], v13 offset:33280\n"
    "v_cvt_pk_bf16_f32 v176, v144, v145\n"
    "v_cvt_pk_bf16_f32 v177, v146, v147\n"
    "v_cvt_pk_bf16_f32 v178, v148, v149\n"
    "v_cvt_pk_bf16_f32 v179, v150, v151\n"
    "s_add_u32 s93, s93, 0x1000\n"
    "buffer_load_dwordx4 a[92:95], v0, s[16:19], s93 offen sc1\n"
    "v_exp_f32_e32 v152, v152\n"
    "v_exp_f32_e32 v153, v153\n"
    "v_mfma_f32_32x32x16_bf16 v[128:143], v[200:203], v[176:179], v[128:143]\n"
    "v_exp_f32_e32 v154, v154\n"
    "v_exp_f32_e32 v155, v155\n"
    "v_exp_f32_e32 v156, v156\n"
    "buffer_load_dwordx4 a[96:99], v0, s[16:19], s93 offen offset:1024 sc1\n"
    "v_exp_f32_e32 v157, v157\n"
    "v_exp_f32_e32 v158, v158\n"
    "v_exp_f32_e32 v159, v159\n"
    "buffer_load_dwordx4 a[100:103], v0, s[16:19], s93 offen offset:2048 sc1\n"
    "v_cvt_pk_bf16_f32 v180, v152, v153\n"
    "v_cvt_pk_bf16_f32 v181, v154, v155\n"
    "v_cvt_pk_bf16_f32 v182, v156, v157\n"
    "v_cvt_pk_bf16_f32 v183, v158, v159\n"
    "s_waitcnt lgkmcnt(3)\n"
    "ds_read_b128 v[212:215], v14 offset:27648\n"
    "v_mfma_f32_32x32x16_bf16 v[128:143], v[204:207], v[180:183], v[128:143]\n"
    "v_exp_f32_e32 v160, v160\n"
    "v_exp_f32_e32 v161, v161\n"
    "v_exp_f32_e32 v162, v162\n"
    "v_exp_f32_e32 v163, v163\n"
    "s_waitcnt lgkmcnt(1)\n"
    "v_mfma_f32_32x32x16_bf16 v[144:159], v[184:187], v[104:107], 0\n"
    "ds_read_b128 v[200:203], v14 offset:33280\n"
    "v_exp_f32_e32 v164, v164\n"
    "v_exp_f32_e32 v165, v165\n"
    "v_exp_f32_e32 v166, v166\n"
    "v_exp_f32_e32 v167, v167\n"
    "buffer_load_dwordx4 a[104:107], v0, s[16:19], s93 offen offset:3072 sc1\n"
    "v_mfma_f32_32x32x16_bf16 v[144:159], v[188:191], v[108:111], v[144:159]\n"
    "ds_read_b128 v[192:195], v12 offset:39936\n"
    "ds_read_b64 v[196:197], v13 offset:39936\n"
    "v_cvt_pk_bf16_f32 v176, v160, v161\n"
    "v_cvt_pk_bf16_f32 v177, v162, v163\n"
    "v_cvt_pk_bf16_f32 v178, v164, v165\n"
    "v_cvt_pk_bf16_f32 v179, v166, v167\n"
    "s_add_u32 s93, s93, 0x1000\n"
    "buffer_load_dwordx4 a[108:111], v0, s[16:19], s93 offen sc1\n"
    "v_exp_f32_e32 v168, v168\n"
    "v_exp_f32_e32 v169, v169\n"
    "v_mfma_f32_32x32x16_bf16 v[128:143], v[208:211], v[176:179], v[128:143]\n"
    "v_exp_f32_e32 v170, v170\n"
    "v_exp_f32_e32 v171, v171\n"
    "v_exp_f32_e32 v172, v172\n"
    "buffer_load_dwordx4 a[112:115], v0, s[16:19], s93 offen offset:1024 sc1\n"
    "v_exp_f32_e32 v173, v173\n"
    "v_exp_f32_e32 v174, v174\n"
    "v_exp_f32_e32 v175, v175\n"
    "buffer_load_dwordx4 a[116:119], v0, s[16:19], s93 offen offset:2048 sc1\n"
    "v_cvt_pk_bf16_f32 v180, v168, v169\n"
    "v_cvt_pk_bf16_f32 v181, v170, v171\n"
    "v_cvt_pk_bf16_f32 v182, v172, v173\n"
    "v_cvt_pk_bf16_f32 v183, v174, v175\n"
    "s_waitcnt lgkmcnt(3)\n"
    "ds_read_b128 v[204:207], v14 offset:34304\n"
    "v_mfma_f32_32x32x16_bf16 v[128:143], v[212:215], v[180:183], v[128:143]\n"
    "v_exp_f32_e32 v144, v144\n"
    "v_exp_f32_e32 v145, v145\n"
    "v_exp_f32_e32 v146, v146\n"
    "v_exp_f32_e32 v147, v147\n"
    "s_waitcnt lgkmcnt(1)\n"
    "v_mfma_f32_32x32x16_bf16 v[160:175], v[192:195], v[104:107], 0\n"
    "ds_read_b128 v[208:211], v14 offset:39936\n"
    "v_exp_f32_e32 v148, v148\n"
    "v_exp_f32_e32 v149, v149\n"
    "v_exp_f32_e32 v150, v150\n"
    "v_exp_f32_e32 v151, v151\n"
    "buffer_load_dwordx4 a[120:123], v0, s[16:19], s93 offen offset:3072 sc1\n"
    "v_mfma_f32_32x32x16_bf16 v[160:175], v[196:199], v[108:111], v[160:175]\n"
    "ds_read_b128 v[184:187], v12 offset:46592\n"
    "ds_read_b64 v[188:189], v13 offset:46592\n"
    "v_cvt_pk_bf16_f32 v176, v144, v145\n"
    "v_cvt_pk_bf16_f32 v177, v146, v147\n"
    "v_cvt_pk_bf16_f32 v178, v148, v149\n"
    "v_cvt_pk_bf16_f32 v179, v150, v151\n"
    "s_add_u32 s93, s93, 0x1000\n"
    "buffer_load_dwordx4 a[124:127], v0, s[16:19], s93 offen sc1\n"
    "v_exp_f32_e32 v152, v152\n"
    "v_exp_f32_e32 v153, v153\n"
    "v_mfma_f32_32x32x16_bf16 v[128:143], v[200:203], v[176:179], v[128:143]\n"
    "v_exp_f32_e32 v154, v154\n"
    "v_exp_f32_e32 v155, v155\n"
    "v_exp_f32_e32 v156, v156\n"
    "buffer_load_dwordx4 a[128:131], v0, s[16:19], s93 offen offset:1024 sc1\n"
    "v_exp_f32_e32 v157, v157\n"
    "v_exp_f32_e32 v158, v158\n"
    "v_exp_f32_e32 v159, v159\n"
    "buffer_load_dwordx4 a[132:135], v0, s[16:19], s93 offen offset:2048 sc1\n"
    "v_cvt_pk_bf16_f32 v180, v152, v153\n"
    "v_cvt_pk_bf16_f32 v181, v154, v155\n"
    "v_cvt_pk_bf16_f32 v182, v156, v157\n"
    "v_cvt_pk_bf16_f32 v183, v158, v159\n"
    "s_waitcnt lgkmcnt(3)\n"
    "ds_read_b128 v[212:215], v14 offset:40960\n"
    "v_mfma_f32_32x32x16_bf16 v[128:143], v[204:207], v[180:183], v[128:143]\n"
    "v_exp_f32_e32 v160, v160\n"
    "v_exp_f32_e32 v161, v161\n"
    "v_exp_f32_e32 v162, v162\n"
    "v_exp_f32_e32 v163, v163\n"
    "s_waitcnt lgkmcnt(1)\n"
    "v_mfma_f32_32x32x16_bf16 v[144:159], v[184:187], v[104:107], 0\n"
    "ds_read_b128 v[200:203], v14 offset:46592\n"
    "v_exp_f32_e32 v164, v164\n"
    "v_exp_f32_e32 v165, v165\n"
    "v_exp_f32_e32 v166, v166\n"
    "v_exp_f32_e32 v167, v167\n"
    "buffer_load_dwordx4 a[136:139], v0, s[16:19], s93 offen offset:3072 sc1\n"
    "v_mfma_f32_32x32x16_bf16 v[144:159], v[188:191], v[108:111], v[144:159]\n"
    "v_cvt_pk_bf16_f32 v176, v160, v161\n"
    "v_cvt_pk_bf16_f32 v177, v162, v163\n"
    "v_cvt_pk_bf16_f32 v178, v164, v165\n"
    "v_cvt_pk_bf16_f32 v179, v166, v167\n"
    "s_add_u32 s93, s93, 0x1000\n"
    "buffer_load_dwordx4 a[140:143], v0, s[16:19], s93 offen sc1\n"
    "v_exp_f32_e32 v168, v168\n"
    "v_exp_f32_e32 v169, v169\n"
    "v_mfma_f32_32x32x16_bf16 v[128:143], v[208:211], v[176:179], v[128:143]\n"
    "v_exp_f32_e32 v170, v170\n"
    "v_exp_f32_e32 v171, v171\n"
    "v_exp_f32_e32 v172, v172\n"
    "buffer_load_dwordx4 a[144:147], v0, s[16:19], s93 offen offset:1024 sc1\n"
    "v_exp_f32_e32 v173, v173\n"
    "v_exp_f32_e32 v174, v174\n"
    "v_exp_f32_e32 v175, v175\n"
    "buffer_load_dwordx4 a[148:151], v0, s[16:19], s93 offen offset:2048 sc1\n"
    "v_cvt_pk_bf16_f32 v180, v168, v169\n"
    "v_cvt_pk_bf16_f32 v181, v170, v171\n"
    "v_cvt_pk_bf16_f32 v182, v172, v173\n"
    "v_cvt_pk_bf16_f32 v183, v174, v175\n"
    "s_waitcnt lgkmcnt(1)\n"
    "ds_read_b128 v[204:207], v14 offset:47616\n"
    "v_mfma_f32_32x32x16_bf16 v[128:143], v[212:215], v[180:183], v[128:143]\n"
    "v_exp_f32_e32 v144, v144\n"
    "v_exp_f32_e32 v145, v145\n"
    "v_exp_f32_e32 v146, v146\n"
    "v_exp_f32_e32 v147, v147\n"
    "v_exp_f32_e32 v148, v148\n"
    "v_exp_f32_e32 v149, v149\n"
    "v_exp_f32_e32 v150, v150\n"
    "v_exp_f32_e32 v151, v151\n"
    "buffer_load_dwordx4 a[152:155], v0, s[16:19], s93 offen offset:3072 sc1\n"
    "v_cvt_pk_bf16_f32 v176, v144, v145\n"
    "v_cvt_pk_bf16_f32 v177, v146, v147\n"
    "v_cvt_pk_bf16_f32 v178, v148, v149\n"
    "v_cvt_pk_bf16_f32 v179, v150, v151\n"
    "v_exp_f32_e32 v152, v152\n"
    "v_exp_f32_e32 v153, v153\n"
    "s_waitcnt lgkmcnt(1)\n"
    "v_mfma_f32_32x32x16_bf16 v[128:143], v[200:203], v[176:179], v[128:143]\n"
    "v_exp_f32_e32 v154, v154\n"
    "v_exp_f32_e32 v155, v155\n"
    "v_exp_f32_e32 v156, v156\n"
    "v_exp_f32_e32 v157, v157\n"
    "v_exp_f32_e32 v158, v158\n"
    "v_exp_f32_e32 v159, v159\n"
    "v_cvt_pk_bf16_f32 v180, v152, v153\n"
    "v_cvt_pk_bf16_f32 v181, v154, v155\n"
    "v_cvt_pk_bf16_f32 v182, v156, v157\n"
    "v_cvt_pk_bf16_f32 v183, v158, v159\n"
    "s_waitcnt lgkmcnt(0)\n"
    "s_nop 0\n"
    "v_mfma_f32_32x32x16_bf16 v[128:143], v[204:207], v[180:183], v[128:143]\n"
    ".Lmk_haloj_45:\n"
    "s_cmp_eq_u32 s29, 0\n"
    "s_cbranch_scc0 .Lmk_l0b_46\n"
    "v_add_f32_e32 v32, v32, v33\n"
    "v_add_f32_e32 v34, v34, v35\n"
    "v_add_f32_e32 v32, v32, v34\n"
    "v_mov_b32_e32 v33, v32\n"
    "s_nop 1\n"
    "v_permlane32_swap_b32 v33, v32\n"
    "v_add_f32_e32 v32, v33, v32\n"
    "v_sub_f32_e32 v32, 0, v32\n"
    "v_cvt_pk_bf16_f32 v33, v32, v32\n"
    "v_lshlrev_b32_e32 v33, 16, v33\n"
    "v_sub_f32_e32 v33, v32, v33\n"
    "v_cvt_pk_bf16_f32 v32, v32, v33\n"
    "v_cndmask_b32_e64 v32, v32, 0, s[48:49]\n"
    "v_accvgpr_write_b32 a14, v32\n"
    "v_lshl_or_b32 v48, v48, 16, v48\n"
    "v_cndmask_b32_e64 v48, v48, 0, s[48:49]\n"
    "v_accvgpr_write_b32 a18, v48\n"
    "v_add_f32_e32 v36, v36, v37\n"
    "v_add_f32_e32 v38, v38, v39\n"
    "v_add_f32_e32 v36, v36, v38\n"
    "v_mov_b32_e32 v37, v36\n"
    "s_nop 1\n"
    "v_permlane32_swap_b32 v37, v36\n"
    "v_add_f32_e32 v36, v37, v36\n"
    "v_sub_f32_e32 v36, 0, v36\n"
    "v_cvt_pk_bf16_f32 v37, v36, v36\n"
    "v_lshlrev_b32_e32 v37, 16, v37\n"
    "v_sub_f32_e32 v37, v36, v37\n"
    "v_cvt_pk_bf16_f32 v36, v36, v37\n"
    "v_cndmask_b32_e64 v36, v36, 0, s[48:49]\n"
    "v_accvgpr_write_b32 a15, v36\n"
    "v_lshl_or_b32 v49, v49, 16, v49\n"
    "v_cndmask_b32_e64 v49, v49, 0, s[48:49]\n"
    "v_accvgpr_write_b32 a19, v49\n"
    "v_add_f32_e32 v40, v40, v41\n"
    "v_add_f32_e32 v42, v42, v43\n"
    "v_add_f32_e32 v40, v40, v42\n"
    "v_mov_b32_e32 v41, v40\n"
    "s_nop 1\n"
    "v_permlane32_swap_b32 v41, v40\n"
    "v_add_f32_e32 v40, v41, v40\n"
    "v_sub_f32_e32 v40, 0, v40\n"
    "v_cvt_pk_bf16_f32 v41, v40, v40\n"
    "v_lshlrev_b32_e32 v41, 16, v41\n"
    "v_sub_f32_e32 v41, v40, v41\n"
    "v_cvt_pk_bf16_f32 v40, v40, v41\n"
    "v_cndmask_b32_e64 v40, v40, 0, s[48:49]\n"
    "v_accvgpr_write_b32 a16, v40\n"
    "v_lshl_or_b32 v50, v50, 16, v50\n"
    "v_cndmask_b32_e64 v50, v50, 0, s[48:49]\n"
    "v_accvgpr_write_b32 a20, v50\n"
    "v_add_f32_e32 v44, v44, v45\n"
    "v_add_f32_e32 v46, v46, v47\n"
    "v_add_f32_e32 v44, v44, v46\n"
    "v_mov_b32_e32 v45, v44\n"
    "s_nop 1\n"
    "v_permlane32_swap_b32 v45, v44\n"
    "v_add_f32_e32 v44, v45, v44\n"
    "v_sub_f32_e32 v44, 0, v44\n"
    "v_cvt_pk_bf16_f32 v45, v44, v44\n"
    "v_lshlrev_b32_e32 v45, 16, v45\n"
    "v_sub_f32_e32 v45, v44, v45\n"
    "v_cvt_pk_bf16_f32 v44, v44, v45\n"
    "v_cndmask_b32_e64 v44, v44, 0, s[48:49]\n"
    "v_accvgpr_write_b32 a17, v44\n"
    "v_lshl_or_b32 v51, v51, 16, v51\n"
    "v_cndmask_b32_e64 v51, v51, 0, s[48:49]\n"
    "v_accvgpr_write_b32 a21, v51\n"
    ".Lmk_l0b_46:\n"
    "s_and_b32 s51, s28, 3\n"
    "s_mul_i32 s51, s51, 5\n"
    "s_lshr_b32 s72, s28, 2\n"
    "s_mul_i32 s72, s72, 5120\n"
    "s_lshl_b32 s53, s51, 8\n"
    "s_add_u32 s72, s72, s53\n"
    "s_mul_i32 s53, s29, 0x280000\n"
    "s_add_u32 s72, s72, s53\n"
    "s_add_u32 s72, s72, 0x1fad800\n"
    "v_and_b32_e32 v21, 0xf0, v0\n"
    "v_bfe_u32 v22, v0, 8, 1\n"
    "v_mul_u32_u24_e32 v22, 20480, v22\n"
    "v_add_u32_e32 v21, v21, v22\n"
    "v_lshrrev_b32_e32 v22, 9, v0\n"
    "v_lshl_add_u32 v22, v22, 8, v21\n"
    "v_mov_b32_e32 v23, 0x7c000000\n"
    "v_cndmask_b32_e64 v23, v21, v23, s[48:49]\n"
    "v_cndmask_b32_e64 v9, v124, v120, s[48:49]\n"
    "v_mul_f32_e32 v15, 0, v120\n"
    "v_fmac_f32_e32 v15, 0, v121\n"
    "v_fmac_f32_e32 v15, 0, v122\n"
    "v_fmac_f32_e32 v15, 0, v123\n"
    "v_cndmask_b32_e64 v15, v15, 0, s[48:49]\n"
    "v_fmac_f32_e32 v15, 0, v112\n"
    "v_fmac_f32_e32 v15, 0, v113\n"
    "v_fmac_f32_e32 v15, 0, v114\n"
    "v_fmac_f32_e32 v15, 0, v115\n"
    "v_fmac_f32_e32 v15, 0, v116\n"
    "v_fmac_f32_e32 v15, 0, v117\n"
    "v_fmac_f32_e32 v15, 0, v118\n"
    "v_fmac_f32_e32 v15, 0, v119\n"
    "v_cndmask_b32_e64 v10, v140, v136, s[48:49]\n"
    "v_mul_f32_e32 v16, 0, v136\n"
    "v_fmac_f32_e32 v16, 0, v137\n"
    "v_fmac_f32_e32 v16, 0, v138\n"
    "v_fmac_f32_e32 v16, 0, v139\n"
    "v_cndmask_b32_e64 v16, v16, 0, s[48:49]\n"
    "v_fmac_f32_e32 v16, 0, v128\n"
    "v_fmac_f32_e32 v16, 0, v129\n"
    "v_fmac_f32_e32 v16, 0, v130\n"
    "v_fmac_f32_e32 v16, 0, v131\n"
    "v_fmac_f32_e32 v16, 0, v132\n"
    "v_fmac_f32_e32 v16, 0, v133\n"
    "v_fmac_f32_e32 v16, 0, v134\n"
    "v_fmac_f32_e32 v16, 0, v135\n"
    "v_mov_b32_e32 v11, 0x2b800000\n"
    "v_mov_b32_e32 v17, 0x53800000\n"
    "v_cmp_nge_f32_e64 s[82:83], v9, v11\n"
    "v_cmp_nle_f32_e64 s[84:85], v9, v17\n"
    "v_cmp_nge_f32_e64 s[86:87], v10, v11\n"
    "v_cmp_nle_f32_e64 s[88:89], v10, v17\n"
    "v_add_f32_e32 v15, v15, v16\n"
    "s_or_b64 s[82:83], s[82:83], s[84:85]\n"
    "s_or_b64 s[86:87], s[86:87], s[88:89]\n"
    "v_cmp_u_f32_e64 s[84:85], v15, v15\n"
    "s_or_b64 s[82:83], s[82:83], s[86:87]\n"
    "s_or_b64 s[82:83], s[82:83], s[84:85]\n"
    "s_cmp_lg_u64 s[82:83], 0\n"
    "s_cbranch_scc1 .Lmk_fix_47\n"
    ".Lmk_back_48:\n"
    "v_cndmask_b32_e64 v9, v124, v120, s[48:49]\n"
    "v_cndmask_b32_e64 v10, v140, v136, s[48:49]\n"
    "v_rcp_f32_e32 v9, v9\n"
    "v_rcp_f32_e32 v10, v10\n"
    "s_mov_b32 s54, s35\n"
    "s_lshl_b32 s55, s27, 2\n"
    "s_add_u32 s54, s54, s55\n"
    "s_mul_i32 s54, s54, 40960\n"
    "s_add_u32 s54, s54, s72\n"
    "s_lshl_b32 s55, s34, 9\n"
    "s_add_u32 s55, s55, s54\n"
    "v_mul_f32_e32 v112, v9, v112\n"
    "v_mul_f32_e32 v113, v9, v113\n"
    "v_mul_f32_e32 v114, v9, v114\n"
    "v_mul_f32_e32 v115, v9, v115\n"
    "v_mul_f32_e32 v116, v9, v116\n"
    "v_mul_f32_e32 v117, v9, v117\n"
    "v_mul_f32_e32 v118, v9, v118\n"
    "v_mul_f32_e32 v119, v9, v119\n"
    "v_cvt_pk_bf16_f32 v112, v112, v113\n"
    "v_cvt_pk_bf16_f32 v113, v114, v115\n"
    "v_cvt_pk_bf16_f32 v114, v116, v117\n"
    "v_cvt_pk_bf16_f32 v115, v118, v119\n"
    "buffer_store_dwordx4 v[112:115], v22, s[16:19], s55 offen sc1\n"
    "v_mul_f32_e32 v120, v9, v120\n"
    "v_mul_f32_e32 v121, v9, v121\n"
    "v_mul_f32_e32 v122, v9, v122\n"
    "v_mul_f32_e32 v123, v9, v123\n"
    "v_cvt_pk_bf16_f32 v116, v120, v121\n"
    "v_cvt_pk_bf16_f32 v117, v122, v123\n"
    "s_lshl_b32 s55, s34, 3\n"
    "s_add_u32 s55, s55, s54\n"
    "buffer_store_dwordx2 v[116:117], v23, s[16:19], s55 offen offset:1024 sc1\n"
    "s_sub_u32 s54, 3, s35\n"
    "s_lshl_b32 s55, s27, 2\n"
    "s_add_u32 s54, s54, s55\n"
    "s_mul_i32 s54, s54, 40960\n"
    "s_add_u32 s54, s54, s72\n"
    "s_lshl_b32 s55, s34, 9\n"
    "s_add_u32 s55, s55, s54\n"
    "v_mul_f32_e32 v128, v10, v128\n"
    "v_mul_f32_e32 v129, v10, v129\n"
    "v_mul_f32_e32 v130, v10, v130\n"
    "v_mul_f32_e32 v131, v10, v131\n"
    "v_mul_f32_e32 v132, v10, v132\n"
    "v_mul_f32_e32 v133, v10, v133\n"
    "v_mul_f32_e32 v134, v10, v134\n"
    "v_mul_f32_e32 v135, v10, v135\n"
    "v_cvt_pk_bf16_f32 v128, v128, v129\n"
    "v_cvt_pk_bf16_f32 v129, v130, v131\n"
    "v_cvt_pk_bf16_f32 v130, v132, v133\n"
    "v_cvt_pk_bf16_f32 v131, v134, v135\n"
    "buffer_store_dwordx4 v[128:131], v22, s[16:19], s55 offen sc1\n"
    "v_mul_f32_e32 v136, v10, v136\n"
    "v_mul_f32_e32 v137, v10, v137\n"
    "v_mul_f32_e32 v138, v10, v138\n"
    "v_mul_f32_e32 v139, v10, v139\n"
    "v_cvt_pk_bf16_f32 v132, v136, v137\n"
    "v_cvt_pk_bf16_f32 v133, v138, v139\n"
    "s_lshl_b32 s55, s34, 3\n"
    "s_add_u32 s55, s55, s54\n"
    "buffer_store_dwordx2 v[132:133], v23, s[16:19], s55 offen offset:1024 sc1\n"
    "s_lshl_b32 s56, s29, 4\n"
    "s_add_u32 s56, s56, s27\n"
    "s_lshl_b32 s56, s56, 9\n"
    "s_add_u32 s56, s56, 0x6000\n"
    "s_lshl_b32 s57, s28, 2\n"
    "s_add_u32 s57, s57, s26\n"
    "s_lshl_b32 s57, s57, 3\n"
    "s_add_u32 s57, s57, s56\n"
    "s_add_u32 s90, s37, 1\n"
    "s_mul_i32 s90, s90, 40960\n"
    "s_mul_i32 s91, s26, 2048\n"
    "s_add_u32 s90, s90, s91\n"
    "s_add_u32 s90, s90, 0x314000\n"
    "s_add_u32 s91, s91, 0x12000\n"
    "s_barrier\n"
    "s_mov_b32 m0, s91\n"
    "s_nop 0\n"
    "buffer_load_dwordx4 v0, s[16:19], s90 offen sc1 lds\n"
    "buffer_load_dwordx4 v0, s[16:19], s90 offen offset:1024 sc1 lds\n"
    "s_waitcnt vmcnt(2)\n"
    "buffer_store_dwordx2 v[2:3], v4, s[16:19], s57 offen sc1\n"
    "v_accvgpr_read_b32 v100, a0\n"
    "v_accvgpr_read_b32 v101, a1\n"
    "v_accvgpr_read_b32 v102, a2\n"
    "v_accvgpr_read_b32 v103, a3\n"
    "v_accvgpr_read_b32 v104, a4\n"
    "v_accvgpr_read_b32 v105, a5\n"
    "v_accvgpr_read_b32 v106, a6\n"
    "v_accvgpr_read_b32 v107, a7\n"
    "v_accvgpr_read_b32 v108, a8\n"
    "v_accvgpr_read_b32 v109, a9\n"
    "v_lshlrev_b32_e32 v16, 16, v100\n"
    "v_and_b32_e32 v17, 0xffff0000, v100\n"
    "v_lshlrev_b32_e32 v18, 16, v101\n"
    "v_and_b32_e32 v19, 0xffff0000, v101\n"
    "v_lshlrev_b32_e32 v20, 16, v102\n"
    "v_and_b32_e32 v21, 0xffff0000, v102\n"
    "v_lshlrev_b32_e32 v22, 16, v103\n"
    "v_and_b32_e32 v23, 0xffff0000, v103\n"
    "v_lshlrev_b32_e32 v24, 16, v104\n"
    "v_and_b32_e32 v25, 0xffff0000, v104\n"
    "v_lshlrev_b32_e32 v26, 16, v105\n"
    "v_and_b32_e32 v27, 0xffff0000, v105\n"
    "v_lshlrev_b32_e32 v28, 16, v106\n"
    "v_and_b32_e32 v29, 0xffff0000, v106\n"
    "v_lshlrev_b32_e32 v30, 16, v107\n"
    "v_and_b32_e32 v31, 0xffff0000, v107\n"
    "v_lshlrev_b32_e32 v32, 16, v108\n"
    "v_and_b32_e32 v33, 0xffff0000, v108\n"
    "v_lshlrev_b32_e32 v34, 16, v109\n"
    "v_and_b32_e32 v35, 0xffff0000, v109\n"
    "s_mul_i32 s61, s29, 0x500000\n"
    "s_lshr_b32 s59, s33, 1\n"
    "s_mul_i32 s59, s59, 40960\n"
    "s_add_u32 s61, s61, s59\n"
    "s_mul_i32 s59, s31, 5120\n"
    "s_add_u32 s61, s61, s59\n"
    "s_add_u32 s61, s61, 0xaad800\n"
    "s_mul_i32 s62, s29, 0x40000\n"
    "s_lshl_b32 s59, s33, 3\n"
    "s_add_u32 s59, s59, s31\n"
    "s_lshl_b32 s59, s59, 7\n"
    "s_add_u32 s62, s62, s59\n"
    "s_add_u32 s62, s62, 0x124d800\n"
    "s_and_b32 s59, s33, 1\n"
    "s_lshl_b32 s59, s59, 8\n"
    "v_and_b32_e32 v10, 0xf0, v0\n"
    "v_and_b32_e32 v12, 0x200, v0\n"
    "v_or_b32_e32 v10, v10, v12\n"
    "v_bfe_u32 v12, v0, 8, 1\n"
    "v_lshl_add_u32 v10, v12, 3, v10\n"
    "v_add_u32_e32 v10, s59, v10\n"
    "v_lshrrev_b32_e32 v11, 1, v0\n"
    "v_mov_b32_e32 v12, 0x7c000000\n"
    "v_cmp_gt_u32_e32 vcc, 0x100, v0\n"
    "s_nop 1\n"
    "v_cndmask_b32_e32 v11, v12, v11, vcc\n"
    "s_add_u32 s64, s26, 0\n"
    "s_and_b32 s64, s64, 3\n"
    "s_sub_u32 s59, s33, s26\n"
    "s_add_u32 s64, s64, s59\n"
    "s_mul_i32 s64, s64, 20480\n"
    "s_mul_i32 s59, s26, 5120\n"
    "s_add_u32 s64, s64, s59\n"
    "s_mul_i32 s59, s29, 0x280000\n"
    "s_add_u32 s64, s64, s59\n"
    "s_add_u32 s64, s64, 0x1fad800\n"
    "s_add_u32 s65, s26, 1\n"
    "s_and_b32 s65, s65, 3\n"
    "s_sub_u32 s59, s33, s26\n"
    "s_add_u32 s65, s65, s59\n"
    "s_mul_i32 s65, s65, 20480\n"
    "s_mul_i32 s59, s26, 5120\n"
    "s_add_u32 s65, s65, s59\n"
    "s_mul_i32 s59, s29, 0x280000\n"
    "s_add_u32 s65, s65, s59\n"
    "s_add_u32 s65, s65, 0x1fad800\n"
    "s_add_u32 s66, s26, 2\n"
    "s_and_b32 s66, s66, 3\n"
    "s_sub_u32 s59, s33, s26\n"
    "s_add_u32 s66, s66, s59\n"
    "s_mul_i32 s66, s66, 20480\n"
    "s_mul_i32 s59, s26, 5120\n"
    "s_add_u32 s66, s66, s59\n"
    "s_mul_i32 s59, s29, 0x280000\n"
    "s_add_u32 s66, s66, s59\n"
    "s_add_u32 s66, s66, 0x1fad800\n"
    "s_add_u32 s67, s26, 3\n"
    "s_and_b32 s67, s67, 3\n"
    "s_sub_u32 s59, s33, s26\n"
    "s_add_u32 s67, s67, s59\n"
    "s_mul_i32 s67, s67, 20480\n"
    "s_mul_i32 s59, s26, 5120\n"
    "s_add_u32 s67, s67, s59\n"
    "s_mul_i32 s59, s29, 0x280000\n"
    "s_add_u32 s67, s67, s59\n"
    "s_add_u32 s67, s67, 0x1fad800\n"
    "s_lshl_b32 s60, s29, 4\n"
    "s_add_u32 s60, s60, s27\n"
    "s_lshl_b32 s60, s60, 2\n"
    "s_add_u32 s60, s60, s26\n"
    "s_lshl_b32 s60, s60, 7\n"
    "s_add_u32 s60, s60, 0x6000\n"
    "v_lshrrev_b32_e32 v9, 1, v0\n"
    "v_and_b32_e32 v9, 0x78, v9\n"
    "s_add_u32 s90, s37, 2\n"
    "s_mul_i32 s90, s90, 40960\n"
    "s_mul_i32 s91, s26, 10240\n"
    "s_add_u32 s90, s90, s91\n"
    "s_add_u32 s90, s90, 0x30c000\n"
    "s_add_u32 s91, s91, 0x14000\n"
    "buffer_load_dwordx2 v[254:255], v9, s[16:19], s60 offen sc1\n"
    "s_waitcnt vmcnt(0)\n"
    "v_cmp_ne_u64_e32 vcc, s[24:25], v[254:255]\n"
    "s_cbranch_vccnz .Lmk_slow_49\n"
    ".Lmk_back_50:\n"
    "buffer_load_dwordx4 v[112:115], v0, s[16:19], s65 offen sc1\n"
    "buffer_load_dwordx4 v[116:119], v0, s[16:19], s65 offen offset:1024 sc1\n"
    "buffer_load_dwordx4 v[120:123], v0, s[16:19], s65 offen offset:2048 sc1\n"
    "buffer_load_dwordx4 v[124:127], v0, s[16:19], s65 offen offset:3072 sc1\n"
    "s_add_u32 s65, s65, 0x1000\n"
    "buffer_load_dwordx4 v[128:131], v0, s[16:19], s65 offen sc1\n"
    "buffer_load_dwordx4 v[132:135], v0, s[16:19], s66 offen sc1\n"
    "buffer_load_dwordx4 v[136:139], v0, s[16:19], s66 offen offset:1024 sc1\n"
    "buffer_load_dwordx4 v[140:143], v0, s[16:19], s66 offen offset:2048 sc1\n"
    "s_waitcnt vmcnt(7)\n"
    "v_mfma_f32_16x16x32_bf16 v[36:39], a[156:159], v[112:115], 0\n"
    "v_mfma_f32_16x16x32_bf16 v[40:43], a[160:163], v[112:115], 0\n"
    "buffer_load_dwordx4 v[144:147], v0, s[16:19], s66 offen offset:3072 sc1\n"
    "v_mfma_f32_16x16x32_bf16 v[44:47], a[164:167], v[112:115], 0\n"
    "v_mfma_f32_16x16x32_bf16 v[48:51], a[168:171], v[112:115], 0\n"
    "v_mfma_f32_16x16x32_bf16 v[52:55], a[172:175], v[112:115], 0\n"
    "s_waitcnt vmcnt(7)\n"
    "v_mfma_f32_16x16x32_bf16 v[36:39], a[176:179], v[116:119], v[36:39]\n"
    "v_mfma_f32_16x16x32_bf16 v[40:43], a[180:183], v[116:119], v[40:43]\n"
    "s_add_u32 s66, s66, 0x1000\n"
    "buffer_load_dwordx4 v[148:151], v0, s[16:19], s66 offen sc1\n"
    "v_mfma_f32_16x16x32_bf16 v[44:47], a[184:187], v[116:119], v[44:47]\n"
    "v_mfma_f32_16x16x32_bf16 v[48:51], a[188:191], v[116:119], v[48:51]\n"
    "v_mfma_f32_16x16x32_bf16 v[52:55], a[192:195], v[116:119], v[52:55]\n"
    "s_waitcnt vmcnt(7)\n"
    "v_mfma_f32_16x16x32_bf16 v[36:39], a[196:199], v[120:123], v[36:39]\n"
    "v_mfma_f32_16x16x32_bf16 v[40:43], a[200:203], v[120:123], v[40:43]\n"
    "buffer_load_dwordx4 v[152:155], v0, s[16:19], s67 offen sc1\n"
    "v_mfma_f32_16x16x32_bf16 v[44:47], a[204:207], v[120:123], v[44:47]\n"
    "v_mfma_f32_16x16x32_bf16 v[48:51], a[208:211], v[120:123], v[48:51]\n"
    "v_mfma_f32_16x16x32_bf16 v[52:55], a[212:215], v[120:123], v[52:55]\n"
    "s_waitcnt vmcnt(7)\n"
    "v_mfma_f32_16x16x32_bf16 v[36:39], a[216:219], v[124:127], v[36:39]\n"
    "v_mfma_f32_16x16x32_bf16 v[40:43], a[220:223], v[124:127], v[40:43]\n"
    "buffer_load_dwordx4 v[156:159], v0, s[16:19], s67 offen offset:1024 sc1\n"
    "v_mfma_f32_16x16x32_bf16 v[44:47], a[224:227], v[124:127], v[44:47]\n"
    "v_mfma_f32_16x16x32_bf16 v[48:51], a[228:231], v[124:127], v[48:51]\n"
    "v_mfma_f32_16x16x32_bf16 v[52:55], a[232:235], v[124:127], v[52:55]\n"
    "s_waitcnt vmcnt(7)\n"
    "v_mfma_f32_16x16x32_bf16 v[36:39], a[236:239], v[128:131], v[36:39]\n"
    "v_mfma_f32_16x16x32_bf16 v[40:43], a[240:243], v[128:131], v[40:43]\n"
    "buffer_load_dwordx4 v[160:163], v0, s[16:19], s67 offen offset:2048 sc1\n"
    "v_mfma_f32_16x16x32_bf16 v[44:47], a[244:247], v[128:131], v[44:47]\n"
    "v_mfma_f32_16x16x32_bf16 v[48:51], a[248:251], v[128:131], v[48:51]\n"
    "v_mfma_f32_16x16x32_bf16 v[52:55], a[252:255], v[128:131], v[52:55]\n"
    "s_waitcnt vmcnt(7)\n"
    "v_mfma_f32_16x16x32_bf16 v[56:59], a[156:159], v[132:135], 0\n"
    "v_mfma_f32_16x16x32_bf16 v[60:63], a[160:163], v[132:135], 0\n"
    "buffer_load_dwordx4 v[164:167], v0, s[16:19], s67 offen offset:3072 sc1\n"
    "v_mfma_f32_16x16x32_bf16 v[64:67], a[164:167], v[132:135], 0\n"
    "v_mfma_f32_16x16x32_bf16 v[68:71], a[168:171], v[132:135], 0\n"
    "v_mfma_f32_16x16x32_bf16 v[72:75], a[172:175], v[132:135], 0\n"
    "s_waitcnt vmcnt(7)\n"
    "v_mfma_f32_16x16x32_bf16 v[56:59], a[176:179], v[136:139], v[56:59]\n"
    "v_mfma_f32_16x16x32_bf16 v[60:63], a[180:183], v[136:139], v[60:63]\n"
    "s_add_u32 s67, s67, 0x1000\n"
    "buffer_load_dwordx4 v[168:171], v0, s[16:19], s67 offen sc1\n"
    "v_mfma_f32_16x16x32_bf16 v[64:67], a[184:187], v[136:139], v[64:67]\n"
    "s_add_u32 s58, s26, 1\n"
    "v_mfma_f32_16x16x32_bf16 v[68:71], a[188:191], v[136:139], v[68:71]\n"
    "s_and_b32 s58, s58, 3\n"
    "v_mfma_f32_16x16x32_bf16 v[72:75], a[192:195], v[136:139], v[72:75]\n"
    "s_mul_i32 s58, s58, 15360\n"
    "s_waitcnt vmcnt(7)\n"
    "v_mfma_f32_16x16x32_bf16 v[56:59], a[196:199], v[140:143], v[56:59]\n"
    "v_mfma_f32_16x16x32_bf16 v[60:63], a[200:203], v[140:143], v[60:63]\n"
    "buffer_load_dwordx4 v[172:175], v0, s[16:19], s64 offen sc1\n"
    "v_mfma_f32_16x16x32_bf16 v[64:67], a[204:207], v[140:143], v[64:67]\n"
    "s_add_u32 s58, s58, 0x14000\n"
    "v_mfma_f32_16x16x32_bf16 v[68:71], a[208:211], v[140:143], v[68:71]\n"
    "v_add_u32_e32 v9, s58, v0\n"
    "v_mfma_f32_16x16x32_bf16 v[72:75], a[212:215], v[140:143], v[72:75]\n"
    "ds_write_b128 v9, v[36:39]\n"
    "s_waitcnt vmcnt(7)\n"
    "v_mfma_f32_16x16x32_bf16 v[56:59], a[216:219], v[144:147], v[56:59]\n"
    "v_mfma_f32_16x16x32_bf16 v[60:63], a[220:223], v[144:147], v[60:63]\n"
    "buffer_load_dwordx4 v[176:179], v0, s[16:19], s64 offen offset:1024 sc1\n"
    "v_mfma_f32_16x16x32_bf16 v[64:67], a[224:227], v[144:147], v[64:67]\n"
    "ds_write_b128 v9, v[40:43] offset:1024\n"
    "v_mfma_f32_16x16x32_bf16 v[68:71], a[228:231], v[144:147], v[68:71]\n"
    "ds_write_b128 v9, v[44:47] offset:2048\n"
    "v_mfma_f32_16x16x32_bf16 v[72:75], a[232:235], v[144:147], v[72:75]\n"
    "ds_write_b128 v9, v[48:51] offset:3072\n"
    "s_waitcnt vmcnt(7)\n"
    "v_mfma_f32_16x16x32_bf16 v[56:59], a[236:239], v[148:151], v[56:59]\n"
    "v_mfma_f32_16x16x32_bf16 v[60:63], a[240:243], v[148:151], v[60:63]\n"
    "buffer_load_dwordx4 v[180:183], v0, s[16:19], s64 offen offset:2048 sc1\n"
    "v_mfma_f32_16x16x32_bf16 v[64:67], a[244:247], v[148:151], v[64:67]\n"
    "ds_write_b128 v9, v[52:55] offset:4096\n"
    "v_mfma_f32_16x16x32_bf16 v[68:71], a[248:251], v[148:151], v[68:71]\n"
    "v_mfma_f32_16x16x32_bf16 v[72:75], a[252:255], v[148:151], v[72:75]\n"
    "s_waitcnt vmcnt(7)\n"
    "v_mfma_f32_16x16x32_bf16 v[76:79], a[156:159], v[152:155], 0\n"
    "v_mfma_f32_16x16x32_bf16 v[80:83], a[160:163], v[152:155], 0\n"
    "buffer_load_dwordx4 v[184:187], v0, s[16:19], s64 offen offset:3072 sc1\n"
    "v_mfma_f32_16x16x32_bf16 v[84:87], a[164:167], v[152:155], 0\n"
    "v_mfma_f32_16x16x32_bf16 v[88:91], a[168:171], v[152:155], 0\n"
    "v_mfma_f32_16x16x32_bf16 v[92:95], a[172:175], v[152:155], 0\n"
    "s_waitcnt vmcnt(7)\n"
    "v_mfma_f32_16x16x32_bf16 v[76:79], a[176:179], v[156:159], v[76:79]\n"
    "v_mfma_f32_16x16x32_bf16 v[80:83], a[180:183], v[156:159], v[80:83]\n"
    "s_add_u32 s64, s64, 0x1000\n"
    "buffer_load_dwordx4 v[188:191], v0, s[16:19], s64 offen sc1\n"
    "v_mfma_f32_16x16x32_bf16 v[84:87], a[184:187], v[156:159], v[84:87]\n"
    "s_add_u32 s58, s26, 2\n"
    "v_mfma_f32_16x16x32_bf16 v[88:91], a[188:191], v[156:159], v[88:91]\n"
    "s_and_b32 s58, s58, 3\n"
    "v_mfma_f32_16x16x32_bf16 v[92:95], a[192:195], v[156:159], v[92:95]\n"
    "s_mul_i32 s58, s58, 15360\n"
    "s_waitcnt vmcnt(7)\n"
    "v_mfma_f32_16x16x32_bf16 v[76:79], a[196:199], v[160:163], v[76:79]\n"
    "v_mfma_f32_16x16x32_bf16 v[80:83], a[200:203], v[160:163], v[80:83]\n"
    "v_mfma_f32_16x16x32_bf16 v[84:87], a[204:207], v[160:163], v[84:87]\n"
    "s_add_u32 s58, s58, 0x15400\n"
    "v_mfma_f32_16x16x32_bf16 v[88:91], a[208:211], v[160:163], v[88:91]\n"
    "v_add_u32_e32 v9, s58, v0\n"
    "v_mfma_f32_16x16x32_bf16 v[92:95], a[212:215], v[160:163], v[92:95]\n"
    "ds_write_b128 v9, v[56:59]\n"
    "s_waitcnt vmcnt(6)\n"
    "v_mfma_f32_16x16x32_bf16 v[76:79], a[216:219], v[164:167], v[76:79]\n"
    "v_mfma_f32_16x16x32_bf16 v[80:83], a[220:223], v[164:167], v[80:83]\n"
    "v_mfma_f32_16x16x32_bf16 v[84:87], a[224:227], v[164:167], v[84:87]\n"
    "ds_write_b128 v9, v[60:63] offset:1024\n"
    "v_mfma_f32_16x16x32_bf16 v[88:91], a[228:231], v[164:167], v[88:91]\n"
    "ds_write_b128 v9, v[64:67] offset:2048\n"
    "v_mfma_f32_16x16x32_bf16 v[92:95], a[232:235], v[164:167], v[92:95]\n"
    "ds_write_b128 v9, v[68:71] offset:3072\n"
    "s_waitcnt vmcnt(5)\n"
    "v_mfma_f32_16x16x32_bf16 v[76:79], a[236:239], v[168:171], v[76:79]\n"
    "v_mfma_f32_16x16x32_bf16 v[80:83], a[240:243], v[168:171], v[80:83]\n"
    "v_mfma_f32_16x16x32_bf16 v[84:87], a[244:247], v[168:171], v[84:87]\n"
    "ds_write_b128 v9, v[72:75] offset:4096\n"
    "v_mfma_f32_16x16x32_bf16 v[88:91], a[248:251], v[168:171], v[88:91]\n"
    "v_mfma_f32_16x16x32_bf16 v[92:95], a[252:255], v[168:171], v[92:95]\n"
    "s_waitcnt vmcnt(4)\n"
    "v_mfma_f32_16x16x32_bf16 v[16:19], a[156:159], v[172:175], v[16:19]\n"
    "v_mfma_f32_16x16x32_bf16 v[20:23], a[160:163], v[172:175], v[20:23]\n"
    "v_mfma_f32_16x16x32_bf16 v[24:27], a[164:167], v[172:175], v[24:27]\n"
    "v_mfma_f32_16x16x32_bf16 v[28:31], a[168:171], v[172:175], v[28:31]\n"
    "v_mfma_f32_16x16x32_bf16 v[32:35], a[172:175], v[172:175], v[32:35]\n"
    "s_waitcnt vmcnt(3)\n"
    "v_mfma_f32_16x16x32_bf16 v[16:19], a[176:179], v[176:179], v[16:19]\n"
    "v_mfma_f32_16x16x32_bf16 v[20:23], a[180:183], v[176:179], v[20:23]\n"
    "v_mfma_f32_16x16x32_bf16 v[24:27], a[184:187], v[176:179], v[24:27]\n"
    "s_add_u32 s58, s26, 3\n"
    "v_mfma_f32_16x16x32_bf16 v[28:31], a[188:191], v[176:179], v[28:31]\n"
    "s_and_b32 s58, s58, 3\n"
    "v_mfma_f32_16x16x32_bf16 v[32:35], a[192:195], v[176:179], v[32:35]\n"
    "s_mul_i32 s58, s58, 15360\n"
    "s_waitcnt vmcnt(2)\n"
    "v_mfma_f32_16x16x32_bf16 v[16:19], a[196:199], v[180:183], v[16:19]\n"
    "v_mfma_f32_16x16x32_bf16 v[20:23], a[200:203], v[180:183], v[20:23]\n"
    "v_mfma_f32_16x16x32_bf16 v[24:27], a[204:207], v[180:183], v[24:27]\n"
    "s_add_u32 s58, s58, 0x16800\n"
    "v_mfma_f32_16x16x32_bf16 v[28:31], a[208:211], v[180:183], v[28:31]\n"
    "v_add_u32_e32 v9, s58, v0\n"
    "v_mfma_f32_16x16x32_bf16 v[32:35], a[212:215], v[180:183], v[32:35]\n"
    "ds_write_b128 v9, v[76:79]\n"
    "s_waitcnt vmcnt(1)\n"
    "v_mfma_f32_16x16x32_bf16 v[16:19], a[216:219], v[184:187], v[16:19]\n"
    "v_mfma_f32_16x16x32_bf16 v[20:23], a[220:223], v[184:187], v[20:23]\n"
    "v_mfma_f32_16x16x32_bf16 v[24:27], a[224:227], v[184:187], v[24:27]\n"
    "ds_write_b128 v9, v[80:83] offset:1024\n"
    "v_mfma_f32_16x16x32_bf16 v[28:31], a[228:231], v[184:187], v[28:31]\n"
    "ds_write_b128 v9, v[84:87] offset:2048\n"
    "v_mfma_f32_16x16x32_bf16 v[32:35], a[232:235], v[184:187], v[32:35]\n"
    "ds_write_b128 v9, v[88:91] offset:3072\n"
    "s_waitcnt vmcnt(0)\n"
    "v_mfma_f32_16x16x32_bf16 v[16:19], a[236:239], v[188:191], v[16:19]\n"
    "v_mfma_f32_16x16x32_bf16 v[20:23], a[240:243], v[188:191], v[20:23]\n"
    "v_mfma_f32_16x16x32_bf16 v[24:27], a[244:247], v[188:191], v[24:27]\n"
    "ds_write_b128 v9, v[92:95] offset:4096\n"
    "v_mfma_f32_16x16x32_bf16 v[28:31], a[248:251], v[188:191], v[28:31]\n"
    "v_mfma_f32_16x16x32_bf16 v[32:35], a[252:255], v[188:191], v[32:35]\n"
    "s_mul_i32 s58, s26, 15360\n"
    "s_add_u32 s58, s58, 0x14000\n"
    "v_add_u32_e32 v9, s58, v0\n"
    "s_waitcnt lgkmcnt(0)\n"
    "s_barrier\n"
    "ds_read_b128 v[36:39], v9\n"
    "ds_read_b128 v[56:59], v9 offset:5120\n"
    "ds_read_b128 v[76:79], v9 offset:10240\n"
    "ds_read_b128 v[40:43], v9 offset:1024\n"
    "ds_read_b128 v[60:63], v9 offset:6144\n"
    "ds_read_b128 v[80:83], v9 offset:11264\n"
    "ds_read_b128 v[44:47], v9 offset:2048\n"
    "ds_read_b128 v[64:67], v9 offset:7168\n"
    "ds_read_b128 v[84:87], v9 offset:12288\n"
    "ds_read_b128 v[48:51], v9 offset:3072\n"
    "ds_read_b128 v[68:71], v9 offset:8192\n"
    "ds_read_b128 v[88:91], v9 offset:13312\n"
    "ds_read_b128 v[52:55], v9 offset:4096\n"
    "ds_read_b128 v[72:75], v9 offset:9216\n"
    "ds_read_b128 v[92:95], v9 offset:14336\n"
    "s_waitcnt lgkmcnt(13)\n"
    "v_pk_add_f32 v[36:37], v[36:37], v[56:57]\n"
    "s_waitcnt lgkmcnt(12)\n"
    "v_pk_add_f32 v[16:17], v[16:17], v[76:77]\n"
    "v_pk_add_f32 v[38:39], v[38:39], v[58:59]\n"
    "v_pk_add_f32 v[18:19], v[18:19], v[78:79]\n"
    "v_pk_add_f32 v[16:17], v[16:17], v[36:37]\n"
    "v_pk_add_f32 v[18:19], v[18:19], v[38:39]\n"
    "s_waitcnt lgkmcnt(10)\n"
    "v_pk_add_f32 v[40:41], v[40:41], v[60:61]\n"
    "s_waitcnt lgkmcnt(9)\n"
    "v_pk_add_f32 v[20:21], v[20:21], v[80:81]\n"
    "v_pk_add_f32 v[42:43], v[42:43], v[62:63]\n"
    "v_pk_add_f32 v[22:23], v[22:23], v[82:83]\n"
    "v_pk_add_f32 v[20:21], v[20:21], v[40:41]\n"
    "v_pk_add_f32 v[22:23], v[22:23], v[42:43]\n"
    "s_waitcnt lgkmcnt(7)\n"
    "v_pk_add_f32 v[44:45], v[44:45], v[64:65]\n"
    "s_waitcnt lgkmcnt(6)\n"
    "v_pk_add_f32 v[24:25], v[24:25], v[84:85]\n"
    "v_pk_add_f32 v[46:47], v[46:47], v[66:67]\n"
    "v_pk_add_f32 v[26:27], v[26:27], v[86:87]\n"
    "v_pk_add_f32 v[24:25], v[24:25], v[44:45]\n"
    "v_pk_add_f32 v[26:27], v[26:27], v[46:47]\n"
    "s_waitcnt lgkmcnt(4)\n"
    "v_pk_add_f32 v[48:49], v[48:49], v[68:69]\n"
    "s_waitcnt lgkmcnt(3)\n"
    "v_pk_add_f32 v[28:29], v[28:29], v[88:89]\n"
    "v_pk_add_f32 v[50:51], v[50:51], v[70:71]\n"
    "v_pk_add_f32 v[30:31], v[30:31], v[90:91]\n"
    "v_pk_add_f32 v[28:29], v[28:29], v[48:49]\n"
    "v_pk_add_f32 v[30:31], v[30:31], v[50:51]\n"
    "s_waitcnt lgkmcnt(1)\n"
    "v_pk_add_f32 v[52:53], v[52:53], v[72:73]\n"
    "s_waitcnt lgkmcnt(0)\n"
    "v_pk_add_f32 v[32:33], v[32:33], v[92:93]\n"
    "v_pk_add_f32 v[54:55], v[54:55], v[74:75]\n"
    "v_pk_add_f32 v[34:35], v[34:35], v[94:95]\n"
    "v_pk_add_f32 v[32:33], v[32:33], v[52:53]\n"
    "v_pk_add_f32 v[34:35], v[34:35], v[54:55]\n"
    "s_add_u32 s57, s61, 0x1000\n"
    "v_cvt_pk_bf16_f32 v100, v16, v17\n"
    "v_cvt_pk_bf16_f32 v101, v18, v19\n"
    "buffer_store_dwordx2 v[100:101], v10, s[16:19], s61 offen sc1\n"
    "v_cvt_pk_bf16_f32 v102, v20, v21\n"
    "v_cvt_pk_bf16_f32 v103, v22, v23\n"
    "buffer_store_dwordx2 v[102:103], v10, s[16:19], s61 offen offset:1024 sc1\n"
    "v_cvt_pk_bf16_f32 v104, v24, v25\n"
    "v_cvt_pk_bf16_f32 v105, v26, v27\n"
    "buffer_store_dwordx2 v[104:105], v10, s[16:19], s61 offen offset:2048 sc1\n"
    "v_cvt_pk_bf16_f32 v106, v28, v29\n"
    "v_cvt_pk_bf16_f32 v107, v30, v31\n"
    "buffer_store_dwordx2 v[106:107], v10, s[16:19], s61 offen offset:3072 sc1\n"
    "v_cvt_pk_bf16_f32 v108, v32, v33\n"
    "v_cvt_pk_bf16_f32 v109, v34, v35\n"
    "buffer_store_dwordx2 v[108:109], v10, s[16:19], s57 offen sc1\n"
    "v_pk_add_f32 v[110:111], v[16:17], v[18:19]\n"
    "v_pk_add_f32 v[112:113], v[20:21], v[22:23]\n"
    "v_pk_add_f32 v[110:111], v[110:111], v[112:113]\n"
    "v_pk_add_f32 v[112:113], v[24:25], v[26:27]\n"
    "v_pk_add_f32 v[110:111], v[110:111], v[112:113]\n"
    "v_pk_add_f32 v[112:113], v[28:29], v[30:31]\n"
    "v_pk_add_f32 v[110:111], v[110:111], v[112:113]\n"
    "v_pk_add_f32 v[112:113], v[32:33], v[34:35]\n"
    "v_pk_add_f32 v[110:111], v[110:111], v[112:113]\n"
    "v_add_f32_e32 v110, v110, v111\n"
    "v_mov_b32_e32 v111, v110\n"
    "s_nop 1\n"
    "v_permlane16_swap_b32 v111, v110\n"
    "v_add_f32_e32 v110, v111, v110\n"
    "v_mov_b32_e32 v111, v110\n"
    "s_nop 1\n"
    "v_permlane32_swap_b32 v111, v110\n"
    "v_add_f32_e32 v110, v111, v110\n"
    "v_mul_f32_e32 v110, 0x3c4ccccd, v110\n"
    "v_sub_f32_e32 v112, 0, v110\n"
    "v_mov_b32_e32 v113, v112\n"
    "v_pk_add_f32 v[16:17], v[16:17], v[112:113]\n"
    "v_pk_mul_f32 v[114:115], v[16:17], v[16:17]\n"
    "v_pk_add_f32 v[18:19], v[18:19], v[112:113]\n"
    "v_pk_fma_f32 v[114:115], v[18:19], v[18:19], v[114:115]\n"
    "v_pk_add_f32 v[20:21], v[20:21], v[112:113]\n"
    "v_pk_fma_f32 v[114:115], v[20:21], v[20:21], v[114:115]\n"
    "v_pk_add_f32 v[22:23], v[22:23], v[112:113]\n"
    "v_pk_fma_f32 v[114:115], v[22:23], v[22:23], v[114:115]\n"
    "v_pk_add_f32 v[24:25], v[24:25], v[112:113]\n"
    "v_pk_fma_f32 v[114:115], v[24:25], v[24:25], v[114:115]\n"
    "v_pk_add_f32 v[26:27], v[26:27], v[112:113]\n"
    "v_pk_fma_f32 v[114:115], v[26:27], v[26:27], v[114:115]\n"
    "v_pk_add_f32 v[28:29], v[28:29], v[112:113]\n"
    "v_pk_fma_f32 v[114:115], v[28:29], v[28:29], v[114:115]\n"
    "v_pk_add_f32 v[30:31], v[30:31], v[112:113]\n"
    "v_pk_fma_f32 v[114:115], v[30:31], v[30:31], v[114:115]\n"
    "v_pk_add_f32 v[32:33], v[32:33], v[112:113]\n"
    "v_pk_fma_f32 v[114:115], v[32:33], v[32:33], v[114:115]\n"
    "v_pk_add_f32 v[34:35], v[34:35], v[112:113]\n"
    "v_pk_fma_f32 v[114:115], v[34:35], v[34:35], v[114:115]\n"
    "v_add_f32_e32 v111, v114, v115\n"
    "v_mov_b32_e32 v112, v111\n"
    "s_nop 1\n"
    "v_permlane16_swap_b32 v112, v111\n"
    "v_add_f32_e32 v111, v112, v111\n"
    "v_mov_b32_e32 v112, v111\n"
    "s_nop 1\n"
    "v_permlane32_swap_b32 v112, v111\n"
    "v_add_f32_e32 v111, v112, v111\n"
    "buffer_store_dwordx2 v[110:111], v11, s[16:19], s62 offen sc1\n"
    "v_accvgpr_write_b32 a0, v100\n"
    "v_accvgpr_write_b32 a1, v101\n"
    "v_accvgpr_write_b32 a2, v102\n"
    "v_accvgpr_write_b32 a3, v103\n"
    "v_accvgpr_write_b32 a4, v104\n"
    "v_accvgpr_write_b32 a5, v105\n"
    "v_accvgpr_write_b32 a6, v106\n"
    "v_accvgpr_write_b32 a7, v107\n"
    "v_accvgpr_write_b32 a8, v108\n"
    "v_accvgpr_write_b32 a9, v109\n"
    "s_barrier\n"
    "s_mov_b32 m0, s91\n"
    "s_nop 0\n"
    "buffer_load_dwordx4 v0, s[16:19], s90 offen sc1 lds\n"
    "buffer_load_dwordx4 v0, s[16:19], s90 offen offset:1024 sc1 lds\n"
    "buffer_load_dwordx4 v0, s[16:19], s90 offen offset:2048 sc1 lds\n"
    "buffer_load_dwordx4 v0, s[16:19], s90 offen offset:3072 sc1 lds\n"
    "s_add_u32 m0, m0, 0x1000\n"
    "s_add_u32 s90, s90, 0x1000\n"
    "buffer_load_dwordx4 v0, s[16:19], s90 offen sc1 lds\n"
    "buffer_load_dwordx4 v0, s[16:19], s90 offen offset:1024 sc1 lds\n"
    "buffer_load_dwordx4 v0, s[16:19], s90 offen offset:2048 sc1 lds\n"
    "buffer_load_dwordx4 v0, s[16:19], s90 offen offset:3072 sc1 lds\n"
    "s_add_u32 m0, m0, 0x1000\n"
    "s_add_u32 s90, s90, 0x1000\n"
    "buffer_load_dwordx4 v0, s[16:19], s90 offen sc1 lds\n"
    "buffer_load_dwordx4 v0, s[16:19], s90 offen offset:1024 sc1 lds\n"
    "s_mul_i32 s50, s29, 0x500000\n"
    "s_mul_i32 s55, s32, 40960\n"
    "s_add_u32 s50, s50, s55\n"
    "s_add_u32 s50, s50, 0xaad800\n"
    "s_mul_i32 s51, s29, 0x40000\n"
    "s_add_u32 s51, s51, 0x124d800\n"
    "s_lshl_b32 s55, s33, 10\n"
    "s_add_u32 s52, s51, s55\n"
    "s_lshl_b32 s55, s32, 11\n"
    "s_add_u32 s51, s51, s55\n"
    "s_mul_i32 s53, s32, 108\n"
    "s_add_u32 s53, s53, s37\n"
    "s_lshl_b32 s53, s53, 10\n"
    "s_mul_i32 s55, s29, 0x6c0000\n"
    "s_add_u32 s53, s53, s55\n"
    "s_add_u32 s53, s53, 0x24ad800\n"
    "v_and_b32_e32 v10, 0xf0, v0\n"
    "v_lshrrev_b32_e32 v10, 1, v10\n"
    "v_and_b32_e32 v11, 0x100, v0\n"
    "v_lshl_or_b32 v10, v11, 2, v10\n"
    "v_and_b32_e32 v11, 0x200, v0\n"
    "v_or_b32_e32 v10, v10, v11\n"
    "v_lshrrev_b32_e32 v12, 1, v0\n"
    "v_and_b32_e32 v12, 0x78, v12\n"
    "v_and_b32_e32 v13, 0x300, v0\n"
    "v_or_b32_e32 v12, v12, v13\n"
    "v_and_b32_e32 v8, 0x2f0, v0\n"
    "v_bfe_u32 v13, v0, 8, 1\n"
    "v_mul_u32_u24_e32 v13, 55296, v13\n"
    "v_add_u32_e32 v8, v8, v13\n"
    "v_mov_b32_e32 v43, v0\n"
    "v_add_u32_e32 v44, 0x10000, v0\n"
    "v_add_u32_e32 v45, 0x20000, v0\n"
    "s_mov_b32 s73, s50\n"
    "s_add_u32 s90, s37, 3\n"
    "s_mul_i32 s90, s90, 40960\n"
    "s_mul_i32 s91, s26, 10240\n"
    "s_add_u32 s90, s90, s91\n"
    "s_add_u32 s90, s90, 0x30c000\n"
    "s_add_u32 s91, s91, 0x1e000\n"
    "s_cmp_lt_u32 s38, 4\n"
    "s_cselect_b32 s56, 0x7c000000, 0\n"
    "v_add_u32_e32 v41, s56, v0\n"
    "buffer_load_dwordx2 v[20:21], v10, s[16:19], s51 offen sc1\n"
    "buffer_load_dwordx2 v[22:23], v10, s[16:19], s51 offen offset:128 sc1\n"
    "buffer_load_dwordx2 v[24:25], v10, s[16:19], s51 offen offset:256 sc1\n"
    "buffer_load_dwordx2 v[26:27], v10, s[16:19], s51 offen offset:384 sc1\n"
    "buffer_load_dwordx4 v[96:99], v0, s[16:19], s50 offen sc1\n"
    "buffer_load_dwordx4 v[100:103], v0, s[16:19], s50 offen offset:1024 sc1\n"
    "buffer_load_dwordx4 v[104:107], v0, s[16:19], s50 offen offset:2048 sc1\n"
    "buffer_load_dwordx4 v[108:111], v0, s[16:19], s50 offen offset:3072 sc1\n"
    "s_add_u32 s50, s50, 0x1000\n"
    "buffer_load_dwordx4 v[112:115], v0, s[16:19], s50 offen sc1\n"
    "buffer_load_dwordx4 v[116:119], v0, s[16:19], s50 offen offset:1024 sc1\n"
    "buffer_load_dwordx4 v[120:123], v0, s[16:19], s50 offen offset:2048 sc1\n"
    "buffer_load_dwordx4 v[124:127], v0, s[16:19], s50 offen offset:3072 sc1\n"
    "s_add_u32 s50, s50, 0x1000\n"
    "buffer_load_dwordx4 v[128:131], v0, s[16:19], s50 offen sc1\n"
    "buffer_load_dwordx4 v[132:135], v0, s[16:19], s50 offen offset:1024 sc1\n"
    "buffer_load_dwordx4 v[136:139], v0, s[16:19], s50 offen offset:2048 sc1\n"
    "buffer_load_dwordx4 v[140:143], v0, s[16:19], s50 offen offset:3072 sc1\n"
    "s_add_u32 s50, s50, 0x1000\n"
    "buffer_load_dwordx4 v[144:147], v0, s[16:19], s50 offen sc1\n"
    "buffer_load_dwordx4 v[148:151], v0, s[16:19], s50 offen offset:1024 sc1\n"
    "s_waitcnt vmcnt(16)\n"
    "v_max3_u32 v40, v20, v21, v22\n"
    "s_waitcnt vmcnt(15)\n"
    "v_max3_u32 v40, v40, v23, v24\n"
    "s_waitcnt vmcnt(14)\n"
    "v_max3_u32 v40, v40, v25, v26\n"
    "v_max_u32_e32 v40, v40, v27\n"
    "v_cmp_eq_u32_e32 vcc, -1, v40\n"
    "s_cbranch_vccnz .Lmk_again_51\n"
    ".Lmk_there_52:\n"
    "ds_read_b128 a[24:27], v43\n"
    "ds_read_b128 a[28:31], v43 offset:40960\n"
    "ds_read_b128 a[32:35], v43 offset:1024\n"
    "ds_read_b128 a[36:39], v43 offset:41984\n"
    "ds_read_b128 a[40:43], v43 offset:2048\n"
    "ds_read_b128 a[44:47], v43 offset:43008\n"
    "s_waitcnt vmcnt(13)\n"
    "v_max3_u32 v40, v96, v97, v98\n"
    "v_max_u32_e32 v40, v40, v99\n"
    "s_waitcnt vmcnt(12)\n"
    "v_max3_u32 v40, v40, v100, v101\n"
    "v_max3_u32 v40, v40, v102, v103\n"
    "s_waitcnt vmcnt(11)\n"
    "v_max3_u32 v40, v40, v104, v105\n"
    "v_max3_u32 v40, v40, v106, v107\n"
    "s_waitcnt vmcnt(10)\n"
    "v_max3_u32 v40, v40, v108, v109\n"
    "v_max3_u32 v40, v40, v110, v111\n"
    "v_cmp_eq_u32_e32 vcc, -1, v40\n"
    "s_cbranch_vccnz .Lmk_again_53\n"
    ".Lmk_there_54:\n"
    "s_waitcnt lgkmcnt(4)\n"
    "v_mfma_f32_32x32x16_bf16 v[48:63], a[24:27], v[96:99], 0\n"
    "ds_read_b128 a[48:51], v43 offset:3072\n"
    "buffer_load_dwordx4 v[152:155], v0, s[16:19], s50 offen offset:2048 sc1\n"
    "v_mfma_f32_32x32x16_bf16 v[64:79], a[28:31], v[96:99], 0\n"
    "ds_read_b128 a[52:55], v43 offset:44032\n"
    "s_waitcnt vmcnt(10)\n"
    "v_max3_u32 v40, v112, v113, v114\n"
    "v_max_u32_e32 v40, v40, v115\n"
    "s_waitcnt lgkmcnt(4)\n"
    "v_mfma_f32_32x32x16_bf16 v[48:63], a[32:35], v[100:103], v[48:63]\n"
    "ds_read_b128 a[24:27], v43 offset:4096\n"
    "buffer_load_dwordx4 v[156:159], v0, s[16:19], s50 offen offset:3072 sc1\n"
    "v_mfma_f32_32x32x16_bf16 v[64:79], a[36:39], v[100:103], v[64:79]\n"
    "ds_read_b128 a[28:31], v43 offset:45056\n"
    "s_waitcnt vmcnt(10)\n"
    "v_max3_u32 v40, v40, v116, v117\n"
    "v_max3_u32 v40, v40, v118, v119\n"
    "s_waitcnt lgkmcnt(4)\n"
    "v_mfma_f32_32x32x16_bf16 v[48:63], a[40:43], v[104:107], v[48:63]\n"
    "ds_read_b128 a[32:35], v43 offset:5120\n"
    "s_add_u32 s50, s50, 0x1000\n"
    "buffer_load_dwordx4 v[160:163], v0, s[16:19], s50 offen sc1\n"
    "v_add_f32_e32 v14, v20, v22\n"
    "v_add_f32_e32 v9, v24, v26\n"
    "v_mfma_f32_32x32x16_bf16 v[64:79], a[44:47], v[104:107], v[64:79]\n"
    "ds_read_b128 a[36:39], v43 offset:46080\n"
    "s_waitcnt vmcnt(10)\n"
    "v_max3_u32 v40, v40, v120, v121\n"
    "v_max3_u32 v40, v40, v122, v123\n"
    "v_add_f32_e32 v15, v21, v23\n"
    "v_add_f32_e32 v14, v14, v9\n"
    "v_add_f32_e32 v9, v25, v27\n"
    "v_add_f32_e32 v15, v15, v9\n"
    "s_waitcnt lgkmcnt(4)\n"
    "v_mfma_f32_32x32x16_bf16 v[48:63], a[48:51], v[108:111], v[48:63]\n"
    "ds_read_b128 a[40:43], v43 offset:6144\n"
    "buffer_load_dwordx4 v[164:167], v0, s[16:19], s50 offen offset:1024 sc1\n"
    "v_mov_b32_e32 v9, v14\n"
    "s_nop 1\n"
    "v_permlane32_swap_b32 v9, v14\n"
    "v_mfma_f32_32x32x16_bf16 v[64:79], a[52:55], v[108:111], v[64:79]\n"
    "ds_read_b128 a[44:47], v43 offset:47104\n"
    "s_waitcnt vmcnt(10)\n"
    "v_max3_u32 v40, v40, v124, v125\n"
    "v_max3_u32 v40, v40, v126, v127\n"
    "v_cmp_eq_u32_e32 vcc, -1, v40\n"
    "s_cbranch_vccnz .Lmk_again_55\n"
    ".Lmk_there_56:\n"
    "v_add_f32_e32 v14, v9, v14\n"
    "v_mul_f32_e32 v14, 0x3e000000, v14\n"
    "v_sub_f32_e32 v20, v20, v14\n"
    "v_sub_f32_e32 v22, v22, v14\n"
    "s_waitcnt lgkmcnt(4)\n"
    "v_mfma_f32_32x32x16_bf16 v[48:63], a[24:27], v[112:115], v[48:63]\n"
    "ds_read_b128 a[48:51], v43 offset:7168\n"
    "buffer_load_dwordx4 v[168:171], v0, s[16:19], s50 offen offset:2048 sc1\n"
    "v_sub_f32_e32 v24, v24, v14\n"
    "v_sub_f32_e32 v26, v26, v14\n"
    "v_mfma_f32_32x32x16_bf16 v[64:79], a[28:31], v[112:115], v[64:79]\n"
    "ds_read_b128 a[52:55], v43 offset:48128\n"
    "s_waitcnt vmcnt(10)\n"
    "v_max3_u32 v40, v128, v129, v130\n"
    "v_max_u32_e32 v40, v40, v131\n"
    "v_mul_f32_e32 v9, v20, v20\n"
    "v_fmac_f32_e32 v9, v22, v22\n"
    "v_fmac_f32_e32 v9, v24, v24\n"
    "v_fmac_f32_e32 v9, v26, v26\n"
    "s_waitcnt lgkmcnt(4)\n"
    "v_mfma_f32_32x32x16_bf16 v[48:63], a[32:35], v[116:119], v[48:63]\n"
    "ds_read_b128 a[24:27], v43 offset:8192\n"
    "buffer_load_dwordx4 v[172:175], v0, s[16:19], s50 offen offset:3072 sc1\n"
    "v_fmac_f32_e32 v15, 0x42a00000, v9\n"
    "v_mov_b32_e32 v9, v15\n"
    "v_mfma_f32_32x32x16_bf16 v[64:79], a[36:39], v[116:119], v[64:79]\n"
    "ds_read_b128 a[28:31], v43 offset:49152\n"
    "s_waitcnt vmcnt(10)\n"
    "v_max3_u32 v40, v40, v132, v133\n"
    "v_max3_u32 v40, v40, v134, v135\n"
    "v_permlane32_swap_b32 v9, v15\n"
    "v_add_f32_e32 v15, v9, v15\n"
    "v_mul_f32_e32 v15, 0x3acccccd, v15\n"
    "v_add_f32_e32 v18, 0x3727c5ac, v15\n"
    "s_waitcnt lgkmcnt(4)\n"
    "v_mfma_f32_32x32x16_bf16 v[48:63], a[40:43], v[120:123], v[48:63]\n"
    "ds_read_b128 a[32:35], v43 offset:9216\n"
    "s_add_u32 s50, s50, 0x1000\n"
    "buffer_load_dwordx4 v[176:179], v0, s[16:19], s50 offen sc1\n"
    "v_rsq_f32_e32 v19, v18\n"
    "v_mfma_f32_32x32x16_bf16 v[64:79], a[44:47], v[120:123], v[64:79]\n"
    "ds_read_b128 a[36:39], v43 offset:50176\n"
    "s_waitcnt vmcnt(10)\n"
    "v_max3_u32 v40, v40, v136, v137\n"
    "v_max3_u32 v40, v40, v138, v139\n"
    "v_sqrt_f32_e32 v17, v18\n"
    "v_mul_f32_e32 v18, v19, v19\n"
    "v_mul_f32_e32 v18, v18, v15\n"
    "s_waitcnt lgkmcnt(4)\n"
    "v_mfma_f32_32x32x16_bf16 v[48:63], a[48:51], v[124:127], v[48:63]\n"
    "ds_read_b128 a[40:43], v43 offset:10240\n"
    "buffer_load_dwordx4 v[180:183], v0, s[16:19], s50 offen offset:1024 sc1\n"
    "v_add_f32_e32 v18, 0x3727c5ac, v18\n"
    "v_mfma_f32_32x32x16_bf16 v[64:79], a[52:55], v[124:127], v[64:79]\n"
    "ds_read_b128 a[44:47], v43 offset:51200\n"
    "s_waitcnt vmcnt(10)\n"
    "v_max3_u32 v40, v40, v140, v141\n"
    "v_max3_u32 v40, v40, v142, v143\n"
    "v_cmp_eq_u32_e32 vcc, -1, v40\n"
    "s_cbranch_vccnz .Lmk_again_57\n"
    ".Lmk_there_58:\n"
    "v_rsq_f32_e32 v16, v18\n"
    "v_sqrt_f32_e32 v18, v18\n"
    "s_waitcnt lgkmcnt(4)\n"
    "v_mfma_f32_32x32x16_bf16 v[48:63], a[24:27], v[128:131], v[48:63]\n"
    "ds_read_b128 a[48:51], v43 offset:11264\n"
    "buffer_load_dwordx4 v[184:187], v0, s[16:19], s50 offen offset:2048 sc1\n"
    "v_mul_f32_e32 v16, v16, v19\n"
    "v_mul_f32_e32 v17, v17, v18\n"
    "v_mfma_f32_32x32x16_bf16 v[64:79], a[28:31], v[128:131], v[64:79]\n"
    "ds_read_b128 a[52:55], v43 offset:52224\n"
    "s_waitcnt vmcnt(10)\n"
    "v_max3_u32 v40, v144, v145, v146\n"
    "v_max_u32_e32 v40, v40, v147\n"
    "v_mul_f32_e32 v5, 0xc0135761, v16\n"
    "v_mul_f32_e32 v18, v16, v16\n"
    "v_mul_f32_e32 v6, 0x3d372713, v5\n"
    "v_mul_f32_e32 v6, v6, v18\n"
    "s_waitcnt lgkmcnt(4)\n"
    "v_mfma_f32_32x32x16_bf16 v[48:63], a[32:35], v[132:135], v[48:63]\n"
    "ds_read_b128 a[24:27], v43 offset:12288\n"
    "buffer_load_dwordx4 v[188:191], v0, s[16:19], s50 offen offset:3072 sc1\n"
    "v_cvt_pk_bf16_f32 v36, v14, v14\n"
    "v_lshlrev_b32_e32 v9, 16, v36\n"
    "v_mfma_f32_32x32x16_bf16 v[64:79], a[36:39], v[132:135], v[64:79]\n"
    "ds_read_b128 a[28:31], v43 offset:53248\n"
    "s_waitcnt vmcnt(10)\n"
    "v_max3_u32 v40, v40, v148, v149\n"
    "v_max3_u32 v40, v40, v150, v151\n"
    "v_sub_f32_e32 v9, v14, v9\n"
    "v_cvt_pk_bf16_f32 v37, v9, v9\n"
    "v_cvt_pk_bf16_f32 v38, v17, v17\n"
    "v_lshlrev_b32_e32 v9, 16, v38\n"
    "s_waitcnt lgkmcnt(4)\n"
    "v_mfma_f32_32x32x16_bf16 v[48:63], a[40:43], v[136:139], v[48:63]\n"
    "ds_read_b128 a[32:35], v43 offset:13312\n"
    "s_add_u32 s50, s50, 0x1000\n"
    "buffer_load_dwordx4 v[192:195], v0, s[16:19], s50 offen sc1\n"
    "v_sub_f32_e32 v9, v17, v9\n"
    "v_cvt_pk_bf16_f32 v38, v17, v9\n"
    "v_mfma_f32_32x32x16_bf16 v[64:79], a[44:47], v[136:139], v[64:79]\n"
    "ds_read_b128 a[36:39], v43 offset:54272\n"
    "s_waitcnt vmcnt(10)\n"
    "v_max3_u32 v40, v40, v152, v153\n"
    "v_max3_u32 v40, v40, v154, v155\n"
    "v_and_b32_e32 v39, 0xffff0000, v38\n"
    "v_sub_f32_e32 v9, v9, v39\n"
    "v_cvt_pk_bf16_f32 v39, v9, 0\n"
    "v_cndmask_b32_e64 v36, v36, 0, s[48:49]\n"
    "s_waitcnt lgkmcnt(4)\n"
    "v_mfma_f32_32x32x16_bf16 v[48:63], a[48:51], v[140:143], v[48:63]\n"
    "ds_read_b128 a[40:43], v43 offset:14336\n"
    "buffer_load_dwordx4 v[196:199], v0, s[16:19], s50 offen offset:1024 sc1\n"
    "v_cndmask_b32_e64 v37, v37, 0, s[48:49]\n"
    "v_cndmask_b32_e64 v38, v38, 0, s[48:49]\n"
    "v_mfma_f32_32x32x16_bf16 v[64:79], a[52:55], v[140:143], v[64:79]\n"
    "ds_read_b128 a[44:47], v43 offset:55296\n"
    "s_waitcnt vmcnt(10)\n"
    "v_max3_u32 v40, v40, v156, v157\n"
    "v_max3_u32 v40, v40, v158, v159\n"
    "v_cmp_eq_u32_e32 vcc, -1, v40\n"
    "s_cbranch_vccnz .Lmk_again_59\n"
    ".Lmk_there_60:\n"
    "v_cndmask_b32_e64 v39, v39, 0, s[48:49]\n"
    "s_waitcnt lgkmcnt(4)\n"
    "v_mfma_f32_32x32x16_bf16 v[48:63], a[24:27], v[144:147], v[48:63]\n"
    "ds_read_b128 a[48:51], v43 offset:15360\n"
    "buffer_load_dwordx4 v[200:203], v0, s[16:19], s50 offen offset:2048 sc1\n"
    "v_mfma_f32_32x32x16_bf16 v[64:79], a[28:31], v[144:147], v[64:79]\n"
    "ds_read_b128 a[52:55], v43 offset:56320\n"
    "s_waitcnt vmcnt(10)\n"
    "v_max3_u32 v40, v160, v161, v162\n"
    "v_max_u32_e32 v40, v40, v163\n"
    "s_waitcnt lgkmcnt(4)\n"
    "v_mfma_f32_32x32x16_bf16 v[48:63], a[32:35], v[148:151], v[48:63]\n"
    "ds_read_b128 a[24:27], v43 offset:16384\n"
    "buffer_load_dwordx4 v[204:207], v0, s[16:19], s50 offen offset:3072 sc1\n"
    "v_mfma_f32_32x32x16_bf16 v[64:79], a[36:39], v[148:151], v[64:79]\n"
    "ds_read_b128 a[28:31], v43 offset:57344\n"
    "s_waitcnt vmcnt(10)\n"
    "v_max3_u32 v40, v40, v164, v165\n"
    "v_max3_u32 v40, v40, v166, v167\n"
    "s_waitcnt lgkmcnt(4)\n"
    "v_mfma_f32_32x32x16_bf16 v[48:63], a[40:43], v[152:155], v[48:63]\n"
    "ds_read_b128 a[32:35], v43 offset:17408\n"
    "s_add_u32 s50, s50, 0x1000\n"
    "buffer_load_dwordx4 v[208:211], v0, s[16:19], s50 offen sc1\n"
    "v_mfma_f32_32x32x16_bf16 v[64:79], a[44:47], v[152:155], v[64:79]\n"
    "ds_read_b128 a[36:39], v43 offset:58368\n"
    "s_waitcnt vmcnt(10)\n"
    "v_max3_u32 v40, v40, v168, v169\n"
    "v_max3_u32 v40, v40, v170, v171\n"
    "s_waitcnt lgkmcnt(4)\n"
    "v_mfma_f32_32x32x16_bf16 v[48:63], a[48:51], v[156:159], v[48:63]\n"
    "ds_read_b128 a[40:43], v43 offset:18432\n"
    "buffer_load_dwordx4 v[212:215], v0, s[16:19], s50 offen offset:1024 sc1\n"
    "v_mfma_f32_32x32x16_bf16 v[64:79], a[52:55], v[156:159], v[64:79]\n"
    "ds_read_b128 a[44:47], v43 offset:59392\n"
    "s_waitcnt vmcnt(10)\n"
    "v_max3_u32 v40, v40, v172, v173\n"
    "v_max3_u32 v40, v40, v174, v175\n"
    "v_cmp_eq_u32_e32 vcc, -1, v40\n"
    "s_cbranch_vccnz .Lmk_again_61\n"
    ".Lmk_there_62:\n"
    "s_waitcnt lgkmcnt(4)\n"
    "v_mfma_f32_32x32x16_bf16 v[48:63], a[24:27], v[160:163], v[48:63]\n"
    "ds_read_b128 a[48:51], v43 offset:19456\n"
    "buffer_load_dwordx4 v[216:219], v0, s[16:19], s50 offen offset:2048 sc1\n"
    "v_mfma_f32_32x32x16_bf16 v[64:79], a[28:31], v[160:163], v[64:79]\n"
    "ds_read_b128 a[52:55], v43 offset:60416\n"
    "s_waitcnt vmcnt(10)\n"
    "v_max3_u32 v40, v176, v177, v178\n"
    "v_max_u32_e32 v40, v40, v179\n"
    "s_waitcnt lgkmcnt(4)\n"
    "v_mfma_f32_32x32x16_bf16 v[48:63], a[32:35], v[164:167], v[48:63]\n"
    "ds_read_b128 a[24:27], v43 offset:20480\n"
    "buffer_load_dwordx4 v[220:223], v0, s[16:19], s50 offen offset:3072 sc1\n"
    "v_mfma_f32_32x32x16_bf16 v[64:79], a[36:39], v[164:167], v[64:79]\n"
    "ds_read_b128 a[28:31], v43 offset:61440\n"
    "s_waitcnt vmcnt(10)\n"
    "v_max3_u32 v40, v40, v180, v181\n"
    "v_max3_u32 v40, v40, v182, v183\n"
    "s_waitcnt lgkmcnt(4)\n"
    "v_mfma_f32_32x32x16_bf16 v[48:63], a[40:43], v[168:171], v[48:63]\n"
    "ds_read_b128 a[32:35], v43 offset:21504\n"
    "s_add_u32 s50, s50, 0x1000\n"
    "buffer_load_dwordx4 v[224:227], v0, s[16:19], s50 offen sc1\n"
    "v_mfma_f32_32x32x16_bf16 v[64:79], a[44:47], v[168:171], v[64:79]\n"
    "ds_read_b128 a[36:39], v43 offset:62464\n"
    "s_waitcnt vmcnt(10)\n"
    "v_max3_u32 v40, v40, v184, v185\n"
    "v_max3_u32 v40, v40, v186, v187\n"
    "s_waitcnt lgkmcnt(4)\n"
    "v_mfma_f32_32x32x16_bf16 v[48:63], a[48:51], v[172:175], v[48:63]\n"
    "ds_read_b128 a[40:43], v43 offset:22528\n"
    "buffer_load_dwordx4 v[228:231], v0, s[16:19], s50 offen offset:1024 sc1\n"
    "v_mfma_f32_32x32x16_bf16 v[64:79], a[52:55], v[172:175], v[64:79]\n"
    "ds_read_b128 a[44:47], v43 offset:63488\n"
    "s_waitcnt vmcnt(10)\n"
    "v_max3_u32 v40, v40, v188, v189\n"
    "v_max3_u32 v40, v40, v190, v191\n"
    "v_cmp_eq_u32_e32 vcc, -1, v40\n"
    "s_cbranch_vccnz .Lmk_again_63\n"
    ".Lmk_there_64:\n"
    "s_waitcnt lgkmcnt(4)\n"
    "v_mfma_f32_32x32x16_bf16 v[48:63], a[24:27], v[176:179], v[48:63]\n"
    "ds_read_b128 a[48:51], v43 offset:23552\n"
    "buffer_load_dwordx4 v[232:235], v0, s[16:19], s50 offen offset:2048 sc1\n"
    "v_mfma_f32_32x32x16_bf16 v[64:79], a[28:31], v[176:179], v[64:79]\n"
    "ds_read_b128 a[52:55], v43 offset:64512\n"
    "s_waitcnt vmcnt(10)\n"
    "v_max3_u32 v40, v192, v193, v194\n"
    "v_max_u32_e32 v40, v40, v195\n"
    "s_waitcnt lgkmcnt(4)\n"
    "v_mfma_f32_32x32x16_bf16 v[48:63], a[32:35], v[180:183], v[48:63]\n"
    "ds_read_b128 a[24:27], v43 offset:24576\n"
    "buffer_load_dwordx4 v[236:239], v0, s[16:19], s50 offen offset:3072 sc1\n"
    "v_mfma_f32_32x32x16_bf16 v[64:79], a[36:39], v[180:183], v[64:79]\n"
    "ds_read_b128 a[28:31], v44\n"
    "s_waitcnt vmcnt(10)\n"
    "v_max3_u32 v40, v40, v196, v197\n"
    "v_max3_u32 v40, v40, v198, v199\n"
    "s_waitcnt lgkmcnt(4)\n"
    "v_mfma_f32_32x32x16_bf16 v[48:63], a[40:43], v[184:187], v[48:63]\n"
    "ds_read_b128 a[32:35], v43 offset:25600\n"
    "s_add_u32 s50, s50, 0x1000\n"
    "buffer_load_dwordx4 v[240:243], v0, s[16:19], s50 offen sc1\n"
    "v_mfma_f32_32x32x16_bf16 v[64:79], a[44:47], v[184:187], v[64:79]\n"
    "ds_read_b128 a[36:39], v44 offset:1024\n"
    "s_waitcnt vmcnt(10)\n"
    "v_max3_u32 v40, v40, v200, v201\n"
    "v_max3_u32 v40, v40, v202, v203\n"
    "s_waitcnt lgkmcnt(4)\n"
    "v_mfma_f32_32x32x16_bf16 v[48:63], a[48:51], v[188:191], v[48:63]\n"
    "ds_read_b128 a[40:43], v43 offset:26624\n"
    "buffer_load_dwordx4 v[244:247], v0, s[16:19], s50 offen offset:1024 sc1\n"
    "v_mfma_f32_32x32x16_bf16 v[64:79], a[52:55], v[188:191], v[64:79]\n"
    "ds_read_b128 a[44:47], v44 offset:2048\n"
    "s_waitcnt vmcnt(10)\n"
    "v_max3_u32 v40, v40, v204, v205\n"
    "v_max3_u32 v40, v40, v206, v207\n"
    "v_cmp_eq_u32_e32 vcc, -1, v40\n"
    "s_cbranch_vccnz .Lmk_again_65\n"
    ".Lmk_there_66:\n"
    "s_waitcnt lgkmcnt(4)\n"
    "v_mfma_f32_32x32x16_bf16 v[48:63], a[24:27], v[192:195], v[48:63]\n"
    "ds_read_b128 a[48:51], v43 offset:27648\n"
    "buffer_load_dwordx4 v[248:251], v0, s[16:19], s50 offen offset:2048 sc1\n"
    "v_mfma_f32_32x32x16_bf16 v[64:79], a[28:31], v[192:195], v[64:79]\n"
    "ds_read_b128 a[52:55], v44 offset:3072\n"
    "s_waitcnt vmcnt(10)\n"
    "v_max3_u32 v40, v208, v209, v210\n"
    "v_max_u32_e32 v40, v40, v211\n"
    "s_waitcnt lgkmcnt(4)\n"
    "v_mfma_f32_32x32x16_bf16 v[48:63], a[32:35], v[196:199], v[48:63]\n"
    "ds_read_b128 a[24:27], v43 offset:28672\n"
    "buffer_load_dwordx4 v[252:255], v0, s[16:19], s50 offen offset:3072 sc1\n"
    "v_mfma_f32_32x32x16_bf16 v[64:79], a[36:39], v[196:199], v[64:79]\n"
    "ds_read_b128 a[28:31], v44 offset:4096\n"
    "s_waitcnt vmcnt(10)\n"
    "v_max3_u32 v40, v40, v212, v213\n"
    "v_max3_u32 v40, v40, v214, v215\n"
    "s_waitcnt lgkmcnt(4)\n"
    "v_mfma_f32_32x32x16_bf16 v[48:63], a[40:43], v[200:203], v[48:63]\n"
    "ds_read_b128 a[32:35], v43 offset:29696\n"
    "v_mfma_f32_32x32x16_bf16 v[64:79], a[44:47], v[200:203], v[64:79]\n"
    "ds_read_b128 a[36:39], v44 offset:5120\n"
    "s_waitcnt vmcnt(9)\n"
    "v_max3_u32 v40, v40, v216, v217\n"
    "v_max3_u32 v40, v40, v218, v219\n"
    "s_waitcnt lgkmcnt(4)\n"
    "v_mfma_f32_32x32x16_bf16 v[48:63], a[48:51], v[204:207], v[48:63]\n"
    "ds_read_b128 a[40:43], v43 offset:30720\n"
    "v_mfma_f32_32x32x16_bf16 v[64:79], a[52:55], v[204:207], v[64:79]\n"
    "ds_read_b128 a[44:47], v44 offset:6144\n"
    "s_waitcnt vmcnt(8)\n"
    "v_max3_u32 v40, v40, v220, v221\n"
    "v_max3_u32 v40, v40, v222, v223\n"
    "v_cmp_eq_u32_e32 vcc, -1, v40\n"
    "s_cbranch_vccnz .Lmk_again_67\n"
    ".Lmk_there_68:\n"
    "s_waitcnt lgkmcnt(4)\n"
    "v_mfma_f32_32x32x16_bf16 v[48:63], a[24:27], v[208:211], v[48:63]\n"
    "ds_read_b128 a[48:51], v43 offset:31744\n"
    "v_mfma_f32_32x32x16_bf16 v[64:79], a[28:31], v[208:211], v[64:79]\n"
    "ds_read_b128 a[52:55], v44 offset:7168\n"
    "s_waitcnt vmcnt(7)\n"
    "v_max3_u32 v40, v224, v225, v226\n"
    "v_max_u32_e32 v40, v40, v227\n"
    "s_waitcnt lgkmcnt(4)\n"
    "v_mfma_f32_32x32x16_bf16 v[48:63], a[32:35], v[212:215], v[48:63]\n"
    "ds_read_b128 a[24:27], v43 offset:32768\n"
    "v_mfma_f32_32x32x16_bf16 v[64:79], a[36:39], v[212:215], v[64:79]\n"
    "ds_read_b128 a[28:31], v44 offset:8192\n"
    "s_waitcnt vmcnt(6)\n"
    "v_max3_u32 v40, v40, v228, v229\n"
    "v_max3_u32 v40, v40, v230, v231\n"
    "s_waitcnt lgkmcnt(4)\n"
    "v_mfma_f32_32x32x16_bf16 v[48:63], a[40:43], v[216:219], v[48:63]\n"
    "ds_read_b128 a[32:35], v43 offset:33792\n"
    "v_mfma_f32_32x32x16_bf16 v[64:79], a[44:47], v[216:219], v[64:79]\n"
    "ds_read_b128 a[36:39], v44 offset:9216\n"
    "s_mov_b32 m0, s91\n"
    "s_nop 0\n"
    "buffer_load_dwordx4 v41, s[16:19], s90 offen sc1 lds\n"
    "s_waitcnt vmcnt(6)\n"
    "v_max3_u32 v40, v40, v232, v233\n"
    "v_max3_u32 v40, v40, v234, v235\n"
    "s_waitcnt lgkmcnt(4)\n"
    "v_mfma_f32_32x32x16_bf16 v[48:63], a[48:51], v[220:223], v[48:63]\n"
    "ds_read_b128 a[40:43], v43 offset:34816\n"
    "v_mfma_f32_32x32x16_bf16 v[64:79], a[52:55], v[220:223], v[64:79]\n"
    "ds_read_b128 a[44:47], v44 offset:10240\n"
    "buffer_load_dwordx4 v41, s[16:19], s90 offen offset:1024 sc1 lds\n"
    "s_waitcnt vmcnt(6)\n"
    "v_max3_u32 v40, v40, v236, v237\n"
    "v_max3_u32 v40, v40, v238, v239\n"
    "v_cmp_eq_u32_e32 vcc, -1, v40\n"
    "s_cbranch_vccnz .Lmk_again_69\n"
    ".Lmk_there_70:\n"
    "s_waitcnt lgkmcnt(4)\n"
    "v_mfma_f32_32x32x16_bf16 v[48:63], a[24:27], v[224:227], v[48:63]\n"
    "ds_read_b128 a[48:51], v43 offset:35840\n"
    "v_mfma_f32_32x32x16_bf16 v[64:79], a[28:31], v[224:227], v[64:79]\n"
    "ds_read_b128 a[52:55], v44 offset:11264\n"
    "buffer_load_dwordx4 v41, s[16:19], s90 offen offset:2048 sc1 lds\n"
    "s_waitcnt vmcnt(6)\n"
    "v_max3_u32 v40, v240, v241, v242\n"
    "v_max_u32_e32 v40, v40, v243\n"
    "s_waitcnt lgkmcnt(4)\n"
    "v_mfma_f32_32x32x16_bf16 v[48:63], a[32:35], v[228:231], v[48:63]\n"
    "ds_read_b128 a[24:27], v43 offset:36864\n"
    "v_mfma_f32_32x32x16_bf16 v[64:79], a[36:39], v[228:231], v[64:79]\n"
    "ds_read_b128 a[28:31], v44 offset:12288\n"
    "buffer_load_dwordx4 v41, s[16:19], s90 offen offset:3072 sc1 lds\n"
    "s_waitcnt vmcnt(6)\n"
    "v_max3_u32 v40, v40, v244, v245\n"
    "v_max3_u32 v40, v40, v246, v247\n"
    "s_waitcnt lgkmcnt(4)\n"
    "v_mfma_f32_32x32x16_bf16 v[48:63], a[40:43], v[232:235], v[48:63]\n"
    "ds_read_b128 a[32:35], v43 offset:37888\n"
    "v_mfma_f32_32x32x16_bf16 v[64:79], a[44:47], v[232:235], v[64:79]\n"
    "ds_read_b128 a[36:39], v44 offset:13312\n"
    "s_add_u32 m0, m0, 0x1000\n"
    "s_add_u32 s90, s90, 0x1000\n"
    "buffer_load_dwordx4 v41, s[16:19], s90 offen sc1 lds\n"
    "s_waitcnt vmcnt(6)\n"
    "v_max3_u32 v40, v40, v248, v249\n"
    "v_max3_u32 v40, v40, v250, v251\n"
    "s_waitcnt lgkmcnt(4)\n"
    "v_mfma_f32_32x32x16_bf16 v[48:63], a[48:51], v[236:239], v[48:63]\n"
    "ds_read_b128 a[40:43], v43 offset:38912\n"
    "v_mfma_f32_32x32x16_bf16 v[64:79], a[52:55], v[236:239], v[64:79]\n"
    "ds_read_b128 a[44:47], v44 offset:14336\n"
    "buffer_load_dwordx4 v41, s[16:19], s90 offen offset:1024 sc1 lds\n"
    "s_waitcnt vmcnt(6)\n"
    "v_max3_u32 v40, v40, v252, v253\n"
    "v_max3_u32 v40, v40, v254, v255\n"
    "v_cmp_eq_u32_e32 vcc, -1, v40\n"
    "s_cbranch_vccnz .Lmk_again_71\n"
    ".Lmk_there_72:\n"
    "s_waitcnt lgkmcnt(4)\n"
    "v_mfma_f32_32x32x16_bf16 v[48:63], a[24:27], v[240:243], v[48:63]\n"
    "ds_read_b128 a[48:51], v43 offset:39936\n"
    "v_mfma_f32_32x32x16_bf16 v[64:79], a[28:31], v[240:243], v[64:79]\n"
    "ds_read_b128 a[52:55], v44 offset:15360\n"
    "buffer_load_dwordx4 v41, s[16:19], s90 offen offset:2048 sc1 lds\n"
    "s_waitcnt lgkmcnt(4)\n"
    "v_mfma_f32_32x32x16_bf16 v[48:63], a[32:35], v[244:247], v[48:63]\n"
    "v_mfma_f32_32x32x16_bf16 v[64:79], a[36:39], v[244:247], v[64:79]\n"
    "buffer_load_dwordx4 v41, s[16:19], s90 offen offset:3072 sc1 lds\n"
    "s_waitcnt lgkmcnt(2)\n"
    "v_mfma_f32_32x32x16_bf16 v[48:63], a[40:43], v[248:251], v[48:63]\n"
    "v_mfma_f32_32x32x16_bf16 v[64:79], a[44:47], v[248:251], v[64:79]\n"
    "s_add_u32 m0, m0, 0x1000\n"
    "s_add_u32 s90, s90, 0x1000\n"
    "buffer_load_dwordx4 v41, s[16:19], s90 offen sc1 lds\n"
    "s_waitcnt lgkmcnt(0)\n"
    "v_mfma_f32_32x32x16_bf16 v[48:63], a[48:51], v[252:255], v[48:63]\n"
    "v_mfma_f32_32x32x16_bf16 v[64:79], a[52:55], v[252:255], v[64:79]\n"
    "buffer_load_dwordx4 v41, s[16:19], s90 offen offset:1024 sc1 lds\n"
    "v_accvgpr_read_b32 v20, a14\n"
    "v_accvgpr_read_b32 v21, a14\n"
    "v_accvgpr_read_b32 v22, a18\n"
    "v_accvgpr_read_b32 v23, a18\n"
    "v_and_b32_e32 v23, 0xffff, v23\n"
    "s_nop 1\n"
    "v_mfma_f32_32x32x16_bf16 v[48:63], v[20:23], v[36:39], v[48:63]\n"
    "s_barrier\n"
    "buffer_load_dwordx2 v[32:33], v12, s[16:19], s52 offen sc1\n"
    "buffer_load_dwordx2 v[34:35], v12, s[16:19], s52 offen offset:128 sc1\n"
    "ds_read_b128 a[24:27], v44 offset:16384\n"
    "ds_read_b128 a[28:31], v44 offset:17408\n"
    "ds_read_b128 a[32:35], v44 offset:18432\n"
    "ds_read_b128 a[36:39], v44 offset:19456\n"
    "ds_read_b128 a[40:43], v44 offset:20480\n"
    "v_accvgpr_read_b32 v20, a15\n"
    "v_accvgpr_read_b32 v21, a15\n"
    "v_accvgpr_read_b32 v22, a19\n"
    "v_accvgpr_read_b32 v23, a19\n"
    "v_and_b32_e32 v23, 0xffff, v23\n"
    "s_nop 1\n"
    "v_mfma_f32_32x32x16_bf16 v[64:79], v[20:23], v[36:39], v[64:79]\n"
    "s_add_u32 s74, s26, 0\n"
    "s_and_b32 s74, s74, 3\n"
    "s_sub_u32 s59, s33, s26\n"
    "s_add_u32 s74, s74, s59\n"
    "s_mul_i32 s74, s74, 55296\n"
    "s_mul_i32 s59, s29, 0x6c0000\n"
    "s_add_u32 s74, s74, s59\n"
    "s_add_u32 s74, s74, 0x24ad800\n"
    "s_add_u32 s75, s26, 1\n"
    "s_and_b32 s75, s75, 3\n"
    "s_sub_u32 s59, s33, s26\n"
    "s_add_u32 s75, s75, s59\n"
    "s_mul_i32 s75, s75, 55296\n"
    "s_mul_i32 s59, s29, 0x6c0000\n"
    "s_add_u32 s75, s75, s59\n"
    "s_add_u32 s75, s75, 0x24ad800\n"
    "s_add_u32 s76, s26, 2\n"
    "s_and_b32 s76, s76, 3\n"
    "s_sub_u32 s59, s33, s26\n"
    "s_add_u32 s76, s76, s59\n"
    "s_mul_i32 s76, s76, 55296\n"
    "s_mul_i32 s59, s29, 0x6c0000\n"
    "s_add_u32 s76, s76, s59\n"
    "s_add_u32 s76, s76, 0x24ad800\n"
    "s_add_u32 s77, s26, 3\n"
    "s_and_b32 s77, s77, 3\n"
    "s_sub_u32 s59, s33, s26\n"
    "s_add_u32 s77, s77, s59\n"
    "s_mul_i32 s77, s77, 55296\n"
    "s_mul_i32 s59, s29, 0x6c0000\n"
    "s_add_u32 s77, s77, s59\n"
    "s_add_u32 s77, s77, 0x24ad800\n"
    "s_add_u32 s59, s26, 0\n"
    "s_lshl_b32 s60, s59, 2\n"
    "s_mul_i32 s64, s59, 3\n"
    "s_add_u32 s64, s64, 6\n"
    "s_cmp_lt_u32 s59, 6\n"
    "s_cselect_b32 s60, s60, s64\n"
    "s_lshl_b32 s60, s60, 10\n"
    "s_add_u32 s59, s26, 4\n"
    "s_lshl_b32 s61, s59, 2\n"
    "s_mul_i32 s64, s59, 3\n"
    "s_add_u32 s64, s64, 6\n"
    "s_cmp_lt_u32 s59, 6\n"
    "s_cselect_b32 s61, s61, s64\n"
    "s_lshl_b32 s61, s61, 10\n"
    "s_add_u32 s59, s26, 8\n"
    "s_lshl_b32 s62, s59, 2\n"
    "s_mul_i32 s64, s59, 3\n"
    "s_add_u32 s64, s64, 6\n"
    "s_cmp_lt_u32 s59, 6\n"
    "s_cselect_b32 s62, s62, s64\n"
    "s_lshl_b32 s62, s62, 10\n"
    "s_add_u32 s59, s26, 12\n"
    "s_lshl_b32 s63, s59, 2\n"
    "s_mul_i32 s64, s59, 3\n"
    "s_add_u32 s64, s64, 6\n"
    "s_cmp_lt_u32 s59, 6\n"
    "s_cselect_b32 s63, s63, s64\n"
    "s_lshl_b32 s63, s63, 10\n"
    "s_add_u32 s64, s61, 3072\n"
    "s_cmp_lt_u32 s26, 2\n"
    "s_cselect_b32 s64, s64, s60\n"
    "v_mul_f32_e32 v24, v48, v48\n"
    "v_mul_f32_e32 v25, v49, v49\n"
    "v_mul_f32_e32 v26, v50, v50\n"
    "v_mul_f32_e32 v27, v51, v51\n"
    "v_fma_f32 v24, v24, v6, v5\n"
    "v_fma_f32 v25, v25, v6, v5\n"
    "v_fma_f32 v26, v26, v6, v5\n"
    "v_fma_f32 v27, v27, v6, v5\n"
    "v_mul_f32_e32 v24, v24, v48\n"
    "v_mul_f32_e32 v25, v25, v49\n"
    "v_mul_f32_e32 v26, v26, v50\n"
    "v_mul_f32_e32 v27, v27, v51\n"
    "v_exp_f32_e32 v24, v24\n"
    "v_exp_f32_e32 v25, v25\n"
    "v_exp_f32_e32 v26, v26\n"
    "v_exp_f32_e32 v27, v27\n"
    "s_add_u32 s93, s93, 0x1000\n"
    "buffer_load_dwordx4 a[156:159], v0, s[16:19], s93 offen sc1\n"
    "v_add_f32_e32 v24, 1.0, v24\n"
    "v_add_f32_e32 v25, 1.0, v25\n"
    "v_add_f32_e32 v26, 1.0, v26\n"
    "v_add_f32_e32 v27, 1.0, v27\n"
    "v_rcp_f32_e32 v24, v24\n"
    "v_rcp_f32_e32 v25, v25\n"
    "v_rcp_f32_e32 v26, v26\n"
    "v_rcp_f32_e32 v27, v27\n"
    "v_mul_f32_e32 v48, v24, v48\n"
    "v_mul_f32_e32 v49, v25, v49\n"
    "v_mul_f32_e32 v50, v26, v50\n"
    "v_mul_f32_e32 v51, v27, v51\n"
    "v_cvt_pk_bf16_f32 v48, v48, v49\n"
    "v_cvt_pk_bf16_f32 v49, v50, v51\n"
    "v_mul_f32_e32 v28, v52, v52\n"
    "v_mul_f32_e32 v29, v53, v53\n"
    "buffer_load_dwordx4 a[160:163], v0, s[16:19], s93 offen offset:1024 sc1\n"
    "v_mul_f32_e32 v30, v54, v54\n"
    "v_mul_f32_e32 v31, v55, v55\n"
    "v_fma_f32 v28, v28, v6, v5\n"
    "v_fma_f32 v29, v29, v6, v5\n"
    "v_fma_f32 v30, v30, v6, v5\n"
    "v_fma_f32 v31, v31, v6, v5\n"
    "v_mul_f32_e32 v28, v28, v52\n"
    "v_mul_f32_e32 v29, v29, v53\n"
    "v_mul_f32_e32 v30, v30, v54\n"
    "v_mul_f32_e32 v31, v31, v55\n"
    "v_exp_f32_e32 v28, v28\n"
    "v_exp_f32_e32 v29, v29\n"
    "v_exp_f32_e32 v30, v30\n"
    "v_exp_f32_e32 v31, v31\n"
    "v_add_f32_e32 v28, 1.0, v28\n"
    "v_add_f32_e32 v29, 1.0, v29\n"
    "buffer_load_dwordx4 a[164:167], v0, s[16:19], s93 offen offset:2048 sc1\n"
    "v_add_f32_e32 v30, 1.0, v30\n"
    "v_add_f32_e32 v31, 1.0, v31\n"
    "v_rcp_f32_e32 v28, v28\n"
    "v_rcp_f32_e32 v29, v29\n"
    "v_rcp_f32_e32 v30, v30\n"
    "v_rcp_f32_e32 v31, v31\n"
    "v_mul_f32_e32 v52, v28, v52\n"
    "v_mul_f32_e32 v53, v29, v53\n"
    "v_mul_f32_e32 v54, v30, v54\n"
    "v_mul_f32_e32 v55, v31, v55\n"
    "v_cvt_pk_bf16_f32 v50, v52, v53\n"
    "v_cvt_pk_bf16_f32 v51, v54, v55\n"
    "s_add_u32 s55, s53, 0\n"
    "buffer_store_dwordx4 v[48:51], v8, s[16:19], s55 offen sc1\n"
    "v_mul_f32_e32 v24, v56, v56\n"
    "v_mul_f32_e32 v25, v57, v57\n"
    "buffer_load_dwordx4 a[168:171], v0, s[16:19], s93 offen offset:3072 sc1\n"
    "v_mul_f32_e32 v26, v58, v58\n"
    "v_mul_f32_e32 v27, v59, v59\n"
    "v_fma_f32 v24, v24, v6, v5\n"
    "v_fma_f32 v25, v25, v6, v5\n"
    "v_fma_f32 v26, v26, v6, v5\n"
    "v_fma_f32 v27, v27, v6, v5\n"
    "v_mul_f32_e32 v24, v24, v56\n"
    "v_mul_f32_e32 v25, v25, v57\n"
    "v_mul_f32_e32 v26, v26, v58\n"
    "v_mul_f32_e32 v27, v27, v59\n"
    "v_exp_f32_e32 v24, v24\n"
    "v_exp_f32_e32 v25, v25\n"
    "v_exp_f32_e32 v26, v26\n"
    "v_exp_f32_e32 v27, v27\n"
    "v_add_f32_e32 v24, 1.0, v24\n"
    "v_add_f32_e32 v25, 1.0, v25\n"
    "s_add_u32 s93, s93, 0x1000\n"
    "buffer_load_dwordx4 a[172:175], v0, s[16:19], s93 offen sc1\n"
    "v_add_f32_e32 v26, 1.0, v26\n"
    "v_add_f32_e32 v27, 1.0, v27\n"
    "v_rcp_f32_e32 v24, v24\n"
    "v_rcp_f32_e32 v25, v25\n"
    "v_rcp_f32_e32 v26, v26\n"
    "v_rcp_f32_e32 v27, v27\n"
    "v_mul_f32_e32 v56, v24, v56\n"
    "v_mul_f32_e32 v57, v25, v57\n"
    "v_mul_f32_e32 v58, v26, v58\n"
    "v_mul_f32_e32 v59, v27, v59\n"
    "v_cvt_pk_bf16_f32 v52, v56, v57\n"
    "v_cvt_pk_bf16_f32 v53, v58, v59\n"
    "v_mul_f32_e32 v28, v60, v60\n"
    "v_mul_f32_e32 v29, v61, v61\n"
    "v_mul_f32_e32 v30, v62, v62\n"
    "v_mul_f32_e32 v31, v63, v63\n"
    "buffer_load_dwordx4 a[176:179], v0, s[16:19], s93 offen offset:1024 sc1\n"
    "v_fma_f32 v28, v28, v6, v5\n"
    "v_fma_f32 v29, v29, v6, v5\n"
    "v_fma_f32 v30, v30, v6, v5\n"
    "v_fma_f32 v31, v31, v6, v5\n"
    "v_mul_f32_e32 v28, v28, v60\n"
    "v_mul_f32_e32 v29, v29, v61\n"
    "v_mul_f32_e32 v30, v30, v62\n"
    "v_mul_f32_e32 v31, v31, v63\n"
    "v_exp_f32_e32 v28, v28\n"
    "v_exp_f32_e32 v29, v29\n"
    "v_exp_f32_e32 v30, v30\n"
    "v_exp_f32_e32 v31, v31\n"
    "v_add_f32_e32 v28, 1.0, v28\n"
    "v_add_f32_e32 v29, 1.0, v29\n"
    "v_add_f32_e32 v30, 1.0, v30\n"
    "v_add_f32_e32 v31, 1.0, v31\n"
    "buffer_load_dwordx4 a[180:183], v0, s[16:19], s93 offen offset:2048 sc1\n"
    "v_rcp_f32_e32 v28, v28\n"
    "v_rcp_f32_e32 v29, v29\n"
    "v_rcp_f32_e32 v30, v30\n"
    "v_rcp_f32_e32 v31, v31\n"
    "v_mul_f32_e32 v60, v28, v60\n"
    "v_mul_f32_e32 v61, v29, v61\n"
    "v_mul_f32_e32 v62, v30, v62\n"
    "v_mul_f32_e32 v63, v31, v63\n"
    "v_cvt_pk_bf16_f32 v54, v60, v61\n"
    "v_cvt_pk_bf16_f32 v55, v62, v63\n"
    "buffer_store_dwordx4 v[52:55], v8, s[16:19], s55 offen offset:256 sc1\n"
    "s_waitcnt lgkmcnt(4)\n"
    "v_mfma_f32_32x32x16_bf16 v[80:95], a[24:27], v[96:99], 0\n"
    "ds_read_b128 a[24:27], v44 offset:21504\n"
    "buffer_load_dwordx4 a[184:187], v0, s[16:19], s93 offen offset:3072 sc1\n"
    "v_mul_f32_e32 v24, v64, v64\n"
    "v_mul_f32_e32 v25, v65, v65\n"
    "v_mul_f32_e32 v26, v66, v66\n"
    "v_mul_f32_e32 v27, v67, v67\n"
    "v_fma_f32 v24, v24, v6, v5\n"
    "s_waitcnt lgkmcnt(4)\n"
    "v_mfma_f32_32x32x16_bf16 v[80:95], a[28:31], v[100:103], v[80:95]\n"
    "ds_read_b128 a[28:31], v44 offset:22528\n"
    "v_fma_f32 v25, v25, v6, v5\n"
    "v_fma_f32 v26, v26, v6, v5\n"
    "v_fma_f32 v27, v27, v6, v5\n"
    "v_mul_f32_e32 v24, v24, v64\n"
    "v_mul_f32_e32 v25, v25, v65\n"
    "s_waitcnt lgkmcnt(4)\n"
    "v_mfma_f32_32x32x16_bf16 v[80:95], a[32:35], v[104:107], v[80:95]\n"
    "ds_read_b128 a[32:35], v44 offset:23552\n"
    "s_add_u32 s93, s93, 0x1000\n"
    "buffer_load_dwordx4 a[188:191], v0, s[16:19], s93 offen sc1\n"
    "v_mul_f32_e32 v26, v26, v66\n"
    "v_mul_f32_e32 v27, v27, v67\n"
    "v_exp_f32_e32 v24, v24\n"
    "s_waitcnt lgkmcnt(4)\n"
    "v_mfma_f32_32x32x16_bf16 v[80:95], a[36:39], v[108:111], v[80:95]\n"
    "ds_read_b128 a[36:39], v44 offset:24576\n"
    "v_exp_f32_e32 v25, v25\n"
    "v_exp_f32_e32 v26, v26\n"
    "s_waitcnt lgkmcnt(4)\n"
    "v_mfma_f32_32x32x16_bf16 v[80:95], a[40:43], v[112:115], v[80:95]\n"
    "ds_read_b128 a[40:43], v44 offset:25600\n"
    "buffer_load_dwordx4 a[192:195], v0, s[16:19], s93 offen offset:1024 sc1\n"
    "v_exp_f32_e32 v27, v27\n"
    "v_add_f32_e32 v24, 1.0, v24\n"
    "v_add_f32_e32 v25, 1.0, v25\n"
    "v_add_f32_e32 v26, 1.0, v26\n"
    "s_waitcnt lgkmcnt(4)\n"
    "v_mfma_f32_32x32x16_bf16 v[80:95], a[24:27], v[116:119], v[80:95]\n"
    "ds_read_b128 a[24:27], v44 offset:26624\n"
    "v_add_f32_e32 v27, 1.0, v27\n"
    "v_rcp_f32_e32 v24, v24\n"
    "v_rcp_f32_e32 v25, v25\n"
    "s_waitcnt lgkmcnt(4)\n"
    "v_mfma_f32_32x32x16_bf16 v[80:95], a[28:31], v[120:123], v[80:95]\n"
    "ds_read_b128 a[28:31], v44 offset:27648\n"
    "buffer_load_dwordx4 a[196:199], v0, s[16:19], s93 offen offset:2048 sc1\n"
    "v_rcp_f32_e32 v26, v26\n"
    "v_rcp_f32_e32 v27, v27\n"
    "v_mul_f32_e32 v64, v24, v64\n"
    "s_waitcnt lgkmcnt(4)\n"
    "v_mfma_f32_32x32x16_bf16 v[80:95], a[32:35], v[124:127], v[80:95]\n"
    "ds_read_b128 a[32:35], v44 offset:28672\n"
    "v_mul_f32_e32 v65, v25, v65\n"
    "v_mul_f32_e32 v66, v26, v66\n"
    "v_mul_f32_e32 v67, v27, v67\n"
    "v_cvt_pk_bf16_f32 v64, v64, v65\n"
    "v_cvt_pk_bf16_f32 v65, v66, v67\n"
    "s_waitcnt lgkmcnt(4)\n"
    "v_mfma_f32_32x32x16_bf16 v[80:95], a[36:39], v[128:131], v[80:95]\n"
    "ds_read_b128 a[36:39], v44 offset:29696\n"
    "buffer_load_dwordx4 a[200:203], v0, s[16:19], s93 offen offset:3072 sc1\n"
    "v_mul_f32_e32 v28, v68, v68\n"
    "v_mul_f32_e32 v29, v69, v69\n"
    "v_mul_f32_e32 v30, v70, v70\n"
    "v_mul_f32_e32 v31, v71, v71\n"
    "v_fma_f32 v28, v28, v6, v5\n"
    "s_waitcnt lgkmcnt(4)\n"
    "v_mfma_f32_32x32x16_bf16 v[80:95], a[40:43], v[132:135], v[80:95]\n"
    "ds_read_b128 a[40:43], v44 offset:30720\n"
    "v_fma_f32 v29, v29, v6, v5\n"
    "v_fma_f32 v30, v30, v6, v5\n"
    "v_fma_f32 v31, v31, v6, v5\n"
    "v_mul_f32_e32 v28, v28, v68\n"
    "v_mul_f32_e32 v29, v29, v69\n"
    "s_waitcnt lgkmcnt(4)\n"
    "v_mfma_f32_32x32x16_bf16 v[80:95], a[24:27], v[136:139], v[80:95]\n"
    "ds_read_b128 a[24:27], v44 offset:31744\n"
    "s_add_u32 s93, s93, 0x1000\n"
    "buffer_load_dwordx4 a[204:207], v0, s[16:19], s93 offen sc1\n"
    "v_mul_f32_e32 v30, v30, v70\n"
    "v_mul_f32_e32 v31, v31, v71\n"
    "v_exp_f32_e32 v28, v28\n"
    "s_waitcnt lgkmcnt(4)\n"
    "v_mfma_f32_32x32x16_bf16 v[80:95], a[28:31], v[140:143], v[80:95]\n"
    "ds_read_b128 a[28:31], v44 offset:32768\n"
    "v_exp_f32_e32 v29, v29\n"
    "v_exp_f32_e32 v30, v30\n"
    "s_waitcnt lgkmcnt(4)\n"
    "v_mfma_f32_32x32x16_bf16 v[80:95], a[32:35], v[144:147], v[80:95]\n"
    "ds_read_b128 a[32:35], v44 offset:33792\n"
    "buffer_load_dwordx4 a[208:211], v0, s[16:19], s93 offen offset:1024 sc1\n"
    "v_exp_f32_e32 v31, v31\n"
    "v_add_f32_e32 v28, 1.0, v28\n"
    "v_add_f32_e32 v29, 1.0, v29\n"
    "v_add_f32_e32 v30, 1.0, v30\n"
    "s_waitcnt lgkmcnt(4)\n"
    "v_mfma_f32_32x32x16_bf16 v[80:95], a[36:39], v[148:151], v[80:95]\n"
    "ds_read_b128 a[36:39], v44 offset:34816\n"
    "v_add_f32_e32 v31, 1.0, v31\n"
    "v_rcp_f32_e32 v28, v28\n"
    "v_rcp_f32_e32 v29, v29\n"
    "s_waitcnt lgkmcnt(4)\n"
    "v_mfma_f32_32x32x16_bf16 v[80:95], a[40:43], v[152:155], v[80:95]\n"
    "ds_read_b128 a[40:43], v44 offset:35840\n"
    "buffer_load_dwordx4 a[212:215], v0, s[16:19], s93 offen offset:2048 sc1\n"
    "v_rcp_f32_e32 v30, v30\n"
    "v_rcp_f32_e32 v31, v31\n"
    "v_mul_f32_e32 v68, v28, v68\n"
    "s_waitcnt lgkmcnt(4)\n"
    "v_mfma_f32_32x32x16_bf16 v[80:95], a[24:27], v[156:159], v[80:95]\n"
    "ds_read_b128 a[24:27], v44 offset:36864\n"
    "v_mul_f32_e32 v69, v29, v69\n"
    "v_mul_f32_e32 v70, v30, v70\n"
    "v_mul_f32_e32 v71, v31, v71\n"
    "v_cvt_pk_bf16_f32 v66, v68, v69\n"
    "v_cvt_pk_bf16_f32 v67, v70, v71\n"
    "s_waitcnt lgkmcnt(4)\n"
    "v_mfma_f32_32x32x16_bf16 v[80:95], a[28:31], v[160:163], v[80:95]\n"
    "ds_read_b128 a[28:31], v44 offset:37888\n"
    "buffer_load_dwordx4 a[216:219], v0, s[16:19], s93 offen offset:3072 sc1\n"
    "s_add_u32 s55, s53, 1024\n"
    "buffer_store_dwordx4 v[64:67], v8, s[16:19], s55 offen sc1\n"
    "v_mul_f32_e32 v24, v72, v72\n"
    "v_mul_f32_e32 v25, v73, v73\n"
    "s_waitcnt lgkmcnt(4)\n"
    "v_mfma_f32_32x32x16_bf16 v[80:95], a[32:35], v[164:167], v[80:95]\n"
    "ds_read_b128 a[32:35], v44 offset:38912\n"
    "v_mul_f32_e32 v26, v74, v74\n"
    "v_mul_f32_e32 v27, v75, v75\n"
    "v_fma_f32 v24, v24, v6, v5\n"
    "v_fma_f32 v25, v25, v6, v5\n"
    "v_fma_f32 v26, v26, v6, v5\n"
    "s_waitcnt lgkmcnt(4)\n"
    "v_mfma_f32_32x32x16_bf16 v[80:95], a[36:39], v[168:171], v[80:95]\n"
    "ds_read_b128 a[36:39], v44 offset:39936\n"
    "s_add_u32 s93, s93, 0x1000\n"
    "buffer_load_dwordx4 a[220:223], v0, s[16:19], s93 offen sc1\n"
    "v_fma_f32 v27, v27, v6, v5\n"
    "v_mul_f32_e32 v24, v24, v72\n"
    "v_mul_f32_e32 v25, v25, v73\n"
    "v_mul_f32_e32 v26, v26, v74\n"
    "v_mul_f32_e32 v27, v27, v75\n"
    "s_waitcnt lgkmcnt(4)\n"
    "v_mfma_f32_32x32x16_bf16 v[80:95], a[40:43], v[172:175], v[80:95]\n"
    "ds_read_b128 a[40:43], v44 offset:40960\n"
    "v_exp_f32_e32 v24, v24\n"
    "v_exp_f32_e32 v25, v25\n"
    "s_waitcnt lgkmcnt(4)\n"
    "v_mfma_f32_32x32x16_bf16 v[80:95], a[24:27], v[176:179], v[80:95]\n"
    "ds_read_b128 a[24:27], v44 offset:41984\n"
    "buffer_load_dwordx4 a[224:227], v0, s[16:19], s93 offen offset:1024 sc1\n"
    "v_exp_f32_e32 v26, v26\n"
    "v_exp_f32_e32 v27, v27\n"
    "v_add_f32_e32 v24, 1.0, v24\n"
    "s_waitcnt lgkmcnt(4)\n"
    "v_mfma_f32_32x32x16_bf16 v[80:95], a[28:31], v[180:183], v[80:95]\n"
    "ds_read_b128 a[28:31], v44 offset:43008\n"
    "v_add_f32_e32 v25, 1.0, v25\n"
    "v_add_f32_e32 v26, 1.0, v26\n"
    "v_add_f32_e32 v27, 1.0, v27\n"
    "v_rcp_f32_e32 v24, v24\n"
    "s_waitcnt lgkmcnt(4)\n"
    "v_mfma_f32_32x32x16_bf16 v[80:95], a[32:35], v[184:187], v[80:95]\n"
    "ds_read_b128 a[32:35], v44 offset:44032\n"
    "buffer_load_dwordx4 a[228:231], v0, s[16:19], s93 offen offset:2048 sc1\n"
    "v_rcp_f32_e32 v25, v25\n"
    "v_rcp_f32_e32 v26, v26\n"
    "s_waitcnt lgkmcnt(4)\n"
    "v_mfma_f32_32x32x16_bf16 v[80:95], a[36:39], v[188:191], v[80:95]\n"
    "ds_read_b128 a[36:39], v44 offset:45056\n"
    "v_rcp_f32_e32 v27, v27\n"
    "v_mul_f32_e32 v72, v24, v72\n"
    "v_mul_f32_e32 v73, v25, v73\n"
    "v_mul_f32_e32 v74, v26, v74\n"
    "s_waitcnt lgkmcnt(4)\n"
    "v_mfma_f32_32x32x16_bf16 v[80:95], a[40:43], v[192:195], v[80:95]\n"
    "ds_read_b128 a[40:43], v44 offset:46080\n"
    "buffer_load_dwordx4 a[232:235], v0, s[16:19], s93 offen offset:3072 sc1\n"
    "v_mul_f32_e32 v75, v27, v75\n"
    "v_cvt_pk_bf16_f32 v68, v72, v73\n"
    "v_cvt_pk_bf16_f32 v69, v74, v75\n"
    "v_mul_f32_e32 v28, v76, v76\n"
    "v_mul_f32_e32 v29, v77, v77\n"
    "s_waitcnt lgkmcnt(4)\n"
    "v_mfma_f32_32x32x16_bf16 v[80:95], a[24:27], v[196:199], v[80:95]\n"
    "ds_read_b128 a[24:27], v44 offset:47104\n"
    "v_mul_f32_e32 v30, v78, v78\n"
    "v_mul_f32_e32 v31, v79, v79\n"
    "v_fma_f32 v28, v28, v6, v5\n"
    "v_fma_f32 v29, v29, v6, v5\n"
    "v_fma_f32 v30, v30, v6, v5\n"
    "s_waitcnt lgkmcnt(4)\n"
    "v_mfma_f32_32x32x16_bf16 v[80:95], a[28:31], v[200:203], v[80:95]\n"
    "ds_read_b128 a[28:31], v44 offset:48128\n"
    "s_add_u32 s93, s93, 0x1000\n"
    "buffer_load_dwordx4 a[236:239], v0, s[16:19], s93 offen sc1\n"
    "v_fma_f32 v31, v31, v6, v5\n"
    "v_mul_f32_e32 v28, v28, v76\n"
    "v_mul_f32_e32 v29, v29, v77\n"
    "v_mul_f32_e32 v30, v30, v78\n"
    "v_mul_f32_e32 v31, v31, v79\n"
    "s_waitcnt lgkmcnt(4)\n"
    "v_mfma_f32_32x32x16_bf16 v[80:95], a[32:35], v[204:207], v[80:95]\n"
    "ds_read_b128 a[32:35], v44 offset:49152\n"
    "v_exp_f32_e32 v28, v28\n"
    "v_exp_f32_e32 v29, v29\n"
    "s_waitcnt lgkmcnt(4)\n"
    "v_mfma_f32_32x32x16_bf16 v[80:95], a[36:39], v[208:211], v[80:95]\n"
    "ds_read_b128 a[36:39], v44 offset:50176\n"
    "buffer_load_dwordx4 a[240:243], v0, s[16:19], s93 offen offset:1024 sc1\n"
    "v_exp_f32_e32 v30, v30\n"
    "v_exp_f32_e32 v31, v31\n"
    "v_add_f32_e32 v28, 1.0, v28\n"
    "s_waitcnt lgkmcnt(4)\n"
    "v_mfma_f32_32x32x16_bf16 v[80:95], a[40:43], v[212:215], v[80:95]\n"
    "ds_read_b128 a[40:43], v44 offset:51200\n"
    "v_add_f32_e32 v29, 1.0, v29\n"
    "v_add_f32_e32 v30, 1.0, v30\n"
    "v_add_f32_e32 v31, 1.0, v31\n"
    "v_rcp_f32_e32 v28, v28\n"
    "s_waitcnt lgkmcnt(4)\n"
    "v_mfma_f32_32x32x16_bf16 v[80:95], a[24:27], v[216:219], v[80:95]\n"
    "ds_read_b128 a[24:27], v44 offset:52224\n"
    "buffer_load_dwordx4 a[244:247], v0, s[16:19], s93 offen offset:2048 sc1\n"
    "v_rcp_f32_e32 v29, v29\n"
    "v_rcp_f32_e32 v30, v30\n"
    "s_waitcnt lgkmcnt(4)\n"
    "v_mfma_f32_32x32x16_bf16 v[80:95], a[28:31], v[220:223], v[80:95]\n"
    "ds_read_b128 a[28:31], v44 offset:53248\n"
    "v_rcp_f32_e32 v31, v31\n"
    "v_mul_f32_e32 v76, v28, v76\n"
    "v_mul_f32_e32 v77, v29, v77\n"
    "v_mul_f32_e32 v78, v30, v78\n"
    "s_waitcnt lgkmcnt(4)\n"
    "v_mfma_f32_32x32x16_bf16 v[80:95], a[32:35], v[224:227], v[80:95]\n"
    "ds_read_b128 a[32:35], v44 offset:54272\n"
    "buffer_load_dwordx4 a[248:251], v0, s[16:19], s93 offen offset:3072 sc1\n"
    "v_mul_f32_e32 v79, v31, v79\n"
    "v_cvt_pk_bf16_f32 v70, v76, v77\n"
    "v_cvt_pk_bf16_f32 v71, v78, v79\n"
    "buffer_store_dwordx4 v[68:71], v8, s[16:19], s55 offen offset:256 sc1\n"
    "s_waitcnt lgkmcnt(4)\n"
    "v_mfma_f32_32x32x16_bf16 v[80:95], a[36:39], v[228:231], v[80:95]\n"
    "ds_read_b128 a[36:39], v44 offset:55296\n"
    "s_waitcnt vmcnt(28)\n"
    "v_add_f32_e32 v18, v32, v34\n"
    "v_mov_b32_e32 v19, v18\n"
    "s_nop 1\n"
    "v_permlane16_swap_b32 v19, v18\n"
    "v_add_f32_e32 v18, v19, v18\n"
    "v_mov_b32_e32 v19, v18\n"
    "s_waitcnt lgkmcnt(4)\n"
    "v_mfma_f32_32x32x16_bf16 v[80:95], a[40:43], v[232:235], v[80:95]\n"
    "ds_read_b128 a[40:43], v44 offset:56320\n"
    "s_add_u32 s93, s93, 0x1000\n"
    "buffer_load_dwordx4 a[252:255], v0, s[16:19], s93 offen sc1\n"
    "v_permlane32_swap_b32 v19, v18\n"
    "v_add_f32_e32 v18, v19, v18\n"
    "v_mul_f32_e32 v18, 0x3e000000, v18\n"
    "v_sub_f32_e32 v32, v32, v18\n"
    "v_sub_f32_e32 v34, v34, v18\n"
    "s_waitcnt lgkmcnt(4)\n"
    "v_mfma_f32_32x32x16_bf16 v[80:95], a[24:27], v[236:239], v[80:95]\n"
    "v_mul_f32_e32 v32, v32, v32\n"
    "v_fmac_f32_e32 v32, v34, v34\n"
    "v_add_f32_e32 v33, v33, v35\n"
    "v_fmac_f32_e32 v33, 0x42a00000, v32\n"
    "v_mov_b32_e32 v19, v33\n"
    "s_waitcnt lgkmcnt(3)\n"
    "v_mfma_f32_32x32x16_bf16 v[80:95], a[28:31], v[240:243], v[80:95]\n"
    "buffer_load_dwordx4 a[44:47], v0, s[16:19], s93 offen offset:1024 sc1\n"
    "v_permlane16_swap_b32 v19, v33\n"
    "v_add_f32_e32 v33, v19, v33\n"
    "v_mov_b32_e32 v19, v33\n"
    "s_nop 1\n"
    "v_permlane32_swap_b32 v19, v33\n"
    "v_add_f32_e32 v33, v19, v33\n"
    "s_waitcnt lgkmcnt(2)\n"
    "v_mfma_f32_32x32x16_bf16 v[80:95], a[32:35], v[244:247], v[80:95]\n"
    "v_mul_f32_e32 v33, 0x3acccccd, v33\n"
    "v_add_f32_e32 v18, 0x3727c5ac, v33\n"
    "v_rsq_f32_e32 v19, v18\n"
    "s_nop 0\n"
    "v_mul_f32_e32 v18, v19, v19\n"
    "s_waitcnt lgkmcnt(1)\n"
    "v_mfma_f32_32x32x16_bf16 v[80:95], a[36:39], v[248:251], v[80:95]\n"
    "buffer_load_dwordx4 a[48:51], v0, s[16:19], s93 offen offset:2048 sc1\n"
    "v_mul_f32_e32 v18, v18, v33\n"
    "v_add_f32_e32 v18, 0x3727c5ac, v18\n"
    "v_rsq_f32_e32 v18, v18\n"
    "s_nop 0\n"
    "v_mul_f32_e32 v7, v18, v19\n"
    "s_waitcnt lgkmcnt(0)\n"
    "v_mfma_f32_32x32x16_bf16 v[80:95], a[40:43], v[252:255], v[80:95]\n"
    "v_accvgpr_read_b32 v20, a16\n"
    "v_accvgpr_read_b32 v21, a16\n"
    "v_accvgpr_read_b32 v22, a20\n"
    "v_accvgpr_read_b32 v23, a20\n"
    "v_and_b32_e32 v23, 0xffff, v23\n"
    "s_nop 1\n"
    "v_mfma_f32_32x32x16_bf16 v[80:95], v[20:23], v[36:39], v[80:95]\n"
    "buffer_load_dwordx4 a[52:55], v0, s[16:19], s93 offen offset:3072 sc1\n"
    "s_add_u32 s93, s93, 0x1000\n"
    "buffer_load_dwordx4 a[56:59], v0, s[16:19], s93 offen sc1\n"
    "s_cmp_lt_u32 s38, 4\n"
    "s_cbranch_scc1 .Lmk_nb3_73\n"
    "s_barrier\n"
    "ds_read_b128 a[24:27], v44 offset:57344\n"
    "ds_read_b128 a[28:31], v44 offset:58368\n"
    "ds_read_b128 a[32:35], v44 offset:59392\n"
    "ds_read_b128 a[36:39], v44 offset:60416\n"
    "ds_read_b128 a[40:43], v44 offset:61440\n"
    "s_waitcnt lgkmcnt(4)\n"
    "v_mfma_f32_32x32x16_bf16 v[48:63], a[24:27], v[96:99], 0\n"
    "ds_read_b128 a[24:27], v44 offset:62464\n"
    "v_mul_f32_e32 v24, v80, v80\n"
    "v_mul_f32_e32 v25, v81, v81\n"
    "v_mul_f32_e32 v26, v82, v82\n"
    "v_mul_f32_e32 v27, v83, v83\n"
    "v_fma_f32 v24, v24, v6, v5\n"
    "v_fma_f32 v25, v25, v6, v5\n"
    "s_waitcnt lgkmcnt(4)\n"
    "v_mfma_f32_32x32x16_bf16 v[48:63], a[28:31], v[100:103], v[48:63]\n"
    "ds_read_b128 a[28:31], v44 offset:63488\n"
    "v_fma_f32 v26, v26, v6, v5\n"
    "v_fma_f32 v27, v27, v6, v5\n"
    "v_mul_f32_e32 v24, v24, v80\n"
    "v_mul_f32_e32 v25, v25, v81\n"
    "v_mul_f32_e32 v26, v26, v82\n"
    "v_mul_f32_e32 v27, v27, v83\n"
    "s_waitcnt lgkmcnt(4)\n"
    "v_mfma_f32_32x32x16_bf16 v[48:63], a[32:35], v[104:107], v[48:63]\n"
    "ds_read_b128 a[32:35], v44 offset:64512\n"
    "v_exp_f32_e32 v24, v24\n"
    "v_exp_f32_e32 v25, v25\n"
    "v_exp_f32_e32 v26, v26\n"
    "s_waitcnt lgkmcnt(4)\n"
    "v_mfma_f32_32x32x16_bf16 v[48:63], a[36:39], v[108:111], v[48:63]\n"
    "ds_read_b128 a[36:39], v45\n"
    "v_exp_f32_e32 v27, v27\n"
    "v_add_f32_e32 v24, 1.0, v24\n"
    "v_add_f32_e32 v25, 1.0, v25\n"
    "v_add_f32_e32 v26, 1.0, v26\n"
    "v_add_f32_e32 v27, 1.0, v27\n"
    "s_waitcnt lgkmcnt(4)\n"
    "v_mfma_f32_32x32x16_bf16 v[48:63], a[40:43], v[112:115], v[48:63]\n"
    "ds_read_b128 a[40:43], v45 offset:1024\n"
    "v_rcp_f32_e32 v24, v24\n"
    "v_rcp_f32_e32 v25, v25\n"
    "v_rcp_f32_e32 v26, v26\n"
    "s_waitcnt lgkmcnt(4)\n"
    "v_mfma_f32_32x32x16_bf16 v[48:63], a[24:27], v[116:119], v[48:63]\n"
    "ds_read_b128 a[24:27], v45 offset:2048\n"
    "v_rcp_f32_e32 v27, v27\n"
    "v_mul_f32_e32 v80, v24, v80\n"
    "v_mul_f32_e32 v81, v25, v81\n"
    "v_mul_f32_e32 v82, v26, v82\n"
    "v_mul_f32_e32 v83, v27, v83\n"
    "s_waitcnt lgkmcnt(4)\n"
    "v_mfma_f32_32x32x16_bf16 v[48:63], a[28:31], v[120:123], v[48:63]\n"
    "ds_read_b128 a[28:31], v45 offset:3072\n"
    "v_cvt_pk_bf16_f32 v80, v80, v81\n"
    "v_cvt_pk_bf16_f32 v81, v82, v83\n"
    "v_mul_f32_e32 v28, v84, v84\n"
    "v_mul_f32_e32 v29, v85, v85\n"
    "v_mul_f32_e32 v30, v86, v86\n"
    "v_mul_f32_e32 v31, v87, v87\n"
    "s_waitcnt lgkmcnt(4)\n"
    "v_mfma_f32_32x32x16_bf16 v[48:63], a[32:35], v[124:127], v[48:63]\n"
    "ds_read_b128 a[32:35], v45 offset:4096\n"
    "v_fma_f32 v28, v28, v6, v5\n"
    "v_fma_f32 v29, v29, v6, v5\n"
    "v_fma_f32 v30, v30, v6, v5\n"
    "v_fma_f32 v31, v31, v6, v5\n"
    "v_mul_f32_e32 v28, v28, v84\n"
    "v_mul_f32_e32 v29, v29, v85\n"
    "s_waitcnt lgkmcnt(4)\n"
    "v_mfma_f32_32x32x16_bf16 v[48:63], a[36:39], v[128:131], v[48:63]\n"
    "ds_read_b128 a[36:39], v45 offset:5120\n"
    "v_mul_f32_e32 v30, v30, v86\n"
    "v_mul_f32_e32 v31, v31, v87\n"
    "v_exp_f32_e32 v28, v28\n"
    "v_exp_f32_e32 v29, v29\n"
    "s_waitcnt lgkmcnt(4)\n"
    "v_mfma_f32_32x32x16_bf16 v[48:63], a[40:43], v[132:135], v[48:63]\n"
    "ds_read_b128 a[40:43], v45 offset:6144\n"
    "v_exp_f32_e32 v30, v30\n"
    "v_exp_f32_e32 v31, v31\n"
    "v_add_f32_e32 v28, 1.0, v28\n"
    "v_add_f32_e32 v29, 1.0, v29\n"
    "s_waitcnt lgkmcnt(4)\n"
    "v_mfma_f32_32x32x16_bf16 v[48:63], a[24:27], v[136:139], v[48:63]\n"
    "ds_read_b128 a[24:27], v45 offset:7168\n"
    "v_add_f32_e32 v30, 1.0, v30\n"
    "v_add_f32_e32 v31, 1.0, v31\n"
    "v_rcp_f32_e32 v28, v28\n"
    "v_rcp_f32_e32 v29, v29\n"
    "s_waitcnt lgkmcnt(4)\n"
    "v_mfma_f32_32x32x16_bf16 v[48:63], a[28:31], v[140:143], v[48:63]\n"
    "ds_read_b128 a[28:31], v45 offset:8192\n"
    "v_rcp_f32_e32 v30, v30\n"
    "v_rcp_f32_e32 v31, v31\n"
    "v_mul_f32_e32 v84, v28, v84\n"
    "v_mul_f32_e32 v85, v29, v85\n"
    "s_waitcnt lgkmcnt(4)\n"
    "v_mfma_f32_32x32x16_bf16 v[48:63], a[32:35], v[144:147], v[48:63]\n"
    "ds_read_b128 a[32:35], v45 offset:9216\n"
    "buffer_load_dwordx4 v[96:99], v0, s[16:19], s93 offen offset:1024 sc1\n"
    "v_mul_f32_e32 v86, v30, v86\n"
    "v_mul_f32_e32 v87, v31, v87\n"
    "v_cvt_pk_bf16_f32 v82, v84, v85\n"
    "v_cvt_pk_bf16_f32 v83, v86, v87\n"
    "s_add_u32 s55, s53, 2048\n"
    "s_waitcnt lgkmcnt(4)\n"
    "v_mfma_f32_32x32x16_bf16 v[48:63], a[36:39], v[148:151], v[48:63]\n"
    "ds_read_b128 a[36:39], v45 offset:10240\n"
    "buffer_store_dwordx4 v[80:83], v8, s[16:19], s55 offen sc1\n"
    "v_mul_f32_e32 v24, v88, v88\n"
    "v_mul_f32_e32 v25, v89, v89\n"
    "v_mul_f32_e32 v26, v90, v90\n"
    "v_mul_f32_e32 v27, v91, v91\n"
    "s_waitcnt lgkmcnt(4)\n"
    "v_mfma_f32_32x32x16_bf16 v[48:63], a[40:43], v[152:155], v[48:63]\n"
    "ds_read_b128 a[40:43], v45 offset:11264\n"
    "buffer_load_dwordx4 v[100:103], v0, s[16:19], s93 offen offset:2048 sc1\n"
    "v_fma_f32 v24, v24, v6, v5\n"
    "v_fma_f32 v25, v25, v6, v5\n"
    "v_fma_f32 v26, v26, v6, v5\n"
    "v_fma_f32 v27, v27, v6, v5\n"
    "v_mul_f32_e32 v24, v24, v88\n"
    "v_mul_f32_e32 v25, v25, v89\n"
    "s_waitcnt lgkmcnt(4)\n"
    "v_mfma_f32_32x32x16_bf16 v[48:63], a[24:27], v[156:159], v[48:63]\n"
    "ds_read_b128 a[24:27], v45 offset:12288\n"
    "v_mul_f32_e32 v26, v26, v90\n"
    "v_mul_f32_e32 v27, v27, v91\n"
    "v_exp_f32_e32 v24, v24\n"
    "v_exp_f32_e32 v25, v25\n"
    "s_waitcnt lgkmcnt(4)\n"
    "v_mfma_f32_32x32x16_bf16 v[48:63], a[28:31], v[160:163], v[48:63]\n"
    "ds_read_b128 a[28:31], v45 offset:13312\n"
    "buffer_load_dwordx4 v[104:107], v0, s[16:19], s93 offen offset:3072 sc1\n"
    "v_exp_f32_e32 v26, v26\n"
    "v_exp_f32_e32 v27, v27\n"
    "v_add_f32_e32 v24, 1.0, v24\n"
    "v_add_f32_e32 v25, 1.0, v25\n"
    "s_waitcnt lgkmcnt(4)\n"
    "v_mfma_f32_32x32x16_bf16 v[48:63], a[32:35], v[164:167], v[48:63]\n"
    "ds_read_b128 a[32:35], v45 offset:14336\n"
    "v_add_f32_e32 v26, 1.0, v26\n"
    "v_add_f32_e32 v27, 1.0, v27\n"
    "v_rcp_f32_e32 v24, v24\n"
    "v_rcp_f32_e32 v25, v25\n"
    "s_waitcnt lgkmcnt(4)\n"
    "v_mfma_f32_32x32x16_bf16 v[48:63], a[36:39], v[168:171], v[48:63]\n"
    "ds_read_b128 a[36:39], v45 offset:15360\n"
    "s_add_u32 s93, s93, 0x1000\n"
    "buffer_load_dwordx4 v[108:111], v0, s[16:19], s93 offen sc1\n"
    "v_rcp_f32_e32 v26, v26\n"
    "v_rcp_f32_e32 v27, v27\n"
    "v_mul_f32_e32 v88, v24, v88\n"
    "v_mul_f32_e32 v89, v25, v89\n"
    "s_waitcnt lgkmcnt(4)\n"
    "v_mfma_f32_32x32x16_bf16 v[48:63], a[40:43], v[172:175], v[48:63]\n"
    "ds_read_b128 a[40:43], v45 offset:16384\n"
    "v_mul_f32_e32 v90, v26, v90\n"
    "v_mul_f32_e32 v91, v27, v91\n"
    "v_cvt_pk_bf16_f32 v84, v88, v89\n"
    "v_cvt_pk_bf16_f32 v85, v90, v91\n"
    "v_mul_f32_e32 v28, v92, v92\n"
    "v_mul_f32_e32 v29, v93, v93\n"
    "s_waitcnt lgkmcnt(4)\n"
    "v_mfma_f32_32x32x16_bf16 v[48:63], a[24:27], v[176:179], v[48:63]\n"
    "ds_read_b128 a[24:27], v45 offset:17408\n"
    "buffer_load_dwordx4 v[112:115], v0, s[16:19], s93 offen offset:1024 sc1\n"
    "v_mul_f32_e32 v30, v94, v94\n"
    "v_mul_f32_e32 v31, v95, v95\n"
    "v_fma_f32 v28, v28, v6, v5\n"
    "v_fma_f32 v29, v29, v6, v5\n"
    "v_fma_f32 v30, v30, v6, v5\n"
    "v_fma_f32 v31, v31, v6, v5\n"
    "s_waitcnt lgkmcnt(4)\n"
    "v_mfma_f32_32x32x16_bf16 v[48:63], a[28:31], v[180:183], v[48:63]\n"
    "ds_read_b128 a[28:31], v45 offset:18432\n"
    "v_mul_f32_e32 v28, v28, v92\n"
    "v_mul_f32_e32 v29, v29, v93\n"
    "v_mul_f32_e32 v30, v30, v94\n"
    "v_mul_f32_e32 v31, v31, v95\n"
    "v_exp_f32_e32 v28, v28\n"
    "s_waitcnt lgkmcnt(4)\n"
    "v_mfma_f32_32x32x16_bf16 v[48:63], a[32:35], v[184:187], v[48:63]\n"
    "ds_read_b128 a[32:35], v45 offset:19456\n"
    "buffer_load_dwordx4 v[116:119], v0, s[16:19], s93 offen offset:2048 sc1\n"
    "v_exp_f32_e32 v29, v29\n"
    "v_exp_f32_e32 v30, v30\n"
    "v_exp_f32_e32 v31, v31\n"
    "s_waitcnt lgkmcnt(4)\n"
    "v_mfma_f32_32x32x16_bf16 v[48:63], a[36:39], v[188:191], v[48:63]\n"
    "ds_read_b128 a[36:39], v45 offset:20480\n"
    "v_add_f32_e32 v28, 1.0, v28\n"
    "v_add_f32_e32 v29, 1.0, v29\n"
    "v_add_f32_e32 v30, 1.0, v30\n"
    "v_add_f32_e32 v31, 1.0, v31\n"
    "v_rcp_f32_e32 v28, v28\n"
    "s_waitcnt lgkmcnt(4)\n"
    "v_mfma_f32_32x32x16_bf16 v[48:63], a[40:43], v[192:195], v[48:63]\n"
    "ds_read_b128 a[40:43], v45 offset:21504\n"
    "v_add_u32_e32 v13, s60, v0\n"
    "buffer_load_dwordx4 v[144:147], v13, s[16:19], s74 offen sc1\n"
    "buffer_load_dwordx4 v[120:123], v0, s[16:19], s93 offen offset:3072 sc1\n"
    "v_rcp_f32_e32 v29, v29\n"
    "v_rcp_f32_e32 v30, v30\n"
    "v_rcp_f32_e32 v31, v31\n"
    "s_waitcnt lgkmcnt(4)\n"
    "v_mfma_f32_32x32x16_bf16 v[48:63], a[24:27], v[196:199], v[48:63]\n"
    "ds_read_b128 a[24:27], v45 offset:22528\n"
    "buffer_load_dwordx4 v[148:151], v13, s[16:19], s75 offen sc1\n"
    "v_mul_f32_e32 v92, v28, v92\n"
    "v_mul_f32_e32 v93, v29, v93\n"
    "v_mul_f32_e32 v94, v30, v94\n"
    "v_mul_f32_e32 v95, v31, v95\n"
    "v_cvt_pk_bf16_f32 v86, v92, v93\n"
    "v_cvt_pk_bf16_f32 v87, v94, v95\n"
    "s_waitcnt lgkmcnt(4)\n"
    "v_mfma_f32_32x32x16_bf16 v[48:63], a[28:31], v[200:203], v[48:63]\n"
    "ds_read_b128 a[28:31], v45 offset:23552\n"
    "buffer_load_dwordx4 v[152:155], v13, s[16:19], s76 offen sc1\n"
    "s_add_u32 s93, s93, 0x1000\n"
    "buffer_load_dwordx4 v[124:127], v0, s[16:19], s93 offen sc1\n"
    "buffer_store_dwordx4 v[84:87], v8, s[16:19], s55 offen offset:256 sc1\n"
    "s_waitcnt lgkmcnt(4)\n"
    "v_mfma_f32_32x32x16_bf16 v[48:63], a[32:35], v[204:207], v[48:63]\n"
    "ds_read_b128 a[32:35], v45 offset:24576\n"
    "buffer_load_dwordx4 v[156:159], v13, s[16:19], s77 offen sc1\n"
    "s_waitcnt lgkmcnt(4)\n"
    "v_mfma_f32_32x32x16_bf16 v[48:63], a[36:39], v[208:211], v[48:63]\n"
    "ds_read_b128 a[36:39], v45 offset:25600\n"
    "v_add_u32_e32 v14, s61, v0\n"
    "buffer_load_dwordx4 v[160:163], v14, s[16:19], s74 offen sc1\n"
    "buffer_load_dwordx4 v[128:131], v0, s[16:19], s93 offen offset:1024 sc1\n"
    "s_waitcnt lgkmcnt(4)\n"
    "v_mfma_f32_32x32x16_bf16 v[48:63], a[40:43], v[212:215], v[48:63]\n"
    "ds_read_b128 a[40:43], v45 offset:26624\n"
    "buffer_load_dwordx4 v[164:167], v14, s[16:19], s75 offen sc1\n"
    "s_waitcnt lgkmcnt(4)\n"
    "v_mfma_f32_32x32x16_bf16 v[48:63], a[24:27], v[216:219], v[48:63]\n"
    "ds_read_b128 a[24:27], v45 offset:27648\n"
    "buffer_load_dwordx4 v[168:171], v14, s[16:19], s76 offen sc1\n"
    "buffer_load_dwordx4 v[132:135], v0, s[16:19], s93 offen offset:2048 sc1\n"
    "s_waitcnt lgkmcnt(4)\n"
    "v_mfma_f32_32x32x16_bf16 v[48:63], a[28:31], v[220:223], v[48:63]\n"
    "ds_read_b128 a[28:31], v45 offset:28672\n"
    "buffer_load_dwordx4 v[172:175], v14, s[16:19], s77 offen sc1\n"
    "s_waitcnt lgkmcnt(4)\n"
    "v_mfma_f32_32x32x16_bf16 v[48:63], a[32:35], v[224:227], v[48:63]\n"
    "ds_read_b128 a[32:35], v45 offset:29696\n"
    "v_add_u32_e32 v13, s62, v0\n"
    "buffer_load_dwordx4 v[176:179], v13, s[16:19], s74 offen sc1\n"
    "buffer_load_dwordx4 v[136:139], v0, s[16:19], s93 offen offset:3072 sc1\n"
    "s_waitcnt lgkmcnt(4)\n"
    "v_mfma_f32_32x32x16_bf16 v[48:63], a[36:39], v[228:231], v[48:63]\n"
    "ds_read_b128 a[36:39], v45 offset:30720\n"
    "buffer_load_dwordx4 v[180:183], v13, s[16:19], s75 offen sc1\n"
    "s_waitcnt lgkmcnt(4)\n"
    "v_mfma_f32_32x32x16_bf16 v[48:63], a[40:43], v[232:235], v[48:63]\n"
    "ds_read_b128 a[40:43], v45 offset:31744\n"
    "buffer_load_dwordx4 v[184:187], v13, s[16:19], s76 offen sc1\n"
    "s_add_u32 s93, s93, 0x1000\n"
    "buffer_load_dwordx4 v[140:143], v0, s[16:19], s93 offen sc1\n"
    "s_waitcnt lgkmcnt(4)\n"
    "v_mfma_f32_32x32x16_bf16 v[48:63], a[24:27], v[236:239], v[48:63]\n"
    "buffer_load_dwordx4 v[188:191], v13, s[16:19], s77 offen sc1\n"
    "s_waitcnt lgkmcnt(3)\n"
    "v_mfma_f32_32x32x16_bf16 v[48:63], a[28:31], v[240:243], v[48:63]\n"
    "v_add_u32_e32 v14, s63, v0\n"
    "buffer_load_dwordx4 v[192:195], v14, s[16:19], s74 offen sc1\n"
    "s_waitcnt lgkmcnt(2)\n"
    "v_mfma_f32_32x32x16_bf16 v[48:63], a[32:35], v[244:247], v[48:63]\n"
    "buffer_load_dwordx4 v[196:199], v14, s[16:19], s75 offen sc1\n"
    "s_waitcnt lgkmcnt(1)\n"
    "v_mfma_f32_32x32x16_bf16 v[48:63], a[36:39], v[248:251], v[48:63]\n"
    "buffer_load_dwordx4 v[200:203], v14, s[16:19], s76 offen sc1\n"
    "s_waitcnt lgkmcnt(0)\n"
    "v_mfma_f32_32x32x16_bf16 v[48:63], a[40:43], v[252:255], v[48:63]\n"
    "buffer_load_dwordx4 v[204:207], v14, s[16:19], s77 offen sc1\n"
    "v_accvgpr_read_b32 v20, a17\n"
    "v_accvgpr_read_b32 v21, a17\n"
    "v_accvgpr_read_b32 v22, a21\n"
    "v_accvgpr_read_b32 v23, a21\n"
    "v_and_b32_e32 v23, 0xffff, v23\n"
    "s_nop 1\n"
    "v_mfma_f32_32x32x16_bf16 v[48:63], v[20:23], v[36:39], v[48:63]\n"
    "s_nop 11\n"
    "v_mul_f32_e32 v24, v48, v48\n"
    "v_mul_f32_e32 v25, v49, v49\n"
    "v_mul_f32_e32 v26, v50, v50\n"
    "v_mul_f32_e32 v27, v51, v51\n"
    "v_fma_f32 v24, v24, v6, v5\n"
    "v_fma_f32 v25, v25, v6, v5\n"
    "v_fma_f32 v26, v26, v6, v5\n"
    "v_fma_f32 v27, v27, v6, v5\n"
    "v_mul_f32_e32 v24, v24, v48\n"
    "buffer_load_dwordx4 a[24:27], v0, s[16:19], s93 offen offset:1024 sc1\n"
    "v_mul_f32_e32 v25, v25, v49\n"
    "v_mul_f32_e32 v26, v26, v50\n"
    "v_mul_f32_e32 v27, v27, v51\n"
    "v_exp_f32_e32 v24, v24\n"
    "v_exp_f32_e32 v25, v25\n"
    "v_exp_f32_e32 v26, v26\n"
    "v_exp_f32_e32 v27, v27\n"
    "v_add_f32_e32 v24, 1.0, v24\n"
    "v_add_f32_e32 v25, 1.0, v25\n"
    "buffer_load_dwordx4 a[28:31], v0, s[16:19], s93 offen offset:2048 sc1\n"
    "v_add_f32_e32 v26, 1.0, v26\n"
    "v_add_f32_e32 v27, 1.0, v27\n"
    "v_rcp_f32_e32 v24, v24\n"
    "v_rcp_f32_e32 v25, v25\n"
    "v_rcp_f32_e32 v26, v26\n"
    "v_rcp_f32_e32 v27, v27\n"
    "v_mul_f32_e32 v48, v24, v48\n"
    "v_mul_f32_e32 v49, v25, v49\n"
    "v_mul_f32_e32 v50, v26, v50\n"
    "buffer_load_dwordx4 a[32:35], v0, s[16:19], s93 offen offset:3072 sc1\n"
    "v_mul_f32_e32 v51, v27, v51\n"
    "v_cvt_pk_bf16_f32 v48, v48, v49\n"
    "v_cvt_pk_bf16_f32 v49, v50, v51\n"
    "v_mul_f32_e32 v28, v52, v52\n"
    "v_mul_f32_e32 v29, v53, v53\n"
    "v_mul_f32_e32 v30, v54, v54\n"
    "v_mul_f32_e32 v31, v55, v55\n"
    "v_fma_f32 v28, v28, v6, v5\n"
    "v_fma_f32 v29, v29, v6, v5\n"
    "s_add_u32 s93, s93, 0x1000\n"
    "buffer_load_dwordx4 a[36:39], v0, s[16:19], s93 offen sc1\n"
    "v_fma_f32 v30, v30, v6, v5\n"
    "v_fma_f32 v31, v31, v6, v5\n"
    "v_mul_f32_e32 v28, v28, v52\n"
    "v_mul_f32_e32 v29, v29, v53\n"
    "v_mul_f32_e32 v30, v30, v54\n"
    "v_mul_f32_e32 v31, v31, v55\n"
    "v_exp_f32_e32 v28, v28\n"
    "v_exp_f32_e32 v29, v29\n"
    "v_exp_f32_e32 v30, v30\n"
    "buffer_load_dwordx4 a[40:43], v0, s[16:19], s93 offen offset:1024 sc1\n"
    "v_exp_f32_e32 v31, v31\n"
    "v_add_f32_e32 v28, 1.0, v28\n"
    "v_add_f32_e32 v29, 1.0, v29\n"
    "v_add_f32_e32 v30, 1.0, v30\n"
    "v_add_f32_e32 v31, 1.0, v31\n"
    "v_rcp_f32_e32 v28, v28\n"
    "v_rcp_f32_e32 v29, v29\n"
    "v_rcp_f32_e32 v30, v30\n"
    "v_rcp_f32_e32 v31, v31\n"
    "v_mul_f32_e32 v52, v28, v52\n"
    "v_mul_f32_e32 v53, v29, v53\n"
    "v_mul_f32_e32 v54, v30, v54\n"
    "v_mul_f32_e32 v55, v31, v55\n"
    "v_cvt_pk_bf16_f32 v50, v52, v53\n"
    "v_cvt_pk_bf16_f32 v51, v54, v55\n"
    "s_add_u32 s55, s53, 3072\n"
    "buffer_store_dwordx4 v[48:51], v8, s[16:19], s55 offen sc1\n"
    "v_mul_f32_e32 v24, v56, v56\n"
    "v_mul_f32_e32 v25, v57, v57\n"
    "v_mul_f32_e32 v26, v58, v58\n"
    "v_mul_f32_e32 v27, v59, v59\n"
    "v_fma_f32 v24, v24, v6, v5\n"
    "v_fma_f32 v25, v25, v6, v5\n"
    "v_fma_f32 v26, v26, v6, v5\n"
    "v_fma_f32 v27, v27, v6, v5\n"
    "v_mul_f32_e32 v24, v24, v56\n"
    "v_mul_f32_e32 v25, v25, v57\n"
    "v_mul_f32_e32 v26, v26, v58\n"
    "v_mul_f32_e32 v27, v27, v59\n"
    "v_exp_f32_e32 v24, v24\n"
    "v_exp_f32_e32 v25, v25\n"
    "v_exp_f32_e32 v26, v26\n"
    "v_exp_f32_e32 v27, v27\n"
    "v_add_f32_e32 v24, 1.0, v24\n"
    "v_add_f32_e32 v25, 1.0, v25\n"
    "v_add_f32_e32 v26, 1.0, v26\n"
    "v_add_f32_e32 v27, 1.0, v27\n"
    "v_rcp_f32_e32 v24, v24\n"
    "v_rcp_f32_e32 v25, v25\n"
    "v_rcp_f32_e32 v26, v26\n"
    "v_rcp_f32_e32 v27, v27\n"
    "v_mul_f32_e32 v56, v24, v56\n"
    "v_mul_f32_e32 v57, v25, v57\n"
    "v_mul_f32_e32 v58, v26, v58\n"
    "v_mul_f32_e32 v59, v27, v59\n"
    "v_cvt_pk_bf16_f32 v52, v56, v57\n"
    "v_cvt_pk_bf16_f32 v53, v58, v59\n"
    "v_mul_f32_e32 v28, v60, v60\n"
    "v_mul_f32_e32 v29, v61, v61\n"
    "v_mul_f32_e32 v30, v62, v62\n"
    "v_mul_f32_e32 v31, v63, v63\n"
    "v_fma_f32 v28, v28, v6, v5\n"
    "v_fma_f32 v29, v29, v6, v5\n"
    "v_fma_f32 v30, v30, v6, v5\n"
    "v_fma_f32 v31, v31, v6, v5\n"
    "v_mul_f32_e32 v28, v28, v60\n"
    "v_mul_f32_e32 v29, v29, v61\n"
    "v_mul_f32_e32 v30, v30, v62\n"
    "v_mul_f32_e32 v31, v31, v63\n"
    "v_exp_f32_e32 v28, v28\n"
    "v_exp_f32_e32 v29, v29\n"
    "v_exp_f32_e32 v30, v30\n"
    "v_exp_f32_e32 v31, v31\n"
    "v_add_f32_e32 v28, 1.0, v28\n"
    "v_add_f32_e32 v29, 1.0, v29\n"
    "v_add_f32_e32 v30, 1.0, v30\n"
    "v_add_f32_e32 v31, 1.0, v31\n"
    "v_rcp_f32_e32 v28, v28\n"
    "v_rcp_f32_e32 v29, v29\n"
    "v_rcp_f32_e32 v30, v30\n"
    "v_rcp_f32_e32 v31, v31\n"
    "v_mul_f32_e32 v60, v28, v60\n"
    "v_mul_f32_e32 v61, v29, v61\n"
    "v_mul_f32_e32 v62, v30, v62\n"
    "v_mul_f32_e32 v63, v31, v63\n"
    "v_cvt_pk_bf16_f32 v54, v60, v61\n"
    "v_cvt_pk_bf16_f32 v55, v62, v63\n"
    "buffer_store_dwordx4 v[52:55], v8, s[16:19], s55 offen offset:256 sc1\n"
    "s_branch .Lmk_nbj_74\n"
    ".Lmk_nb3_73:\n"
    "s_nop 6\n"
    "v_mul_f32_e32 v24, v80, v80\n"
    "v_mul_f32_e32 v25, v81, v81\n"
    "v_mul_f32_e32 v26, v82, v82\n"
    "v_mul_f32_e32 v27, v83, v83\n"
    "v_fma_f32 v24, v24, v6, v5\n"
    "v_fma_f32 v25, v25, v6, v5\n"
    "v_fma_f32 v26, v26, v6, v5\n"
    "v_fma_f32 v27, v27, v6, v5\n"
    "v_mul_f32_e32 v24, v24, v80\n"
    "buffer_load_dwordx4 v[96:99], v0, s[16:19], s93 offen offset:1024 sc1\n"
    "v_mul_f32_e32 v25, v25, v81\n"
    "v_mul_f32_e32 v26, v26, v82\n"
    "v_mul_f32_e32 v27, v27, v83\n"
    "v_exp_f32_e32 v24, v24\n"
    "v_exp_f32_e32 v25, v25\n"
    "v_exp_f32_e32 v26, v26\n"
    "v_exp_f32_e32 v27, v27\n"
    "v_add_f32_e32 v24, 1.0, v24\n"
    "v_add_f32_e32 v25, 1.0, v25\n"
    "buffer_load_dwordx4 v[100:103], v0, s[16:19], s93 offen offset:2048 sc1\n"
    "v_add_f32_e32 v26, 1.0, v26\n"
    "v_add_f32_e32 v27, 1.0, v27\n"
    "v_rcp_f32_e32 v24, v24\n"
    "v_rcp_f32_e32 v25, v25\n"
    "v_rcp_f32_e32 v26, v26\n"
    "v_rcp_f32_e32 v27, v27\n"
    "v_mul_f32_e32 v80, v24, v80\n"
    "v_mul_f32_e32 v81, v25, v81\n"
    "v_mul_f32_e32 v82, v26, v82\n"
    "buffer_load_dwordx4 v[104:107], v0, s[16:19], s93 offen offset:3072 sc1\n"
    "v_mul_f32_e32 v83, v27, v83\n"
    "v_cvt_pk_bf16_f32 v80, v80, v81\n"
    "v_cvt_pk_bf16_f32 v81, v82, v83\n"
    "v_mul_f32_e32 v28, v84, v84\n"
    "v_mul_f32_e32 v29, v85, v85\n"
    "v_mul_f32_e32 v30, v86, v86\n"
    "v_mul_f32_e32 v31, v87, v87\n"
    "v_fma_f32 v28, v28, v6, v5\n"
    "v_fma_f32 v29, v29, v6, v5\n"
    "s_add_u32 s93, s93, 0x1000\n"
    "buffer_load_dwordx4 v[108:111], v0, s[16:19], s93 offen sc1\n"
    "v_fma_f32 v30, v30, v6, v5\n"
    "v_fma_f32 v31, v31, v6, v5\n"
    "v_mul_f32_e32 v28, v28, v84\n"
    "v_mul_f32_e32 v29, v29, v85\n"
    "v_mul_f32_e32 v30, v30, v86\n"
    "v_mul_f32_e32 v31, v31, v87\n"
    "v_exp_f32_e32 v28, v28\n"
    "v_exp_f32_e32 v29, v29\n"
    "v_exp_f32_e32 v30, v30\n"
    "buffer_load_dwordx4 v[112:115], v0, s[16:19], s93 offen offset:1024 sc1\n"
    "v_exp_f32_e32 v31, v31\n"
    "v_add_f32_e32 v28, 1.0, v28\n"
    "v_add_f32_e32 v29, 1.0, v29\n"
    "v_add_f32_e32 v30, 1.0, v30\n"
    "v_add_f32_e32 v31, 1.0, v31\n"
    "v_rcp_f32_e32 v28, v28\n"
    "v_rcp_f32_e32 v29, v29\n"
    "v_rcp_f32_e32 v30, v30\n"
    "v_rcp_f32_e32 v31, v31\n"
    "buffer_load_dwordx4 v[116:119], v0, s[16:19], s93 offen offset:2048 sc1\n"
    "v_mul_f32_e32 v84, v28, v84\n"
    "v_mul_f32_e32 v85, v29, v85\n"
    "v_mul_f32_e32 v86, v30, v86\n"
    "v_mul_f32_e32 v87, v31, v87\n"
    "v_cvt_pk_bf16_f32 v82, v84, v85\n"
    "v_cvt_pk_bf16_f32 v83, v86, v87\n"
    "s_add_u32 s55, s53, 2048\n"
    "buffer_store_dwordx4 v[80:83], v8, s[16:19], s55 offen sc1\n"
    "v_mul_f32_e32 v24, v88, v88\n"
    "buffer_load_dwordx4 v[120:123], v0, s[16:19], s93 offen offset:3072 sc1\n"
    "v_mul_f32_e32 v25, v89, v89\n"
    "v_mul_f32_e32 v26, v90, v90\n"
    "v_mul_f32_e32 v27, v91, v91\n"
    "v_fma_f32 v24, v24, v6, v5\n"
    "v_fma_f32 v25, v25, v6, v5\n"
    "v_fma_f32 v26, v26, v6, v5\n"
    "v_fma_f32 v27, v27, v6, v5\n"
    "v_mul_f32_e32 v24, v24, v88\n"
    "v_mul_f32_e32 v25, v25, v89\n"
    "s_add_u32 s93, s93, 0x1000\n"
    "buffer_load_dwordx4 v[124:127], v0, s[16:19], s93 offen sc1\n"
    "v_mul_f32_e32 v26, v26, v90\n"
    "v_mul_f32_e32 v27, v27, v91\n"
    "v_exp_f32_e32 v24, v24\n"
    "v_exp_f32_e32 v25, v25\n"
    "v_exp_f32_e32 v26, v26\n"
    "v_exp_f32_e32 v27, v27\n"
    "v_add_f32_e32 v24, 1.0, v24\n"
    "v_add_f32_e32 v25, 1.0, v25\n"
    "v_add_f32_e32 v26, 1.0, v26\n"
    "buffer_load_dwordx4 v[128:131], v0, s[16:19], s93 offen offset:1024 sc1\n"
    "v_add_f32_e32 v27, 1.0, v27\n"
    "v_rcp_f32_e32 v24, v24\n"
    "v_rcp_f32_e32 v25, v25\n"
    "v_rcp_f32_e32 v26, v26\n"
    "v_rcp_f32_e32 v27, v27\n"
    "v_mul_f32_e32 v88, v24, v88\n"
    "v_mul_f32_e32 v89, v25, v89\n"
    "v_mul_f32_e32 v90, v26, v90\n"
    "v_mul_f32_e32 v91, v27, v91\n"
    "buffer_load_dwordx4 v[132:135], v0, s[16:19], s93 offen offset:2048 sc1\n"
    "v_cvt_pk_bf16_f32 v84, v88, v89\n"
    "v_cvt_pk_bf16_f32 v85, v90, v91\n"
    "v_mul_f32_e32 v28, v92, v92\n"
    "v_mul_f32_e32 v29, v93, v93\n"
    "v_mul_f32_e32 v30, v94, v94\n"
    "v_mul_f32_e32 v31, v95, v95\n"
    "v_fma_f32 v28, v28, v6, v5\n"
    "v_fma_f32 v29, v29, v6, v5\n"
    "v_fma_f32 v30, v30, v6, v5\n"
    "buffer_load_dwordx4 v[136:139], v0, s[16:19], s93 offen offset:3072 sc1\n"
    "v_fma_f32 v31, v31, v6, v5\n"
    "v_mul_f32_e32 v28, v28, v92\n"
    "v_mul_f32_e32 v29, v29, v93\n"
    "v_mul_f32_e32 v30, v30, v94\n"
    "v_mul_f32_e32 v31, v31, v95\n"
    "v_exp_f32_e32 v28, v28\n"
    "v_exp_f32_e32 v29, v29\n"
    "v_exp_f32_e32 v30, v30\n"
    "v_exp_f32_e32 v31, v31\n"
    "s_add_u32 s93, s93, 0x1000\n"
    "buffer_load_dwordx4 v[140:143], v0, s[16:19], s93 offen sc1\n"
    "v_add_f32_e32 v28, 1.0, v28\n"
    "v_add_f32_e32 v29, 1.0, v29\n"
    "v_add_f32_e32 v30, 1.0, v30\n"
    "v_add_f32_e32 v31, 1.0, v31\n"
    "v_rcp_f32_e32 v28, v28\n"
    "v_rcp_f32_e32 v29, v29\n"
    "v_rcp_f32_e32 v30, v30\n"
    "v_rcp_f32_e32 v31, v31\n"
    "v_mul_f32_e32 v92, v28, v92\n"
    "buffer_load_dwordx4 a[24:27], v0, s[16:19], s93 offen offset:1024 sc1\n"
    "v_mul_f32_e32 v93, v29, v93\n"
    "v_mul_f32_e32 v94, v30, v94\n"
    "v_mul_f32_e32 v95, v31, v95\n"
    "v_cvt_pk_bf16_f32 v86, v92, v93\n"
    "v_cvt_pk_bf16_f32 v87, v94, v95\n"
    "buffer_store_dwordx4 v[84:87], v8, s[16:19], s55 offen offset:256 sc1\n"
    "buffer_load_dwordx4 a[28:31], v0, s[16:19], s93 offen offset:2048 sc1\n"
    "buffer_load_dwordx4 a[32:35], v0, s[16:19], s93 offen offset:3072 sc1\n"
    "s_add_u32 s93, s93, 0x1000\n"
    "buffer_load_dwordx4 a[36:39], v0, s[16:19], s93 offen sc1\n"
    "buffer_load_dwordx4 a[40:43], v0, s[16:19], s93 offen offset:1024 sc1\n"
    "v_add_u32_e32 v13, s60, v0\n"
    "buffer_load_dwordx4 v[144:147], v13, s[16:19], s74 offen sc1\n"
    "buffer_load_dwordx4 v[148:151], v13, s[16:19], s75 offen sc1\n"
    "buffer_load_dwordx4 v[152:155], v13, s[16:19], s76 offen sc1\n"
    "buffer_load_dwordx4 v[156:159], v13, s[16:19], s77 offen sc1\n"
    "v_add_u32_e32 v14, s61, v0\n"
    "buffer_load_dwordx4 v[160:163], v14, s[16:19], s74 offen sc1\n"
    "buffer_load_dwordx4 v[164:167], v14, s[16:19], s75 offen sc1\n"
    "buffer_load_dwordx4 v[168:171], v14, s[16:19], s76 offen sc1\n"
    "buffer_load_dwordx4 v[172:175], v14, s[16:19], s77 offen sc1\n"
    "v_add_u32_e32 v13, s62, v0\n"
    "buffer_load_dwordx4 v[176:179], v13, s[16:19], s74 offen sc1\n"
    "buffer_load_dwordx4 v[180:183], v13, s[16:19], s75 offen sc1\n"
    "buffer_load_dwordx4 v[184:187], v13, s[16:19], s76 offen sc1\n"
    "buffer_load_dwordx4 v[188:191], v13, s[16:19], s77 offen sc1\n"
    "v_add_u32_e32 v14, s63, v0\n"
    "buffer_load_dwordx4 v[192:195], v14, s[16:19], s74 offen sc1\n"
    "buffer_load_dwordx4 v[196:199], v14, s[16:19], s75 offen sc1\n"
    "buffer_load_dwordx4 v[200:203], v14, s[16:19], s76 offen sc1\n"
    "buffer_load_dwordx4 v[204:207], v14, s[16:19], s77 offen sc1\n"
    ".Lmk_nbj_74:\n"
    "s_barrier\n"
    "s_mul_i32 s90, s28, 0x25800\n"
    "s_add_u32 s90, s90, 0xb8000\n"
    "s_mul_i32 s91, s26, 10240\n"
    "s_add_u32 s90, s90, s91\n"
    "s_add_u32 s91, s91, 0x14000\n"
    "s_mul_i32 s92, s28, 0x25800\n"
    "s_add_u32 s92, s92, 0xc2000\n"
    "s_mul_i32 s93, s26, 7680\n"
    "s_add_u32 s92, s92, s93\n"
    "s_add_u32 s93, s93, 0x1e000\n"
    "s_cmp_eq_u32 s29, 0\n"
    "s_cselect_b32 s51, 0, 0x7c000000\n"
    "v_add_u32_e32 v241, s51, v0\n"
    "s_waitcnt vmcnt(15)\n"
    "v_max3_u32 v240, v144, v145, v146\n"
    "s_waitcnt vmcnt(14)\n"
    "v_max3_u32 v240, v240, v147, v148\n"
    "v_max3_u32 v240, v240, v149, v150\n"
    "s_waitcnt vmcnt(13)\n"
    "v_max3_u32 v240, v240, v151, v152\n"
    "v_max3_u32 v240, v240, v153, v154\n"
    "s_waitcnt vmcnt(12)\n"
    "v_max3_u32 v240, v240, v155, v156\n"
    "v_max3_u32 v240, v240, v157, v158\n"
    "v_max_u32_e32 v240, v240, v159\n"
    "v_cmp_eq_u32_e32 vcc, -1, v240\n"
    "s_cbranch_vccnz .Lmk_hre_75\n"
    ".Lmk_hok_76:\n"
    "v_mfma_f32_16x16x32_bf16 v[36:39], a[60:63], v[148:151], 0\n"
    "s_add_u32 s67, s60, 1024\n"
    "v_add_u32_e32 v13, s67, v0\n"
    "buffer_load_dwordx4 v[208:211], v13, s[16:19], s74 offen sc1\n"
    "v_mfma_f32_16x16x32_bf16 v[40:43], a[64:67], v[148:151], 0\n"
    "v_mfma_f32_16x16x32_bf16 v[44:47], a[68:71], v[148:151], 0\n"
    "buffer_load_dwordx4 v[212:215], v13, s[16:19], s75 offen sc1\n"
    "v_mfma_f32_16x16x32_bf16 v[48:51], a[72:75], v[148:151], 0\n"
    "v_mfma_f32_16x16x32_bf16 v[52:55], a[76:79], v[148:151], 0\n"
    "buffer_load_dwordx4 v[216:219], v13, s[16:19], s76 offen sc1\n"
    "v_mfma_f32_16x16x32_bf16 v[56:59], a[60:63], v[152:155], 0\n"
    "v_mfma_f32_16x16x32_bf16 v[60:63], a[64:67], v[152:155], 0\n"
    "buffer_load_dwordx4 v[220:223], v13, s[16:19], s77 offen sc1\n"
    "v_mfma_f32_16x16x32_bf16 v[64:67], a[68:71], v[152:155], 0\n"
    "v_mfma_f32_16x16x32_bf16 v[68:71], a[72:75], v[152:155], 0\n"
    "s_add_u32 s67, s61, 1024\n"
    "v_add_u32_e32 v14, s67, v0\n"
    "buffer_load_dwordx4 v[224:227], v14, s[16:19], s74 offen sc1\n"
    "v_mfma_f32_16x16x32_bf16 v[72:75], a[76:79], v[152:155], 0\n"
    "v_mfma_f32_16x16x32_bf16 v[76:79], a[60:63], v[156:159], 0\n"
    "s_waitcnt vmcnt(16)\n"
    "v_max3_u32 v240, v160, v161, v162\n"
    "buffer_load_dwordx4 v[228:231], v14, s[16:19], s75 offen sc1\n"
    "v_mfma_f32_16x16x32_bf16 v[80:83], a[64:67], v[156:159], 0\n"
    "s_waitcnt vmcnt(16)\n"
    "v_max3_u32 v240, v240, v163, v164\n"
    "v_mfma_f32_16x16x32_bf16 v[84:87], a[68:71], v[156:159], 0\n"
    "v_max3_u32 v240, v240, v165, v166\n"
    "buffer_load_dwordx4 v[232:235], v14, s[16:19], s76 offen sc1\n"
    "v_mfma_f32_16x16x32_bf16 v[88:91], a[72:75], v[156:159], 0\n"
    "s_waitcnt vmcnt(16)\n"
    "v_max3_u32 v240, v240, v167, v168\n"
    "v_mfma_f32_16x16x32_bf16 v[92:95], a[76:79], v[156:159], 0\n"
    "v_max3_u32 v240, v240, v169, v170\n"
    "buffer_load_dwordx4 v[236:239], v14, s[16:19], s77 offen sc1\n"
    "v_mfma_f32_16x16x32_bf16 v[16:19], a[60:63], v[144:147], 0\n"
    "s_waitcnt vmcnt(16)\n"
    "v_max3_u32 v240, v240, v171, v172\n"
    "v_mfma_f32_16x16x32_bf16 v[20:23], a[64:67], v[144:147], 0\n"
    "v_max3_u32 v240, v240, v173, v174\n"
    "v_mfma_f32_16x16x32_bf16 v[24:27], a[68:71], v[144:147], 0\n"
    "v_max_u32_e32 v240, v240, v175\n"
    "v_mfma_f32_16x16x32_bf16 v[28:31], a[72:75], v[144:147], 0\n"
    "v_cmp_eq_u32_e32 vcc, -1, v240\n"
    "v_mfma_f32_16x16x32_bf16 v[32:35], a[76:79], v[144:147], 0\n"
    "s_cbranch_vccnz .Lmk_hre_77\n"
    ".Lmk_hok_78:\n"
    "v_mfma_f32_16x16x32_bf16 v[36:39], a[80:83], v[164:167], v[36:39]\n"
    "s_add_u32 s67, s62, 1024\n"
    "v_add_u32_e32 v13, s67, v0\n"
    "buffer_load_dwordx4 v[144:147], v13, s[16:19], s74 offen sc1\n"
    "v_mfma_f32_16x16x32_bf16 v[40:43], a[84:87], v[164:167], v[40:43]\n"
    "v_mfma_f32_16x16x32_bf16 v[44:47], a[88:91], v[164:167], v[44:47]\n"
    "v_mfma_f32_16x16x32_bf16 v[48:51], a[92:95], v[164:167], v[48:51]\n"
    "v_mfma_f32_16x16x32_bf16 v[52:55], a[96:99], v[164:167], v[52:55]\n"
    "buffer_load_dwordx4 v[148:151], v13, s[16:19], s75 offen sc1\n"
    "v_mfma_f32_16x16x32_bf16 v[56:59], a[80:83], v[168:171], v[56:59]\n"
    "v_mfma_f32_16x16x32_bf16 v[60:63], a[84:87], v[168:171], v[60:63]\n"
    "v_mfma_f32_16x16x32_bf16 v[64:67], a[88:91], v[168:171], v[64:67]\n"
    "v_mfma_f32_16x16x32_bf16 v[68:71], a[92:95], v[168:171], v[68:71]\n"
    "buffer_load_dwordx4 v[152:155], v13, s[16:19], s76 offen sc1\n"
    "v_mfma_f32_16x16x32_bf16 v[72:75], a[96:99], v[168:171], v[72:75]\n"
    "v_mfma_f32_16x16x32_bf16 v[76:79], a[80:83], v[172:175], v[76:79]\n"
    "s_waitcnt vmcnt(18)\n"
    "v_max3_u32 v240, v176, v177, v178\n"
    "v_mfma_f32_16x16x32_bf16 v[80:83], a[84:87], v[172:175], v[80:83]\n"
    "s_waitcnt vmcnt(17)\n"
    "v_max3_u32 v240, v240, v179, v180\n"
    "v_mfma_f32_16x16x32_bf16 v[84:87], a[88:91], v[172:175], v[84:87]\n"
    "v_max3_u32 v240, v240, v181, v182\n"
    "buffer_load_dwordx4 v[156:159], v13, s[16:19], s77 offen sc1\n"
    "v_mfma_f32_16x16x32_bf16 v[88:91], a[92:95], v[172:175], v[88:91]\n"
    "s_waitcnt vmcnt(17)\n"
    "v_max3_u32 v240, v240, v183, v184\n"
    "v_mfma_f32_16x16x32_bf16 v[92:95], a[96:99], v[172:175], v[92:95]\n"
    "v_max3_u32 v240, v240, v185, v186\n"
    "v_mfma_f32_16x16x32_bf16 v[16:19], a[80:83], v[160:163], v[16:19]\n"
    "s_waitcnt vmcnt(16)\n"
    "v_max3_u32 v240, v240, v187, v188\n"
    "v_mfma_f32_16x16x32_bf16 v[20:23], a[84:87], v[160:163], v[20:23]\n"
    "v_max3_u32 v240, v240, v189, v190\n"
    "v_mfma_f32_16x16x32_bf16 v[24:27], a[88:91], v[160:163], v[24:27]\n"
    "v_max_u32_e32 v240, v240, v191\n"
    "v_mfma_f32_16x16x32_bf16 v[28:31], a[92:95], v[160:163], v[28:31]\n"
    "v_cmp_eq_u32_e32 vcc, -1, v240\n"
    "v_mfma_f32_16x16x32_bf16 v[32:35], a[96:99], v[160:163], v[32:35]\n"
    "s_cbranch_vccnz .Lmk_hre_79\n"
    ".Lmk_hok_80:\n"
    "v_mfma_f32_16x16x32_bf16 v[36:39], a[100:103], v[180:183], v[36:39]\n"
    "s_add_u32 s67, s63, 1024\n"
    "v_add_u32_e32 v14, s67, v0\n"
    "buffer_load_dwordx4 v[160:163], v14, s[16:19], s74 offen sc1\n"
    "v_mfma_f32_16x16x32_bf16 v[40:43], a[104:107], v[180:183], v[40:43]\n"
    "v_mfma_f32_16x16x32_bf16 v[44:47], a[108:111], v[180:183], v[44:47]\n"
    "v_mfma_f32_16x16x32_bf16 v[48:51], a[112:115], v[180:183], v[48:51]\n"
    "v_mfma_f32_16x16x32_bf16 v[52:55], a[116:119], v[180:183], v[52:55]\n"
    "buffer_load_dwordx4 v[164:167], v14, s[16:19], s75 offen sc1\n"
    "v_mfma_f32_16x16x32_bf16 v[56:59], a[100:103], v[184:187], v[56:59]\n"
    "v_mfma_f32_16x16x32_bf16 v[60:63], a[104:107], v[184:187], v[60:63]\n"
    "v_mfma_f32_16x16x32_bf16 v[64:67], a[108:111], v[184:187], v[64:67]\n"
    "v_mfma_f32_16x16x32_bf16 v[68:71], a[112:115], v[184:187], v[68:71]\n"
    "buffer_load_dwordx4 v[168:171], v14, s[16:19], s76 offen sc1\n"
    "v_mfma_f32_16x16x32_bf16 v[72:75], a[116:119], v[184:187], v[72:75]\n"
    "v_mfma_f32_16x16x32_bf16 v[76:79], a[100:103], v[188:191], v[76:79]\n"
    "s_waitcnt vmcnt(18)\n"
    "v_max3_u32 v240, v192, v193, v194\n"
    "v_mfma_f32_16x16x32_bf16 v[80:83], a[104:107], v[188:191], v[80:83]\n"
    "s_waitcnt vmcnt(17)\n"
    "v_max3_u32 v240, v240, v195, v196\n"
    "v_mfma_f32_16x16x32_bf16 v[84:87], a[108:111], v[188:191], v[84:87]\n"
    "v_max3_u32 v240, v240, v197, v198\n"
    "buffer_load_dwordx4 v[172:175], v14, s[16:19], s77 offen sc1\n"
    "v_mfma_f32_16x16x32_bf16 v[88:91], a[112:115], v[188:191], v[88:91]\n"
    "s_waitcnt vmcnt(17)\n"
    "v_max3_u32 v240, v240, v199, v200\n"
    "v_mfma_f32_16x16x32_bf16 v[92:95], a[116:119], v[188:191], v[92:95]\n"
    "v_max3_u32 v240, v240, v201, v202\n"
    "v_mfma_f32_16x16x32_bf16 v[16:19], a[100:103], v[176:179], v[16:19]\n"
    "s_waitcnt vmcnt(16)\n"
    "v_max3_u32 v240, v240, v203, v204\n"
    "v_mfma_f32_16x16x32_bf16 v[20:23], a[104:107], v[176:179], v[20:23]\n"
    "v_max3_u32 v240, v240, v205, v206\n"
    "v_mfma_f32_16x16x32_bf16 v[24:27], a[108:111], v[176:179], v[24:27]\n"
    "v_max_u32_e32 v240, v240, v207\n"
    "v_mfma_f32_16x16x32_bf16 v[28:31], a[112:115], v[176:179], v[28:31]\n"
    "v_cmp_eq_u32_e32 vcc, -1, v240\n"
    "v_mfma_f32_16x16x32_bf16 v[32:35], a[116:119], v[176:179], v[32:35]\n"
    "s_cbranch_vccnz .Lmk_hre_81\n"
    ".Lmk_hok_82:\n"
    "v_mfma_f32_16x16x32_bf16 v[36:39], a[120:123], v[196:199], v[36:39]\n"
    "s_add_u32 s67, s63, 2048\n"
    "v_add_u32_e32 v13, s67, v0\n"
    "buffer_load_dwordx4 v[176:179], v13, s[16:19], s74 offen sc1\n"
    "v_mfma_f32_16x16x32_bf16 v[40:43], a[124:127], v[196:199], v[40:43]\n"
    "v_mfma_f32_16x16x32_bf16 v[44:47], a[128:131], v[196:199], v[44:47]\n"
    "v_mfma_f32_16x16x32_bf16 v[48:51], a[132:135], v[196:199], v[48:51]\n"
    "v_mfma_f32_16x16x32_bf16 v[52:55], a[136:139], v[196:199], v[52:55]\n"
    "buffer_load_dwordx4 v[180:183], v13, s[16:19], s75 offen sc1\n"
    "v_mfma_f32_16x16x32_bf16 v[56:59], a[120:123], v[200:203], v[56:59]\n"
    "v_mfma_f32_16x16x32_bf16 v[60:63], a[124:127], v[200:203], v[60:63]\n"
    "v_mfma_f32_16x16x32_bf16 v[64:67], a[128:131], v[200:203], v[64:67]\n"
    "v_mfma_f32_16x16x32_bf16 v[68:71], a[132:135], v[200:203], v[68:71]\n"
    "buffer_load_dwordx4 v[184:187], v13, s[16:19], s76 offen sc1\n"
    "v_mfma_f32_16x16x32_bf16 v[72:75], a[136:139], v[200:203], v[72:75]\n"
    "v_mfma_f32_16x16x32_bf16 v[76:79], a[120:123], v[204:207], v[76:79]\n"
    "s_waitcnt vmcnt(18)\n"
    "v_max3_u32 v240, v208, v209, v210\n"
    "v_mfma_f32_16x16x32_bf16 v[80:83], a[124:127], v[204:207], v[80:83]\n"
    "s_waitcnt vmcnt(17)\n"
    "v_max3_u32 v240, v240, v211, v212\n"
    "v_mfma_f32_16x16x32_bf16 v[84:87], a[128:131], v[204:207], v[84:87]\n"
    "v_max3_u32 v240, v240, v213, v214\n"
    "buffer_load_dwordx4 v[188:191], v13, s[16:19], s77 offen sc1\n"
    "v_mfma_f32_16x16x32_bf16 v[88:91], a[132:135], v[204:207], v[88:91]\n"
    "s_waitcnt vmcnt(17)\n"
    "v_max3_u32 v240, v240, v215, v216\n"
    "v_mfma_f32_16x16x32_bf16 v[92:95], a[136:139], v[204:207], v[92:95]\n"
    "v_max3_u32 v240, v240, v217, v218\n"
    "v_mfma_f32_16x16x32_bf16 v[16:19], a[120:123], v[192:195], v[16:19]\n"
    "s_waitcnt vmcnt(16)\n"
    "v_max3_u32 v240, v240, v219, v220\n"
    "v_mfma_f32_16x16x32_bf16 v[20:23], a[124:127], v[192:195], v[20:23]\n"
    "v_max3_u32 v240, v240, v221, v222\n"
    "v_mfma_f32_16x16x32_bf16 v[24:27], a[128:131], v[192:195], v[24:27]\n"
    "v_max_u32_e32 v240, v240, v223\n"
    "v_mfma_f32_16x16x32_bf16 v[28:31], a[132:135], v[192:195], v[28:31]\n"
    "v_cmp_eq_u32_e32 vcc, -1, v240\n"
    "v_mfma_f32_16x16x32_bf16 v[32:35], a[136:139], v[192:195], v[32:35]\n"
    "s_cbranch_vccnz .Lmk_hre_83\n"
    ".Lmk_hok_84:\n"
    "v_mfma_f32_16x16x32_bf16 v[36:39], a[140:143], v[212:215], v[36:39]\n"
    "s_add_u32 s67, s62, 2048\n"
    "v_add_u32_e32 v14, s67, v0\n"
    "buffer_load_dwordx4 v[192:195], v14, s[16:19], s74 offen sc1\n"
    "v_mfma_f32_16x16x32_bf16 v[40:43], a[144:147], v[212:215], v[40:43]\n"
    "v_mfma_f32_16x16x32_bf16 v[44:47], a[148:151], v[212:215], v[44:47]\n"
    "v_mfma_f32_16x16x32_bf16 v[48:51], a[152:155], v[212:215], v[48:51]\n"
    "v_mfma_f32_16x16x32_bf16 v[52:55], a[156:159], v[212:215], v[52:55]\n"
    "buffer_load_dwordx4 v[196:199], v14, s[16:19], s75 offen sc1\n"
    "v_mfma_f32_16x16x32_bf16 v[56:59], a[140:143], v[216:219], v[56:59]\n"
    "v_mfma_f32_16x16x32_bf16 v[60:63], a[144:147], v[216:219], v[60:63]\n"
    "v_mfma_f32_16x16x32_bf16 v[64:67], a[148:151], v[216:219], v[64:67]\n"
    "v_mfma_f32_16x16x32_bf16 v[68:71], a[152:155], v[216:219], v[68:71]\n"
    "buffer_load_dwordx4 v[200:203], v14, s[16:19], s76 offen sc1\n"
    "v_mfma_f32_16x16x32_bf16 v[72:75], a[156:159], v[216:219], v[72:75]\n"
    "v_mfma_f32_16x16x32_bf16 v[76:79], a[140:143], v[220:223], v[76:79]\n"
    "s_waitcnt vmcnt(18)\n"
    "v_max3_u32 v240, v224, v225, v226\n"
    "v_mfma_f32_16x16x32_bf16 v[80:83], a[144:147], v[220:223], v[80:83]\n"
    "s_waitcnt vmcnt(17)\n"
    "v_max3_u32 v240, v240, v227, v228\n"
    "v_mfma_f32_16x16x32_bf16 v[84:87], a[148:151], v[220:223], v[84:87]\n"
    "v_max3_u32 v240, v240, v229, v230\n"
    "buffer_load_dwordx4 v[204:207], v14, s[16:19], s77 offen sc1\n"
    "v_mfma_f32_16x16x32_bf16 v[88:91], a[152:155], v[220:223], v[88:91]\n"
    "s_waitcnt vmcnt(17)\n"
    "v_max3_u32 v240, v240, v231, v232\n"
    "v_mfma_f32_16x16x32_bf16 v[92:95], a[156:159], v[220:223], v[92:95]\n"
    "v_max3_u32 v240, v240, v233, v234\n"
    "v_mfma_f32_16x16x32_bf16 v[16:19], a[140:143], v[208:211], v[16:19]\n"
    "s_waitcnt vmcnt(16)\n"
    "v_max3_u32 v240, v240, v235, v236\n"
    "v_mfma_f32_16x16x32_bf16 v[20:23], a[144:147], v[208:211], v[20:23]\n"
    "v_max3_u32 v240, v240, v237, v238\n"
    "v_mfma_f32_16x16x32_bf16 v[24:27], a[148:151], v[208:211], v[24:27]\n"
    "v_max_u32_e32 v240, v240, v239\n"
    "v_mfma_f32_16x16x32_bf16 v[28:31], a[152:155], v[208:211], v[28:31]\n"
    "v_cmp_eq_u32_e32 vcc, -1, v240\n"
    "v_mfma_f32_16x16x32_bf16 v[32:35], a[156:159], v[208:211], v[32:35]\n"
    "s_cbranch_vccnz .Lmk_hre_85\n"
    ".Lmk_hok_86:\n"
    "v_mfma_f32_16x16x32_bf16 v[36:39], a[160:163], v[228:231], v[36:39]\n"
    "s_add_u32 s67, s61, 2048\n"
    "v_add_u32_e32 v13, s67, v0\n"
    "buffer_load_dwordx4 v[208:211], v13, s[16:19], s74 offen sc1\n"
    "v_mfma_f32_16x16x32_bf16 v[40:43], a[164:167], v[228:231], v[40:43]\n"
    "v_mfma_f32_16x16x32_bf16 v[44:47], a[168:171], v[228:231], v[44:47]\n"
    "v_mfma_f32_16x16x32_bf16 v[48:51], a[172:175], v[228:231], v[48:51]\n"
    "v_mfma_f32_16x16x32_bf16 v[52:55], a[176:179], v[228:231], v[52:55]\n"
    "buffer_load_dwordx4 v[212:215], v13, s[16:19], s75 offen sc1\n"
    "v_mfma_f32_16x16x32_bf16 v[56:59], a[160:163], v[232:235], v[56:59]\n"
    "v_mfma_f32_16x16x32_bf16 v[60:63], a[164:167], v[232:235], v[60:63]\n"
    "v_mfma_f32_16x16x32_bf16 v[64:67], a[168:171], v[232:235], v[64:67]\n"
    "v_mfma_f32_16x16x32_bf16 v[68:71], a[172:175], v[232:235], v[68:71]\n"
    "buffer_load_dwordx4 v[216:219], v13, s[16:19], s76 offen sc1\n"
    "v_mfma_f32_16x16x32_bf16 v[72:75], a[176:179], v[232:235], v[72:75]\n"
    "v_mfma_f32_16x16x32_bf16 v[76:79], a[160:163], v[236:239], v[76:79]\n"
    "s_waitcnt vmcnt(18)\n"
    "v_max3_u32 v240, v144, v145, v146\n"
    "v_mfma_f32_16x16x32_bf16 v[80:83], a[164:167], v[236:239], v[80:83]\n"
    "s_waitcnt vmcnt(17)\n"
    "v_max3_u32 v240, v240, v147, v148\n"
    "v_mfma_f32_16x16x32_bf16 v[84:87], a[168:171], v[236:239], v[84:87]\n"
    "v_max3_u32 v240, v240, v149, v150\n"
    "buffer_load_dwordx4 v[220:223], v13, s[16:19], s77 offen sc1\n"
    "v_mfma_f32_16x16x32_bf16 v[88:91], a[172:175], v[236:239], v[88:91]\n"
    "s_waitcnt vmcnt(17)\n"
    "v_max3_u32 v240, v240, v151, v152\n"
    "v_mfma_f32_16x16x32_bf16 v[92:95], a[176:179], v[236:239], v[92:95]\n"
    "v_max3_u32 v240, v240, v153, v154\n"
    "v_mfma_f32_16x16x32_bf16 v[16:19], a[160:163], v[224:227], v[16:19]\n"
    "s_waitcnt vmcnt(16)\n"
    "v_max3_u32 v240, v240, v155, v156\n"
    "v_mfma_f32_16x16x32_bf16 v[20:23], a[164:167], v[224:227], v[20:23]\n"
    "v_max3_u32 v240, v240, v157, v158\n"
    "v_mfma_f32_16x16x32_bf16 v[24:27], a[168:171], v[224:227], v[24:27]\n"
    "v_max_u32_e32 v240, v240, v159\n"
    "v_mfma_f32_16x16x32_bf16 v[28:31], a[172:175], v[224:227], v[28:31]\n"
    "v_cmp_eq_u32_e32 vcc, -1, v240\n"
    "v_mfma_f32_16x16x32_bf16 v[32:35], a[176:179], v[224:227], v[32:35]\n"
    "s_cbranch_vccnz .Lmk_hre_87\n"
    ".Lmk_hok_88:\n"
    "v_mfma_f32_16x16x32_bf16 v[36:39], a[180:183], v[148:151], v[36:39]\n"
    "s_add_u32 s67, s60, 2048\n"
    "v_add_u32_e32 v14, s67, v0\n"
    "buffer_load_dwordx4 v[224:227], v14, s[16:19], s74 offen sc1\n"
    "v_mfma_f32_16x16x32_bf16 v[40:43], a[184:187], v[148:151], v[40:43]\n"
    "v_mfma_f32_16x16x32_bf16 v[44:47], a[188:191], v[148:151], v[44:47]\n"
    "v_mfma_f32_16x16x32_bf16 v[48:51], a[192:195], v[148:151], v[48:51]\n"
    "v_mfma_f32_16x16x32_bf16 v[52:55], a[196:199], v[148:151], v[52:55]\n"
    "buffer_load_dwordx4 v[228:231], v14, s[16:19], s75 offen sc1\n"
    "v_mfma_f32_16x16x32_bf16 v[56:59], a[180:183], v[152:155], v[56:59]\n"
    "v_mfma_f32_16x16x32_bf16 v[60:63], a[184:187], v[152:155], v[60:63]\n"
    "v_mfma_f32_16x16x32_bf16 v[64:67], a[188:191], v[152:155], v[64:67]\n"
    "v_mfma_f32_16x16x32_bf16 v[68:71], a[192:195], v[152:155], v[68:71]\n"
    "buffer_load_dwordx4 v[232:235], v14, s[16:19], s76 offen sc1\n"
    "v_mfma_f32_16x16x32_bf16 v[72:75], a[196:199], v[152:155], v[72:75]\n"
    "v_mfma_f32_16x16x32_bf16 v[76:79], a[180:183], v[156:159], v[76:79]\n"
    "s_waitcnt vmcnt(18)\n"
    "v_max3_u32 v240, v160, v161, v162\n"
    "v_mfma_f32_16x16x32_bf16 v[80:83], a[184:187], v[156:159], v[80:83]\n"
    "s_waitcnt vmcnt(17)\n"
    "v_max3_u32 v240, v240, v163, v164\n"
    "v_mfma_f32_16x16x32_bf16 v[84:87], a[188:191], v[156:159], v[84:87]\n"
    "v_max3_u32 v240, v240, v165, v166\n"
    "buffer_load_dwordx4 v[236:239], v14, s[16:19], s77 offen sc1\n"
    "v_mfma_f32_16x16x32_bf16 v[88:91], a[192:195], v[156:159], v[88:91]\n"
    "s_waitcnt vmcnt(17)\n"
    "v_max3_u32 v240, v240, v167, v168\n"
    "v_mfma_f32_16x16x32_bf16 v[92:95], a[196:199], v[156:159], v[92:95]\n"
    "v_max3_u32 v240, v240, v169, v170\n"
    "v_mfma_f32_16x16x32_bf16 v[16:19], a[180:183], v[144:147], v[16:19]\n"
    "s_waitcnt vmcnt(16)\n"
    "v_max3_u32 v240, v240, v171, v172\n"
    "v_mfma_f32_16x16x32_bf16 v[20:23], a[184:187], v[144:147], v[20:23]\n"
    "v_max3_u32 v240, v240, v173, v174\n"
    "v_mfma_f32_16x16x32_bf16 v[24:27], a[188:191], v[144:147], v[24:27]\n"
    "v_max_u32_e32 v240, v240, v175\n"
    "v_mfma_f32_16x16x32_bf16 v[28:31], a[192:195], v[144:147], v[28:31]\n"
    "v_cmp_eq_u32_e32 vcc, -1, v240\n"
    "v_mfma_f32_16x16x32_bf16 v[32:35], a[196:199], v[144:147], v[32:35]\n"
    "s_cbranch_vccnz .Lmk_hre_89\n"
    ".Lmk_hok_90:\n"
    "v_mfma_f32_16x16x32_bf16 v[36:39], a[200:203], v[164:167], v[36:39]\n"
    "s_add_u32 s67, s60, 3072\n"
    "v_add_u32_e32 v13, s67, v0\n"
    "buffer_load_dwordx4 v[144:147], v13, s[16:19], s74 offen sc1\n"
    "v_mfma_f32_16x16x32_bf16 v[40:43], a[204:207], v[164:167], v[40:43]\n"
    "v_mfma_f32_16x16x32_bf16 v[44:47], a[208:211], v[164:167], v[44:47]\n"
    "v_mfma_f32_16x16x32_bf16 v[48:51], a[212:215], v[164:167], v[48:51]\n"
    "v_mfma_f32_16x16x32_bf16 v[52:55], a[216:219], v[164:167], v[52:55]\n"
    "buffer_load_dwordx4 v[148:151], v13, s[16:19], s75 offen sc1\n"
    "v_mfma_f32_16x16x32_bf16 v[56:59], a[200:203], v[168:171], v[56:59]\n"
    "v_mfma_f32_16x16x32_bf16 v[60:63], a[204:207], v[168:171], v[60:63]\n"
    "v_mfma_f32_16x16x32_bf16 v[64:67], a[208:211], v[168:171], v[64:67]\n"
    "v_mfma_f32_16x16x32_bf16 v[68:71], a[212:215], v[168:171], v[68:71]\n"
    "buffer_load_dwordx4 v[152:155], v13, s[16:19], s76 offen sc1\n"
    "v_mfma_f32_16x16x32_bf16 v[72:75], a[216:219], v[168:171], v[72:75]\n"
    "v_mfma_f32_16x16x32_bf16 v[76:79], a[200:203], v[172:175], v[76:79]\n"
    "s_waitcnt vmcnt(18)\n"
    "v_max3_u32 v240, v176, v177, v178\n"
    "v_mfma_f32_16x16x32_bf16 v[80:83], a[204:207], v[172:175], v[80:83]\n"
    "s_waitcnt vmcnt(17)\n"
    "v_max3_u32 v240, v240, v179, v180\n"
    "v_mfma_f32_16x16x32_bf16 v[84:87], a[208:211], v[172:175], v[84:87]\n"
    "v_max3_u32 v240, v240, v181, v182\n"
    "buffer_load_dwordx4 v[156:159], v13, s[16:19], s77 offen sc1\n"
    "v_mfma_f32_16x16x32_bf16 v[88:91], a[212:215], v[172:175], v[88:91]\n"
    "s_waitcnt vmcnt(17)\n"
    "v_max3_u32 v240, v240, v183, v184\n"
    "v_mfma_f32_16x16x32_bf16 v[92:95], a[216:219], v[172:175], v[92:95]\n"
    "v_max3_u32 v240, v240, v185, v186\n"
    "v_mfma_f32_16x16x32_bf16 v[16:19], a[200:203], v[160:163], v[16:19]\n"
    "s_waitcnt vmcnt(16)\n"
    "v_max3_u32 v240, v240, v187, v188\n"
    "v_mfma_f32_16x16x32_bf16 v[20:23], a[204:207], v[160:163], v[20:23]\n"
    "v_max3_u32 v240, v240, v189, v190\n"
    "v_mfma_f32_16x16x32_bf16 v[24:27], a[208:211], v[160:163], v[24:27]\n"
    "v_max_u32_e32 v240, v240, v191\n"
    "v_mfma_f32_16x16x32_bf16 v[28:31], a[212:215], v[160:163], v[28:31]\n"
    "v_cmp_eq_u32_e32 vcc, -1, v240\n"
    "v_mfma_f32_16x16x32_bf16 v[32:35], a[216:219], v[160:163], v[32:35]\n"
    "s_cbranch_vccnz .Lmk_hre_91\n"
    ".Lmk_hok_92:\n"
    "v_mfma_f32_16x16x32_bf16 v[36:39], v[104:107], v[180:183], v[36:39]\n"
    "v_add_u32_e32 v14, s64, v0\n"
    "buffer_load_dwordx4 v[160:163], v14, s[16:19], s74 offen sc1\n"
    "v_mfma_f32_16x16x32_bf16 v[40:43], v[108:111], v[180:183], v[40:43]\n"
    "v_mfma_f32_16x16x32_bf16 v[44:47], v[112:115], v[180:183], v[44:47]\n"
    "v_mfma_f32_16x16x32_bf16 v[48:51], v[116:119], v[180:183], v[48:51]\n"
    "v_mfma_f32_16x16x32_bf16 v[52:55], v[120:123], v[180:183], v[52:55]\n"
    "buffer_load_dwordx4 v[164:167], v14, s[16:19], s75 offen sc1\n"
    "v_mfma_f32_16x16x32_bf16 v[56:59], v[104:107], v[184:187], v[56:59]\n"
    "v_mfma_f32_16x16x32_bf16 v[60:63], v[108:111], v[184:187], v[60:63]\n"
    "v_mfma_f32_16x16x32_bf16 v[64:67], v[112:115], v[184:187], v[64:67]\n"
    "v_mfma_f32_16x16x32_bf16 v[68:71], v[116:119], v[184:187], v[68:71]\n"
    "buffer_load_dwordx4 v[168:171], v14, s[16:19], s76 offen sc1\n"
    "v_mfma_f32_16x16x32_bf16 v[72:75], v[120:123], v[184:187], v[72:75]\n"
    "v_mfma_f32_16x16x32_bf16 v[76:79], v[104:107], v[188:191], v[76:79]\n"
    "s_waitcnt vmcnt(18)\n"
    "v_max3_u32 v240, v192, v193, v194\n"
    "v_mfma_f32_16x16x32_bf16 v[80:83], v[108:111], v[188:191], v[80:83]\n"
    "s_waitcnt vmcnt(17)\n"
    "v_max3_u32 v240, v240, v195, v196\n"
    "v_mfma_f32_16x16x32_bf16 v[84:87], v[112:115], v[188:191], v[84:87]\n"
    "v_max3_u32 v240, v240, v197, v198\n"
    "buffer_load_dwordx4 v[172:175], v14, s[16:19], s77 offen sc1\n"
    "v_mfma_f32_16x16x32_bf16 v[88:91], v[116:119], v[188:191], v[88:91]\n"
    "s_waitcnt vmcnt(17)\n"
    "v_max3_u32 v240, v240, v199, v200\n"
    "v_mfma_f32_16x16x32_bf16 v[92:95], v[120:123], v[188:191], v[92:95]\n"
    "v_max3_u32 v240, v240, v201, v202\n"
    "v_mfma_f32_16x16x32_bf16 v[16:19], v[104:107], v[176:179], v[16:19]\n"
    "s_waitcnt vmcnt(16)\n"
    "v_max3_u32 v240, v240, v203, v204\n"
    "v_mfma_f32_16x16x32_bf16 v[20:23], v[108:111], v[176:179], v[20:23]\n"
    "v_max3_u32 v240, v240, v205, v206\n"
    "v_mfma_f32_16x16x32_bf16 v[24:27], v[112:115], v[176:179], v[24:27]\n"
    "v_max_u32_e32 v240, v240, v207\n"
    "v_mfma_f32_16x16x32_bf16 v[28:31], v[116:119], v[176:179], v[28:31]\n"
    "v_cmp_eq_u32_e32 vcc, -1, v240\n"
    "v_mfma_f32_16x16x32_bf16 v[32:35], v[120:123], v[176:179], v[32:35]\n"
    "s_cbranch_vccnz .Lmk_hre_93\n"
    ".Lmk_hok_94:\n"
    "s_mov_b32 s20, s44\n"
    "s_and_b32 s21, s45, 0xffff\n"
    "s_mov_b32 s22, 0x500\n"
    "v_lshrrev_b32_e32 v9, 8, v0\n"
    "v_lshlrev_b32_e32 v9, 3, v9\n"
    "s_mul_i32 s50, s31, 160\n"
    "buffer_load_dwordx2 v[176:177], v9, s[20:23], s50 offen\n"
    "buffer_load_dwordx2 v[178:179], v9, s[20:23], s50 offen offset:32\n"
    "buffer_load_dwordx2 v[180:181], v9, s[20:23], s50 offen offset:64\n"
    "buffer_load_dwordx2 v[182:183], v9, s[20:23], s50 offen offset:96\n"
    "buffer_load_dwordx2 v[184:185], v9, s[20:23], s50 offen offset:128\n"
    "v_mfma_f32_16x16x32_bf16 v[36:39], a[48:51], v[196:199], v[36:39]\n"
    "v_mfma_f32_16x16x32_bf16 v[40:43], a[52:55], v[196:199], v[40:43]\n"
    "s_mov_b32 m0, s91\n"
    "s_nop 0\n"
    "buffer_load_dwordx4 v241, s[16:19], s90 offen sc1 lds\n"
    "v_mfma_f32_16x16x32_bf16 v[44:47], a[56:59], v[196:199], v[44:47]\n"
    "v_mfma_f32_16x16x32_bf16 v[48:51], v[96:99], v[196:199], v[48:51]\n"
    "v_mfma_f32_16x16x32_bf16 v[52:55], v[100:103], v[196:199], v[52:55]\n"
    "v_mfma_f32_16x16x32_bf16 v[56:59], a[48:51], v[200:203], v[56:59]\n"
    "v_mfma_f32_16x16x32_bf16 v[60:63], a[52:55], v[200:203], v[60:63]\n"
    "buffer_load_dwordx4 v241, s[16:19], s90 offen offset:1024 sc1 lds\n"
    "v_mfma_f32_16x16x32_bf16 v[64:67], a[56:59], v[200:203], v[64:67]\n"
    "v_mfma_f32_16x16x32_bf16 v[68:71], v[96:99], v[200:203], v[68:71]\n"
    "v_mfma_f32_16x16x32_bf16 v[72:75], v[100:103], v[200:203], v[72:75]\n"
    "v_mfma_f32_16x16x32_bf16 v[76:79], a[48:51], v[204:207], v[76:79]\n"
    "s_waitcnt vmcnt(22)\n"
    "v_max3_u32 v240, v208, v209, v210\n"
    "v_mfma_f32_16x16x32_bf16 v[80:83], a[52:55], v[204:207], v[80:83]\n"
    "s_waitcnt vmcnt(21)\n"
    "v_max3_u32 v240, v240, v211, v212\n"
    "buffer_load_dwordx4 v241, s[16:19], s90 offen offset:2048 sc1 lds\n"
    "v_mfma_f32_16x16x32_bf16 v[84:87], a[56:59], v[204:207], v[84:87]\n"
    "v_max3_u32 v240, v240, v213, v214\n"
    "v_mfma_f32_16x16x32_bf16 v[88:91], v[96:99], v[204:207], v[88:91]\n"
    "s_waitcnt vmcnt(21)\n"
    "v_max3_u32 v240, v240, v215, v216\n"
    "v_mfma_f32_16x16x32_bf16 v[92:95], v[100:103], v[204:207], v[92:95]\n"
    "v_max3_u32 v240, v240, v217, v218\n"
    "v_mfma_f32_16x16x32_bf16 v[16:19], a[48:51], v[192:195], v[16:19]\n"
    "s_waitcnt vmcnt(20)\n"
    "v_max3_u32 v240, v240, v219, v220\n"
    "v_mfma_f32_16x16x32_bf16 v[20:23], a[52:55], v[192:195], v[20:23]\n"
    "v_max3_u32 v240, v240, v221, v222\n"
    "buffer_load_dwordx4 v241, s[16:19], s90 offen offset:3072 sc1 lds\n"
    "v_mfma_f32_16x16x32_bf16 v[24:27], a[56:59], v[192:195], v[24:27]\n"
    "v_max_u32_e32 v240, v240, v223\n"
    "v_mfma_f32_16x16x32_bf16 v[28:31], v[96:99], v[192:195], v[28:31]\n"
    "v_cmp_eq_u32_e32 vcc, -1, v240\n"
    "v_mfma_f32_16x16x32_bf16 v[32:35], v[100:103], v[192:195], v[32:35]\n"
    "s_cbranch_vccnz .Lmk_hre_95\n"
    ".Lmk_hok_96:\n"
    "v_mfma_f32_16x16x32_bf16 v[36:39], a[240:243], v[212:215], v[36:39]\n"
    "v_accvgpr_read_b32 v9, a0\n"
    "v_lshlrev_b32_e32 v11, 16, v9\n"
    "v_mfma_f32_16x16x32_bf16 v[40:43], a[244:247], v[212:215], v[40:43]\n"
    "s_add_u32 m0, m0, 0x1000\n"
    "s_add_u32 s90, s90, 0x1000\n"
    "buffer_load_dwordx4 v241, s[16:19], s90 offen sc1 lds\n"
    "v_mfma_f32_16x16x32_bf16 v[44:47], a[248:251], v[212:215], v[44:47]\n"
    "v_and_b32_e32 v9, 0xffff0000, v9\n"
    "s_waitcnt vmcnt(9)\n"
    "v_lshlrev_b32_e32 v12, 16, v176\n"
    "v_mfma_f32_16x16x32_bf16 v[48:51], a[252:255], v[212:215], v[48:51]\n"
    "v_and_b32_e32 v15, 0xffff0000, v176\n"
    "v_add_f32_e32 v186, v11, v12\n"
    "v_mfma_f32_16x16x32_bf16 v[52:55], a[44:47], v[212:215], v[52:55]\n"
    "v_add_f32_e32 v187, v9, v15\n"
    "v_accvgpr_read_b32 v9, a1\n"
    "v_mfma_f32_16x16x32_bf16 v[56:59], a[240:243], v[216:219], v[56:59]\n"
    "v_lshlrev_b32_e32 v11, 16, v9\n"
    "v_and_b32_e32 v9, 0xffff0000, v9\n"
    "v_mfma_f32_16x16x32_bf16 v[60:63], a[244:247], v[216:219], v[60:63]\n"
    "buffer_load_dwordx4 v241, s[16:19], s90 offen offset:1024 sc1 lds\n"
    "v_mfma_f32_16x16x32_bf16 v[64:67], a[248:251], v[216:219], v[64:67]\n"
    "v_lshlrev_b32_e32 v12, 16, v177\n"
    "v_and_b32_e32 v15, 0xffff0000, v177\n"
    "v_mfma_f32_16x16x32_bf16 v[68:71], a[252:255], v[216:219], v[68:71]\n"
    "v_add_f32_e32 v188, v11, v12\n"
    "v_add_f32_e32 v189, v9, v15\n"
    "v_mfma_f32_16x16x32_bf16 v[72:75], a[44:47], v[216:219], v[72:75]\n"
    "v_accvgpr_read_b32 v9, a2\n"
    "v_lshlrev_b32_e32 v11, 16, v9\n"
    "v_mfma_f32_16x16x32_bf16 v[76:79], a[240:243], v[220:223], v[76:79]\n"
    "v_max3_u32 v240, v224, v225, v226\n"
    "v_and_b32_e32 v9, 0xffff0000, v9\n"
    "s_waitcnt vmcnt(9)\n"
    "v_lshlrev_b32_e32 v12, 16, v178\n"
    "v_mfma_f32_16x16x32_bf16 v[80:83], a[244:247], v[220:223], v[80:83]\n"
    "v_max3_u32 v240, v240, v227, v228\n"
    "buffer_load_dwordx4 v241, s[16:19], s90 offen offset:2048 sc1 lds\n"
    "v_mfma_f32_16x16x32_bf16 v[84:87], a[248:251], v[220:223], v[84:87]\n"
    "v_max3_u32 v240, v240, v229, v230\n"
    "v_and_b32_e32 v15, 0xffff0000, v178\n"
    "v_add_f32_e32 v190, v11, v12\n"
    "v_mfma_f32_16x16x32_bf16 v[88:91], a[252:255], v[220:223], v[88:91]\n"
    "v_max3_u32 v240, v240, v231, v232\n"
    "v_add_f32_e32 v191, v9, v15\n"
    "v_accvgpr_read_b32 v9, a3\n"
    "v_mfma_f32_16x16x32_bf16 v[92:95], a[44:47], v[220:223], v[92:95]\n"
    "v_max3_u32 v240, v240, v233, v234\n"
    "v_lshlrev_b32_e32 v11, 16, v9\n"
    "v_and_b32_e32 v9, 0xffff0000, v9\n"
    "v_mfma_f32_16x16x32_bf16 v[16:19], a[240:243], v[208:211], v[16:19]\n"
    "v_max3_u32 v240, v240, v235, v236\n"
    "v_lshlrev_b32_e32 v12, 16, v179\n"
    "v_and_b32_e32 v15, 0xffff0000, v179\n"
    "v_mfma_f32_16x16x32_bf16 v[20:23], a[244:247], v[208:211], v[20:23]\n"
    "v_max3_u32 v240, v240, v237, v238\n"
    "buffer_load_dwordx4 v241, s[16:19], s90 offen offset:3072 sc1 lds\n"
    "v_mfma_f32_16x16x32_bf16 v[24:27], a[248:251], v[208:211], v[24:27]\n"
    "v_max_u32_e32 v240, v240, v239\n"
    "v_add_f32_e32 v192, v11, v12\n"
    "v_add_f32_e32 v193, v9, v15\n"
    "v_mfma_f32_16x16x32_bf16 v[28:31], a[252:255], v[208:211], v[28:31]\n"
    "v_cmp_eq_u32_e32 vcc, -1, v240\n"
    "v_accvgpr_read_b32 v9, a4\n"
    "v_lshlrev_b32_e32 v11, 16, v9\n"
    "v_mfma_f32_16x16x32_bf16 v[32:35], a[44:47], v[208:211], v[32:35]\n"
    "v_and_b32_e32 v9, 0xffff0000, v9\n"
    "s_waitcnt vmcnt(10)\n"
    "v_lshlrev_b32_e32 v12, 16, v180\n"
    "s_cbranch_vccnz .Lmk_hre_97\n"
    ".Lmk_hok_98:\n"
    "v_mfma_f32_16x16x32_bf16 v[36:39], a[220:223], v[228:231], v[36:39]\n"
    "v_and_b32_e32 v15, 0xffff0000, v180\n"
    "v_add_f32_e32 v194, v11, v12\n"
    "v_mfma_f32_16x16x32_bf16 v[40:43], a[224:227], v[228:231], v[40:43]\n"
    "s_add_u32 m0, m0, 0x1000\n"
    "s_add_u32 s90, s90, 0x1000\n"
    "buffer_load_dwordx4 v241, s[16:19], s90 offen sc1 lds\n"
    "v_mfma_f32_16x16x32_bf16 v[44:47], a[228:231], v[228:231], v[44:47]\n"
    "v_add_f32_e32 v195, v9, v15\n"
    "v_accvgpr_read_b32 v9, a5\n"
    "v_mfma_f32_16x16x32_bf16 v[48:51], a[232:235], v[228:231], v[48:51]\n"
    "v_lshlrev_b32_e32 v11, 16, v9\n"
    "v_and_b32_e32 v9, 0xffff0000, v9\n"
    "v_mfma_f32_16x16x32_bf16 v[52:55], a[236:239], v[228:231], v[52:55]\n"
    "v_lshlrev_b32_e32 v12, 16, v181\n"
    "v_and_b32_e32 v15, 0xffff0000, v181\n"
    "v_mfma_f32_16x16x32_bf16 v[56:59], a[220:223], v[232:235], v[56:59]\n"
    "v_add_f32_e32 v196, v11, v12\n"
    "v_add_f32_e32 v197, v9, v15\n"
    "v_mfma_f32_16x16x32_bf16 v[60:63], a[224:227], v[232:235], v[60:63]\n"
    "buffer_load_dwordx4 v241, s[16:19], s90 offen offset:1024 sc1 lds\n"
    "v_mfma_f32_16x16x32_bf16 v[64:67], a[228:231], v[232:235], v[64:67]\n"
    "v_accvgpr_read_b32 v9, a6\n"
    "v_lshlrev_b32_e32 v11, 16, v9\n"
    "v_mfma_f32_16x16x32_bf16 v[68:71], a[232:235], v[232:235], v[68:71]\n"
    "v_and_b32_e32 v9, 0xffff0000, v9\n"
    "s_waitcnt vmcnt(11)\n"
    "v_lshlrev_b32_e32 v12, 16, v182\n"
    "v_mfma_f32_16x16x32_bf16 v[72:75], a[236:239], v[232:235], v[72:75]\n"
    "v_and_b32_e32 v15, 0xffff0000, v182\n"
    "v_add_f32_e32 v198, v11, v12\n"
    "v_mfma_f32_16x16x32_bf16 v[76:79], a[220:223], v[236:239], v[76:79]\n"
    "v_max3_u32 v240, v144, v145, v146\n"
    "v_add_f32_e32 v199, v9, v15\n"
    "v_accvgpr_read_b32 v9, a7\n"
    "v_mfma_f32_16x16x32_bf16 v[80:83], a[224:227], v[236:239], v[80:83]\n"
    "v_max3_u32 v240, v240, v147, v148\n"
    "s_mov_b32 m0, s93\n"
    "s_nop 0\n"
    "buffer_load_dwordx4 v241, s[16:19], s92 offen sc1 lds\n"
    "v_mfma_f32_16x16x32_bf16 v[84:87], a[228:231], v[236:239], v[84:87]\n"
    "v_max3_u32 v240, v240, v149, v150\n"
    "v_lshlrev_b32_e32 v11, 16, v9\n"
    "v_and_b32_e32 v9, 0xffff0000, v9\n"
    "v_mfma_f32_16x16x32_bf16 v[88:91], a[232:235], v[236:239], v[88:91]\n"
    "v_max3_u32 v240, v240, v151, v152\n"
    "v_lshlrev_b32_e32 v12, 16, v183\n"
    "v_and_b32_e32 v15, 0xffff0000, v183\n"
    "v_mfma_f32_16x16x32_bf16 v[92:95], a[236:239], v[236:239], v[92:95]\n"
    "v_max3_u32 v240, v240, v153, v154\n"
    "v_add_f32_e32 v200, v11, v12\n"
    "v_add_f32_e32 v201, v9, v15\n"
    "v_mfma_f32_16x16x32_bf16 v[16:19], a[220:223], v[224:227], v[16:19]\n"
    "v_max3_u32 v240, v240, v155, v156\n"
    "v_accvgpr_read_b32 v9, a8\n"
    "v_lshlrev_b32_e32 v11, 16, v9\n"
    "v_mfma_f32_16x16x32_bf16 v[20:23], a[224:227], v[224:227], v[20:23]\n"
    "v_max3_u32 v240, v240, v157, v158\n"
    "buffer_load_dwordx4 v241, s[16:19], s92 offen offset:1024 sc1 lds\n"
    "v_mfma_f32_16x16x32_bf16 v[24:27], a[228:231], v[224:227], v[24:27]\n"
    "v_max_u32_e32 v240, v240, v159\n"
    "v_and_b32_e32 v9, 0xffff0000, v9\n"
    "s_waitcnt vmcnt(12)\n"
    "v_lshlrev_b32_e32 v12, 16, v184\n"
    "v_mfma_f32_16x16x32_bf16 v[28:31], a[232:235], v[224:227], v[28:31]\n"
    "v_cmp_eq_u32_e32 vcc, -1, v240\n"
    "v_and_b32_e32 v15, 0xffff0000, v184\n"
    "v_add_f32_e32 v202, v11, v12\n"
    "v_mfma_f32_16x16x32_bf16 v[32:35], a[236:239], v[224:227], v[32:35]\n"
    "v_add_f32_e32 v203, v9, v15\n"
    "v_accvgpr_read_b32 v9, a9\n"
    "s_cbranch_vccnz .Lmk_hre_99\n"
    ".Lmk_hok_100:\n"
    "v_mfma_f32_16x16x32_bf16 v[36:39], v[124:127], v[148:151], v[36:39]\n"
    "v_lshlrev_b32_e32 v11, 16, v9\n"
    "v_and_b32_e32 v9, 0xffff0000, v9\n"
    "v_mfma_f32_16x16x32_bf16 v[40:43], v[128:131], v[148:151], v[40:43]\n"
    "buffer_load_dwordx4 v241, s[16:19], s92 offen offset:2048 sc1 lds\n"
    "v_mfma_f32_16x16x32_bf16 v[44:47], v[132:135], v[148:151], v[44:47]\n"
    "v_lshlrev_b32_e32 v12, 16, v185\n"
    "v_and_b32_e32 v15, 0xffff0000, v185\n"
    "v_mfma_f32_16x16x32_bf16 v[48:51], v[136:139], v[148:151], v[48:51]\n"
    "v_add_f32_e32 v204, v11, v12\n"
    "v_add_f32_e32 v205, v9, v15\n"
    "v_mfma_f32_16x16x32_bf16 v[52:55], v[140:143], v[148:151], v[52:55]\n"
    "v_mfma_f32_16x16x32_bf16 v[56:59], v[124:127], v[152:155], v[56:59]\n"
    "v_mfma_f32_16x16x32_bf16 v[60:63], v[128:131], v[152:155], v[60:63]\n"
    "buffer_load_dwordx4 v241, s[16:19], s92 offen offset:3072 sc1 lds\n"
    "v_mfma_f32_16x16x32_bf16 v[64:67], v[132:135], v[152:155], v[64:67]\n"
    "v_mfma_f32_16x16x32_bf16 v[68:71], v[136:139], v[152:155], v[68:71]\n"
    "v_mfma_f32_16x16x32_bf16 v[72:75], v[140:143], v[152:155], v[72:75]\n"
    "v_mfma_f32_16x16x32_bf16 v[76:79], v[124:127], v[156:159], v[76:79]\n"
    "v_max3_u32 v240, v160, v161, v162\n"
    "v_mfma_f32_16x16x32_bf16 v[80:83], v[128:131], v[156:159], v[80:83]\n"
    "v_max3_u32 v240, v240, v163, v164\n"
    "s_add_u32 m0, m0, 0x1000\n"
    "s_add_u32 s92, s92, 0x1000\n"
    "buffer_load_dwordx4 v241, s[16:19], s92 offen sc1 lds\n"
    "v_mfma_f32_16x16x32_bf16 v[84:87], v[132:135], v[156:159], v[84:87]\n"
    "v_max3_u32 v240, v240, v165, v166\n"
    "v_mfma_f32_16x16x32_bf16 v[88:91], v[136:139], v[156:159], v[88:91]\n"
    "v_max3_u32 v240, v240, v167, v168\n"
    "v_mfma_f32_16x16x32_bf16 v[92:95], v[140:143], v[156:159], v[92:95]\n"
    "v_max3_u32 v240, v240, v169, v170\n"
    "v_mfma_f32_16x16x32_bf16 v[16:19], v[124:127], v[144:147], v[16:19]\n"
    "v_max3_u32 v240, v240, v171, v172\n"
    "v_mfma_f32_16x16x32_bf16 v[20:23], v[128:131], v[144:147], v[20:23]\n"
    "v_max3_u32 v240, v240, v173, v174\n"
    "buffer_load_dwordx4 v241, s[16:19], s92 offen offset:1024 sc1 lds\n"
    "v_mfma_f32_16x16x32_bf16 v[24:27], v[132:135], v[144:147], v[24:27]\n"
    "v_max_u32_e32 v240, v240, v175\n"
    "v_mfma_f32_16x16x32_bf16 v[28:31], v[136:139], v[144:147], v[28:31]\n"
    "v_cmp_eq_u32_e32 vcc, -1, v240\n"
    "v_mfma_f32_16x16x32_bf16 v[32:35], v[140:143], v[144:147], v[32:35]\n"
    "s_cbranch_vccnz .Lmk_hre_101\n"
    ".Lmk_hok_102:\n"
    "v_mfma_f32_16x16x32_bf16 v[36:39], a[24:27], v[164:167], v[36:39]\n"
    "v_mfma_f32_16x16x32_bf16 v[40:43], a[28:31], v[164:167], v[40:43]\n"
    "buffer_load_dwordx4 v241, s[16:19], s92 offen offset:2048 sc1 lds\n"
    "v_mfma_f32_16x16x32_bf16 v[44:47], a[32:35], v[164:167], v[44:47]\n"
    "v_mfma_f32_16x16x32_bf16 v[48:51], a[36:39], v[164:167], v[48:51]\n"
    "v_mfma_f32_16x16x32_bf16 v[52:55], a[40:43], v[164:167], v[52:55]\n"
    "v_mfma_f32_16x16x32_bf16 v[56:59], a[24:27], v[168:171], v[56:59]\n"
    "v_mfma_f32_16x16x32_bf16 v[60:63], a[28:31], v[168:171], v[60:63]\n"
    "s_mov_b32 exec_hi, 0\n"
    "buffer_load_dwordx4 v241, s[16:19], s92 offen offset:3072 sc1 lds\n"
    "s_mov_b32 exec_hi, -1\n"
    "v_mfma_f32_16x16x32_bf16 v[64:67], a[32:35], v[168:171], v[64:67]\n"
    "v_mfma_f32_16x16x32_bf16 v[68:71], a[36:39], v[168:171], v[68:71]\n"
    "v_mfma_f32_16x16x32_bf16 v[72:75], a[40:43], v[168:171], v[72:75]\n"
    "v_mfma_f32_16x16x32_bf16 v[76:79], a[24:27], v[172:175], v[76:79]\n"
    "v_mfma_f32_16x16x32_bf16 v[80:83], a[28:31], v[172:175], v[80:83]\n"
    "s_add_u32 s58, s26, 1\n"
    "s_and_b32 s58, s58, 3\n"
    "v_mfma_f32_16x16x32_bf16 v[84:87], a[32:35], v[172:175], v[84:87]\n"
    "s_mul_i32 s58, s58, 15360\n"
    "s_add_u32 s58, s58, 0x0\n"
    "v_mfma_f32_16x16x32_bf16 v[88:91], a[36:39], v[172:175], v[88:91]\n"
    "v_add_u32_e32 v9, s58, v0\n"
    "ds_write_b128 v9, v[36:39]\n"
    "v_mfma_f32_16x16x32_bf16 v[92:95], a[40:43], v[172:175], v[92:95]\n"
    "ds_write_b128 v9, v[40:43] offset:1024\n"
    "ds_write_b128 v9, v[44:47] offset:2048\n"
    "ds_write_b128 v9, v[48:51] offset:3072\n"
    "ds_write_b128 v9, v[52:55] offset:4096\n"
    "v_mfma_f32_16x16x32_bf16 v[16:19], a[24:27], v[160:163], v[16:19]\n"
    "v_mfma_f32_16x16x32_bf16 v[20:23], a[28:31], v[160:163], v[20:23]\n"
    "s_add_u32 s58, s26, 2\n"
    "s_and_b32 s58, s58, 3\n"
    "v_mfma_f32_16x16x32_bf16 v[24:27], a[32:35], v[160:163], v[24:27]\n"
    "s_mul_i32 s58, s58, 15360\n"
    "s_add_u32 s58, s58, 0x1400\n"
    "v_mfma_f32_16x16x32_bf16 v[28:31], a[36:39], v[160:163], v[28:31]\n"
    "v_add_u32_e32 v9, s58, v0\n"
    "ds_write_b128 v9, v[56:59]\n"
    "v_mfma_f32_16x16x32_bf16 v[32:35], a[40:43], v[160:163], v[32:35]\n"
    "ds_write_b128 v9, v[60:63] offset:1024\n"
    "ds_write_b128 v9, v[64:67] offset:2048\n"
    "ds_write_b128 v9, v[68:71] offset:3072\n"
    "ds_write_b128 v9, v[72:75] offset:4096\n"
    "s_add_u32 s58, s26, 3\n"
    "s_and_b32 s58, s58, 3\n"
    "s_mul_i32 s58, s58, 15360\n"
    "s_add_u32 s58, s58, 0x2800\n"
    "v_add_u32_e32 v9, s58, v0\n"
    "ds_write_b128 v9, v[76:79]\n"
    "ds_write_b128 v9, v[80:83] offset:1024\n"
    "ds_write_b128 v9, v[84:87] offset:2048\n"
    "ds_write_b128 v9, v[88:91] offset:3072\n"
    "ds_write_b128 v9, v[92:95] offset:4096\n"
    "s_mul_i32 s61, s29, 0x0\n"
    "s_lshr_b32 s59, s33, 1\n"
    "s_mul_i32 s59, s59, 40960\n"
    "s_add_u32 s61, s61, s59\n"
    "s_mul_i32 s59, s31, 5120\n"
    "s_add_u32 s61, s61, s59\n"
    "s_add_u32 s61, s61, 0xd2d800\n"
    "s_mul_i32 s62, s29, 0x0\n"
    "s_lshl_b32 s59, s33, 3\n"
    "s_add_u32 s59, s59, s31\n"
    "s_lshl_b32 s59, s59, 7\n"
    "s_add_u32 s62, s62, s59\n"
    "s_add_u32 s62, s62, 0x126d800\n"
    "s_and_b32 s59, s33, 1\n"
    "s_lshl_b32 s59, s59, 8\n"
    "v_and_b32_e32 v10, 0xf0, v0\n"
    "v_and_b32_e32 v12, 0x200, v0\n"
    "v_or_b32_e32 v10, v10, v12\n"
    "v_bfe_u32 v12, v0, 8, 1\n"
    "v_lshl_add_u32 v10, v12, 3, v10\n"
    "v_add_u32_e32 v10, s59, v10\n"
    "v_lshrrev_b32_e32 v11, 1, v0\n"
    "v_mov_b32_e32 v12, 0x7c000000\n"
    "v_cmp_gt_u32_e32 vcc, 0x100, v0\n"
    "s_nop 1\n"
    "v_cndmask_b32_e32 v11, v12, v11, vcc\n"
    "s_mul_i32 s58, s26, 15360\n"
    "s_add_u32 s58, s58, 0x0\n"
    "v_add_u32_e32 v9, s58, v0\n"
    "s_waitcnt lgkmcnt(0)\n"
    "s_barrier\n"
    "ds_read_b128 v[36:39], v9\n"
    "ds_read_b128 v[56:59], v9 offset:5120\n"
    "ds_read_b128 v[76:79], v9 offset:10240\n"
    "ds_read_b128 v[40:43], v9 offset:1024\n"
    "ds_read_b128 v[60:63], v9 offset:6144\n"
    "ds_read_b128 v[80:83], v9 offset:11264\n"
    "ds_read_b128 v[44:47], v9 offset:2048\n"
    "ds_read_b128 v[64:67], v9 offset:7168\n"
    "ds_read_b128 v[84:87], v9 offset:12288\n"
    "ds_read_b128 v[48:51], v9 offset:3072\n"
    "ds_read_b128 v[68:71], v9 offset:8192\n"
    "ds_read_b128 v[88:91], v9 offset:13312\n"
    "ds_read_b128 v[52:55], v9 offset:4096\n"
    "ds_read_b128 v[72:75], v9 offset:9216\n"
    "ds_read_b128 v[92:95], v9 offset:14336\n"
    "s_waitcnt lgkmcnt(13)\n"
    "v_pk_add_f32 v[36:37], v[36:37], v[56:57]\n"
    "s_waitcnt lgkmcnt(12)\n"
    "v_pk_add_f32 v[16:17], v[16:17], v[76:77]\n"
    "v_pk_add_f32 v[38:39], v[38:39], v[58:59]\n"
    "v_pk_add_f32 v[18:19], v[18:19], v[78:79]\n"
    "v_pk_add_f32 v[16:17], v[16:17], v[36:37]\n"
    "v_pk_add_f32 v[18:19], v[18:19], v[38:39]\n"
    "s_waitcnt lgkmcnt(10)\n"
    "v_pk_add_f32 v[40:41], v[40:41], v[60:61]\n"
    "s_waitcnt lgkmcnt(9)\n"
    "v_pk_add_f32 v[20:21], v[20:21], v[80:81]\n"
    "v_pk_add_f32 v[42:43], v[42:43], v[62:63]\n"
    "v_pk_add_f32 v[22:23], v[22:23], v[82:83]\n"
    "v_pk_add_f32 v[20:21], v[20:21], v[40:41]\n"
    "v_pk_add_f32 v[22:23], v[22:23], v[42:43]\n"
    "s_waitcnt lgkmcnt(7)\n"
    "v_pk_add_f32 v[44:45], v[44:45], v[64:65]\n"
    "s_waitcnt lgkmcnt(6)\n"
    "v_pk_add_f32 v[24:25], v[24:25], v[84:85]\n"
    "v_pk_add_f32 v[46:47], v[46:47], v[66:67]\n"
    "v_pk_add_f32 v[26:27], v[26:27], v[86:87]\n"
    "v_pk_add_f32 v[24:25], v[24:25], v[44:45]\n"
    "v_pk_add_f32 v[26:27], v[26:27], v[46:47]\n"
    "s_waitcnt lgkmcnt(4)\n"
    "v_pk_add_f32 v[48:49], v[48:49], v[68:69]\n"
    "s_waitcnt lgkmcnt(3)\n"
    "v_pk_add_f32 v[28:29], v[28:29], v[88:89]\n"
    "v_pk_add_f32 v[50:51], v[50:51], v[70:71]\n"
    "v_pk_add_f32 v[30:31], v[30:31], v[90:91]\n"
    "v_pk_add_f32 v[28:29], v[28:29], v[48:49]\n"
    "v_pk_add_f32 v[30:31], v[30:31], v[50:51]\n"
    "s_waitcnt lgkmcnt(1)\n"
    "v_pk_add_f32 v[52:53], v[52:53], v[72:73]\n"
    "s_waitcnt lgkmcnt(0)\n"
    "v_pk_add_f32 v[32:33], v[32:33], v[92:93]\n"
    "v_pk_add_f32 v[54:55], v[54:55], v[74:75]\n"
    "v_pk_add_f32 v[34:35], v[34:35], v[94:95]\n"
    "v_pk_add_f32 v[32:33], v[32:33], v[52:53]\n"
    "v_pk_add_f32 v[34:35], v[34:35], v[54:55]\n"
    "v_fma_f32 v16, v16, v7, v186\n"
    "v_fma_f32 v17, v17, v7, v187\n"
    "v_fma_f32 v18, v18, v7, v188\n"
    "v_fma_f32 v19, v19, v7, v189\n"
    "v_fma_f32 v20, v20, v7, v190\n"
    "v_fma_f32 v21, v21, v7, v191\n"
    "v_fma_f32 v22, v22, v7, v192\n"
    "v_fma_f32 v23, v23, v7, v193\n"
    "v_fma_f32 v24, v24, v7, v194\n"
    "v_fma_f32 v25, v25, v7, v195\n"
    "v_fma_f32 v26, v26, v7, v196\n"
    "v_fma_f32 v27, v27, v7, v197\n"
    "v_fma_f32 v28, v28, v7, v198\n"
    "v_fma_f32 v29, v29, v7, v199\n"
    "v_fma_f32 v30, v30, v7, v200\n"
    "v_fma_f32 v31, v31, v7, v201\n"
    "v_fma_f32 v32, v32, v7, v202\n"
    "v_fma_f32 v33, v33, v7, v203\n"
    "v_fma_f32 v34, v34, v7, v204\n"
    "v_fma_f32 v35, v35, v7, v205\n"
    "s_cmp_eq_u32 s29, 0\n"
    "s_cbranch_scc0 .Lmk_out\n"
    "s_mul_i32 s90, s28, 0x25800\n"
    "s_add_u32 s90, s90, 0xae000\n"
    "s_mul_i32 s91, s26, 10240\n"
    "s_add_u32 s90, s90, s91\n"
    "s_add_u32 s91, s91, 0xa000\n"
    "s_add_u32 s57, s61, 0x1000\n"
    "v_cvt_pk_bf16_f32 v100, v16, v17\n"
    "v_cvt_pk_bf16_f32 v101, v18, v19\n"
    "buffer_store_dwordx2 v[100:101], v10, s[16:19], s61 offen sc1\n"
    "v_cvt_pk_bf16_f32 v102, v20, v21\n"
    "v_cvt_pk_bf16_f32 v103, v22, v23\n"
    "buffer_store_dwordx2 v[102:103], v10, s[16:19], s61 offen offset:1024 sc1\n"
    "v_cvt_pk_bf16_f32 v104, v24, v25\n"
    "v_cvt_pk_bf16_f32 v105, v26, v27\n"
    "buffer_store_dwordx2 v[104:105], v10, s[16:19], s61 offen offset:2048 sc1\n"
    "v_cvt_pk_bf16_f32 v106, v28, v29\n"
    "v_cvt_pk_bf16_f32 v107, v30, v31\n"
    "buffer_store_dwordx2 v[106:107], v10, s[16:19], s61 offen offset:3072 sc1\n"
    "v_cvt_pk_bf16_f32 v108, v32, v33\n"
    "v_cvt_pk_bf16_f32 v109, v34, v35\n"
    "buffer_store_dwordx2 v[108:109], v10, s[16:19], s57 offen sc1\n"
    "v_pk_add_f32 v[110:111], v[16:17], v[18:19]\n"
    "v_pk_add_f32 v[112:113], v[20:21], v[22:23]\n"
    "v_pk_add_f32 v[110:111], v[110:111], v[112:113]\n"
    "v_pk_add_f32 v[112:113], v[24:25], v[26:27]\n"
    "v_pk_add_f32 v[110:111], v[110:111], v[112:113]\n"
    "v_pk_add_f32 v[112:113], v[28:29], v[30:31]\n"
    "v_pk_add_f32 v[110:111], v[110:111], v[112:113]\n"
    "v_pk_add_f32 v[112:113], v[32:33], v[34:35]\n"
    "v_pk_add_f32 v[110:111], v[110:111], v[112:113]\n"
    "v_add_f32_e32 v110, v110, v111\n"
    "v_mov_b32_e32 v111, v110\n"
    "s_nop 1\n"
    "v_permlane16_swap_b32 v111, v110\n"
    "v_add_f32_e32 v110, v111, v110\n"
    "v_mov_b32_e32 v111, v110\n"
    "s_nop 1\n"
    "v_permlane32_swap_b32 v111, v110\n"
    "v_add_f32_e32 v110, v111, v110\n"
    "v_mul_f32_e32 v110, 0x3c4ccccd, v110\n"
    "v_sub_f32_e32 v112, 0, v110\n"
    "v_mov_b32_e32 v113, v112\n"
    "v_pk_add_f32 v[16:17], v[16:17], v[112:113]\n"
    "v_pk_mul_f32 v[114:115], v[16:17], v[16:17]\n"
    "v_pk_add_f32 v[18:19], v[18:19], v[112:113]\n"
    "v_pk_fma_f32 v[114:115], v[18:19], v[18:19], v[114:115]\n"
    "v_pk_add_f32 v[20:21], v[20:21], v[112:113]\n"
    "v_pk_fma_f32 v[114:115], v[20:21], v[20:21], v[114:115]\n"
    "v_pk_add_f32 v[22:23], v[22:23], v[112:113]\n"
    "v_pk_fma_f32 v[114:115], v[22:23], v[22:23], v[114:115]\n"
    "v_pk_add_f32 v[24:25], v[24:25], v[112:113]\n"
    "v_pk_fma_f32 v[114:115], v[24:25], v[24:25], v[114:115]\n"
    "v_pk_add_f32 v[26:27], v[26:27], v[112:113]\n"
    "v_pk_fma_f32 v[114:115], v[26:27], v[26:27], v[114:115]\n"
    "v_pk_add_f32 v[28:29], v[28:29], v[112:113]\n"
    "v_pk_fma_f32 v[114:115], v[28:29], v[28:29], v[114:115]\n"
    "v_pk_add_f32 v[30:31], v[30:31], v[112:113]\n"
    "v_pk_fma_f32 v[114:115], v[30:31], v[30:31], v[114:115]\n"
    "v_pk_add_f32 v[32:33], v[32:33], v[112:113]\n"
    "v_pk_fma_f32 v[114:115], v[32:33], v[32:33], v[114:115]\n"
    "v_pk_add_f32 v[34:35], v[34:35], v[112:113]\n"
    "v_pk_fma_f32 v[114:115], v[34:35], v[34:35], v[114:115]\n"
    "v_add_f32_e32 v111, v114, v115\n"
    "v_mov_b32_e32 v112, v111\n"
    "s_nop 1\n"
    "v_permlane16_swap_b32 v112, v111\n"
    "v_add_f32_e32 v111, v112, v111\n"
    "v_mov_b32_e32 v112, v111\n"
    "s_nop 1\n"
    "v_permlane32_swap_b32 v112, v111\n"
    "v_add_f32_e32 v111, v112, v111\n"
    "buffer_store_dwordx2 v[110:111], v11, s[16:19], s62 offen sc1\n"
    "v_accvgpr_write_b32 a0, v100\n"
    "v_accvgpr_write_b32 a1, v101\n"
    "v_accvgpr_write_b32 a2, v102\n"
    "v_accvgpr_write_b32 a3, v103\n"
    "v_accvgpr_write_b32 a4, v104\n"
    "v_accvgpr_write_b32 a5, v105\n"
    "v_accvgpr_write_b32 a6, v106\n"
    "v_accvgpr_write_b32 a7, v107\n"
    "v_accvgpr_write_b32 a8, v108\n"
    "v_accvgpr_write_b32 a9, v109\n"
    "s_barrier\n"
    "s_mov_b32 m0, s91\n"
    "s_nop 0\n"
    "buffer_load_dwordx4 v0, s[16:19], s90 offen sc1 lds\n"
    "buffer_load_dwordx4 v0, s[16:19], s90 offen offset:1024 sc1 lds\n"
    "buffer_load_dwordx4 v0, s[16:19], s90 offen offset:2048 sc1 lds\n"
    "buffer_load_dwordx4 v0, s[16:19], s90 offen offset:3072 sc1 lds\n"
    "s_add_u32 m0, m0, 0x1000\n"
    "s_add_u32 s90, s90, 0x1000\n"
    "buffer_load_dwordx4 v0, s[16:19], s90 offen sc1 lds\n"
    "buffer_load_dwordx4 v0, s[16:19], s90 offen offset:1024 sc1 lds\n"
    "buffer_load_dwordx4 v0, s[16:19], s90 offen offset:2048 sc1 lds\n"
    "buffer_load_dwordx4 v0, s[16:19], s90 offen offset:3072 sc1 lds\n"
    "s_add_u32 m0, m0, 0x1000\n"
    "s_add_u32 s90, s90, 0x1000\n"
    "buffer_load_dwordx4 v0, s[16:19], s90 offen sc1 lds\n"
    "buffer_load_dwordx4 v0, s[16:19], s90 offen offset:1024 sc1 lds\n"
    "s_mov_b32 s29, 1\n"
    "s_branch .Lmk_layer\n"
    ".Lmk_fix_47:\n"
    "s_nop 7\n"
    "s_mov_b32 s50, s35\n"
    "s_lshl_b32 s52, s27, 2\n"
    "s_sub_i32 s52, 8, s52\n"
    "s_max_i32 s52, s52, s50\n"
    "v_mov_b32_e32 v102, 0\n"
    "v_mov_b32_e32 v103, 0\n"
    "v_mov_b32_e32 v9, 0xff7fffff\n"
    "s_mov_b32 s51, s52\n"
    ".Lmk_fb_103:\n"
    "s_mul_i32 s53, s51, 6656\n"
    "v_add_u32_e32 v10, s53, v12\n"
    "ds_read_b128 v[184:187], v10\n"
    "v_add_u32_e32 v10, s53, v13\n"
    "ds_read_b64 v[188:189], v10\n"
    "v_add_u32_e32 v10, s53, v14\n"
    "ds_read_b128 v[200:203], v10\n"
    "ds_read_b128 v[204:207], v10 offset:1024\n"
    "s_add_u32 s54, s50, 8\n"
    "s_cmp_eq_u32 s51, s54\n"
    "s_cselect_b64 s[82:83], -1, 0\n"
    "s_cmp_eq_u32 s51, s50\n"
    "s_cselect_b64 s[84:85], -1, 0\n"
    "v_cndmask_b32_e64 v144, 0, v216, s[82:83]\n"
    "v_cndmask_b32_e64 v144, v144, v232, s[84:85]\n"
    "v_cndmask_b32_e64 v145, 0, v217, s[82:83]\n"
    "v_cndmask_b32_e64 v145, v145, v233, s[84:85]\n"
    "v_cndmask_b32_e64 v146, 0, v218, s[82:83]\n"
    "v_cndmask_b32_e64 v146, v146, v234, s[84:85]\n"
    "v_cndmask_b32_e64 v147, 0, v219, s[82:83]\n"
    "v_cndmask_b32_e64 v147, v147, v235, s[84:85]\n"
    "v_cndmask_b32_e64 v148, 0, v220, s[82:83]\n"
    "v_cndmask_b32_e64 v148, v148, v236, s[84:85]\n"
    "v_cndmask_b32_e64 v149, 0, v221, s[82:83]\n"
    "v_cndmask_b32_e64 v149, v149, v237, s[84:85]\n"
    "v_cndmask_b32_e64 v150, 0, v222, s[82:83]\n"
    "v_cndmask_b32_e64 v150, v150, v238, s[84:85]\n"
    "v_cndmask_b32_e64 v151, 0, v223, s[82:83]\n"
    "v_cndmask_b32_e64 v151, v151, v239, s[84:85]\n"
    "v_cndmask_b32_e64 v152, 0, v224, s[82:83]\n"
    "v_cndmask_b32_e64 v152, v152, v240, s[84:85]\n"
    "v_cndmask_b32_e64 v153, 0, v225, s[82:83]\n"
    "v_cndmask_b32_e64 v153, v153, v241, s[84:85]\n"
    "v_cndmask_b32_e64 v154, 0, v226, s[82:83]\n"
    "v_cndmask_b32_e64 v154, v154, v242, s[84:85]\n"
    "v_cndmask_b32_e64 v155, 0, v227, s[82:83]\n"
    "v_cndmask_b32_e64 v155, v155, v243, s[84:85]\n"
    "v_cndmask_b32_e64 v156, 0, v228, s[82:83]\n"
    "v_cndmask_b32_e64 v156, v156, v244, s[84:85]\n"
    "v_cndmask_b32_e64 v157, 0, v229, s[82:83]\n"
    "v_cndmask_b32_e64 v157, v157, v245, s[84:85]\n"
    "v_cndmask_b32_e64 v158, 0, v230, s[82:83]\n"
    "v_cndmask_b32_e64 v158, v158, v246, s[84:85]\n"
    "v_cndmask_b32_e64 v159, 0, v231, s[82:83]\n"
    "v_cndmask_b32_e64 v159, v159, v247, s[84:85]\n"
    "s_waitcnt lgkmcnt(0)\n"
    "s_nop 1\n"
    "v_mfma_f32_32x32x16_bf16 v[144:159], v[184:187], v[96:99], v[144:159]\n"
    "v_mfma_f32_32x32x16_bf16 v[144:159], v[188:191], v[100:103], v[144:159]\n"
    "s_nop 11\n"
    "v_max3_f32 v9, v144, v145, v9\n"
    "v_max3_f32 v9, v146, v147, v9\n"
    "v_max3_f32 v9, v148, v149, v9\n"
    "v_max3_f32 v9, v150, v151, v9\n"
    "v_max3_f32 v9, v152, v153, v9\n"
    "v_max3_f32 v9, v154, v155, v9\n"
    "v_max3_f32 v9, v156, v157, v9\n"
    "v_max3_f32 v9, v158, v159, v9\n"
    "s_nop 12\n"
    "s_add_u32 s51, s51, 1\n"
    "s_add_u32 s54, s50, 8\n"
    "s_cmp_le_u32 s51, s54\n"
    "s_cbranch_scc1 .Lmk_fb_103\n"
    "v_mov_b32_e32 v10, v9\n"
    "s_nop 1\n"
    "v_permlane32_swap_b32 v10, v9\n"
    "v_max_f32_e32 v9, v10, v9\n"
    "v_sub_f32_e32 v9, 0, v9\n"
    "v_cvt_pk_bf16_f32 v10, v9, v9\n"
    "v_lshlrev_b32_e32 v11, 16, v10\n"
    "v_sub_f32_e32 v11, v9, v11\n"
    "v_cvt_pk_bf16_f32 v102, v9, v11\n"
    "v_and_b32_e32 v10, 0xffff0000, v102\n"
    "v_sub_f32_e32 v11, v11, v10\n"
    "v_cvt_pk_bf16_f32 v103, v11, 0\n"
    "v_cndmask_b32_e64 v102, 0, v102, s[80:81]\n"
    "v_cndmask_b32_e64 v103, 0, v103, s[80:81]\n"
    "s_nop 1\n"
    "v_mov_b32_e32 v112, 0\n"
    "v_mov_b32_e32 v113, 0\n"
    "v_mov_b32_e32 v114, 0\n"
    "v_mov_b32_e32 v115, 0\n"
    "v_mov_b32_e32 v116, 0\n"
    "v_mov_b32_e32 v117, 0\n"
    "v_mov_b32_e32 v118, 0\n"
    "v_mov_b32_e32 v119, 0\n"
    "v_mov_b32_e32 v120, 0\n"
    "v_mov_b32_e32 v121, 0\n"
    "v_mov_b32_e32 v122, 0\n"
    "v_mov_b32_e32 v123, 0\n"
    "v_mov_b32_e32 v124, 0\n"
    "v_mov_b32_e32 v125, 0\n"
    "v_mov_b32_e32 v126, 0\n"
    "v_mov_b32_e32 v127, 0\n"
    "s_mov_b32 s51, s52\n"
    ".Lmk_fb_106:\n"
    "s_mul_i32 s53, s51, 6656\n"
    "v_add_u32_e32 v10, s53, v12\n"
    "ds_read_b128 v[184:187], v10\n"
    "v_add_u32_e32 v10, s53, v13\n"
    "ds_read_b64 v[188:189], v10\n"
    "v_add_u32_e32 v10, s53, v14\n"
    "ds_read_b128 v[200:203], v10\n"
    "ds_read_b128 v[204:207], v10 offset:1024\n"
    "s_add_u32 s54, s50, 8\n"
    "s_cmp_eq_u32 s51, s54\n"
    "s_cselect_b64 s[82:83], -1, 0\n"
    "s_cmp_eq_u32 s51, s50\n"
    "s_cselect_b64 s[84:85], -1, 0\n"
    "v_cndmask_b32_e64 v144, 0, v216, s[82:83]\n"
    "v_cndmask_b32_e64 v144, v144, v232, s[84:85]\n"
    "v_cndmask_b32_e64 v145, 0, v217, s[82:83]\n"
    "v_cndmask_b32_e64 v145, v145, v233, s[84:85]\n"
    "v_cndmask_b32_e64 v146, 0, v218, s[82:83]\n"
    "v_cndmask_b32_e64 v146, v146, v234, s[84:85]\n"
    "v_cndmask_b32_e64 v147, 0, v219, s[82:83]\n"
    "v_cndmask_b32_e64 v147, v147, v235, s[84:85]\n"
    "v_cndmask_b32_e64 v148, 0, v220, s[82:83]\n"
    "v_cndmask_b32_e64 v148, v148, v236, s[84:85]\n"
    "v_cndmask_b32_e64 v149, 0, v221, s[82:83]\n"
    "v_cndmask_b32_e64 v149, v149, v237, s[84:85]\n"
    "v_cndmask_b32_e64 v150, 0, v222, s[82:83]\n"
    "v_cndmask_b32_e64 v150, v150, v238, s[84:85]\n"
    "v_cndmask_b32_e64 v151, 0, v223, s[82:83]\n"
    "v_cndmask_b32_e64 v151, v151, v239, s[84:85]\n"
    "v_cndmask_b32_e64 v152, 0, v224, s[82:83]\n"
    "v_cndmask_b32_e64 v152, v152, v240, s[84:85]\n"
    "v_cndmask_b32_e64 v153, 0, v225, s[82:83]\n"
    "v_cndmask_b32_e64 v153, v153, v241, s[84:85]\n"
    "v_cndmask_b32_e64 v154, 0, v226, s[82:83]\n"
    "v_cndmask_b32_e64 v154, v154, v242, s[84:85]\n"
    "v_cndmask_b32_e64 v155, 0, v227, s[82:83]\n"
    "v_cndmask_b32_e64 v155, v155, v243, s[84:85]\n"
    "v_cndmask_b32_e64 v156, 0, v228, s[82:83]\n"
    "v_cndmask_b32_e64 v156, v156, v244, s[84:85]\n"
    "v_cndmask_b32_e64 v157, 0, v229, s[82:83]\n"
    "v_cndmask_b32_e64 v157, v157, v245, s[84:85]\n"
    "v_cndmask_b32_e64 v158, 0, v230, s[82:83]\n"
    "v_cndmask_b32_e64 v158, v158, v246, s[84:85]\n"
    "v_cndmask_b32_e64 v159, 0, v231, s[82:83]\n"
    "v_cndmask_b32_e64 v159, v159, v247, s[84:85]\n"
    "s_waitcnt lgkmcnt(0)\n"
    "s_nop 1\n"
    "v_mfma_f32_32x32x16_bf16 v[144:159], v[184:187], v[96:99], v[144:159]\n"
    "v_mfma_f32_32x32x16_bf16 v[144:159], v[188:191], v[100:103], v[144:159]\n"
    "s_nop 11\n"
    "v_exp_f32_e32 v144, v144\n"
    "v_exp_f32_e32 v145, v145\n"
    "v_exp_f32_e32 v146, v146\n"
    "v_exp_f32_e32 v147, v147\n"
    "v_exp_f32_e32 v148, v148\n"
    "v_exp_f32_e32 v149, v149\n"
    "v_exp_f32_e32 v150, v150\n"
    "v_exp_f32_e32 v151, v151\n"
    "v_exp_f32_e32 v152, v152\n"
    "v_exp_f32_e32 v153, v153\n"
    "v_exp_f32_e32 v154, v154\n"
    "v_exp_f32_e32 v155, v155\n"
    "v_exp_f32_e32 v156, v156\n"
    "v_exp_f32_e32 v157, v157\n"
    "v_exp_f32_e32 v158, v158\n"
    "v_exp_f32_e32 v159, v159\n"
    "v_cvt_pk_bf16_f32 v176, v144, v145\n"
    "v_cvt_pk_bf16_f32 v177, v146, v147\n"
    "v_cvt_pk_bf16_f32 v178, v148, v149\n"
    "v_cvt_pk_bf16_f32 v179, v150, v151\n"
    "v_cvt_pk_bf16_f32 v180, v152, v153\n"
    "v_cvt_pk_bf16_f32 v181, v154, v155\n"
    "v_cvt_pk_bf16_f32 v182, v156, v157\n"
    "v_cvt_pk_bf16_f32 v183, v158, v159\n"
    "v_mfma_f32_32x32x16_bf16 v[112:127], v[200:203], v[176:179], v[112:127]\n"
    "s_nop 0\n"
    "v_mfma_f32_32x32x16_bf16 v[112:127], v[204:207], v[180:183], v[112:127]\n"
    "s_nop 12\n"
    "s_add_u32 s51, s51, 1\n"
    "s_add_u32 s54, s50, 8\n"
    "s_cmp_le_u32 s51, s54\n"
    "s_cbranch_scc1 .Lmk_fb_106\n"
    "s_sub_u32 s50, 3, s35\n"
    "s_lshl_b32 s52, s27, 2\n"
    "s_sub_i32 s52, 8, s52\n"
    "s_max_i32 s52, s52, s50\n"
    "v_mov_b32_e32 v110, 0\n"
    "v_mov_b32_e32 v111, 0\n"
    "v_mov_b32_e32 v9, 0xff7fffff\n"
    "s_mov_b32 s51, s52\n"
    ".Lmk_fb_109:\n"
    "s_mul_i32 s53, s51, 6656\n"
    "v_add_u32_e32 v10, s53, v12\n"
    "ds_read_b128 v[184:187], v10\n"
    "v_add_u32_e32 v10, s53, v13\n"
    "ds_read_b64 v[188:189], v10\n"
    "v_add_u32_e32 v10, s53, v14\n"
    "ds_read_b128 v[200:203], v10\n"
    "ds_read_b128 v[204:207], v10 offset:1024\n"
    "s_add_u32 s54, s50, 8\n"
    "s_cmp_eq_u32 s51, s54\n"
    "s_cselect_b64 s[82:83], -1, 0\n"
    "s_cmp_eq_u32 s51, s50\n"
    "s_cselect_b64 s[84:85], -1, 0\n"
    "v_cndmask_b32_e64 v144, 0, v216, s[82:83]\n"
    "v_cndmask_b32_e64 v144, v144, v232, s[84:85]\n"
    "v_cndmask_b32_e64 v145, 0, v217, s[82:83]\n"
    "v_cndmask_b32_e64 v145, v145, v233, s[84:85]\n"
    "v_cndmask_b32_e64 v146, 0, v218, s[82:83]\n"
    "v_cndmask_b32_e64 v146, v146, v234, s[84:85]\n"
    "v_cndmask_b32_e64 v147, 0, v219, s[82:83]\n"
    "v_cndmask_b32_e64 v147, v147, v235, s[84:85]\n"
    "v_cndmask_b32_e64 v148, 0, v220, s[82:83]\n"
    "v_cndmask_b32_e64 v148, v148, v236, s[84:85]\n"
    "v_cndmask_b32_e64 v149, 0, v221, s[82:83]\n"
    "v_cndmask_b32_e64 v149, v149, v237, s[84:85]\n"
    "v_cndmask_b32_e64 v150, 0, v222, s[82:83]\n"
    "v_cndmask_b32_e64 v150, v150, v238, s[84:85]\n"
    "v_cndmask_b32_e64 v151, 0, v223, s[82:83]\n"
    "v_cndmask_b32_e64 v151, v151, v239, s[84:85]\n"
    "v_cndmask_b32_e64 v152, 0, v224, s[82:83]\n"
    "v_cndmask_b32_e64 v152, v152, v240, s[84:85]\n"
    "v_cndmask_b32_e64 v153, 0, v225, s[82:83]\n"
    "v_cndmask_b32_e64 v153, v153, v241, s[84:85]\n"
    "v_cndmask_b32_e64 v154, 0, v226, s[82:83]\n"
    "v_cndmask_b32_e64 v154, v154, v242, s[84:85]\n"
    "v_cndmask_b32_e64 v155, 0, v227, s[82:83]\n"
    "v_cndmask_b32_e64 v155, v155, v243, s[84:85]\n"
    "v_cndmask_b32_e64 v156, 0, v228, s[82:83]\n"
    "v_cndmask_b32_e64 v156, v156, v244, s[84:85]\n"
    "v_cndmask_b32_e64 v157, 0, v229, s[82:83]\n"
    "v_cndmask_b32_e64 v157, v157, v245, s[84:85]\n"
    "v_cndmask_b32_e64 v158, 0, v230, s[82:83]\n"
    "v_cndmask_b32_e64 v158, v158, v246, s[84:85]\n"
    "v_cndmask_b32_e64 v159, 0, v231, s[82:83]\n"
    "v_cndmask_b32_e64 v159, v159, v247, s[84:85]\n"
    "s_waitcnt lgkmcnt(0)\n"
    "s_nop 1\n"
    "v_mfma_f32_32x32x16_bf16 v[144:159], v[184:187], v[104:107], v[144:159]\n"
    "v_mfma_f32_32x32x16_bf16 v[144:159], v[188:191], v[108:111], v[144:159]\n"
    "s_nop 11\n"
    "v_max3_f32 v9, v144, v145, v9\n"
    "v_max3_f32 v9, v146, v147, v9\n"
    "v_max3_f32 v9, v148, v149, v9\n"
    "v_max3_f32 v9, v150, v151, v9\n"
    "v_max3_f32 v9, v152, v153, v9\n"
    "v_max3_f32 v9, v154, v155, v9\n"
    "v_max3_f32 v9, v156, v157, v9\n"
    "v_max3_f32 v9, v158, v159, v9\n"
    "s_nop 12\n"
    "s_add_u32 s51, s51, 1\n"
    "s_add_u32 s54, s50, 8\n"
    "s_cmp_le_u32 s51, s54\n"
    "s_cbranch_scc1 .Lmk_fb_109\n"
    "v_mov_b32_e32 v10, v9\n"
    "s_nop 1\n"
    "v_permlane32_swap_b32 v10, v9\n"
    "v_max_f32_e32 v9, v10, v9\n"
    "v_sub_f32_e32 v9, 0, v9\n"
    "v_cvt_pk_bf16_f32 v10, v9, v9\n"
    "v_lshlrev_b32_e32 v11, 16, v10\n"
    "v_sub_f32_e32 v11, v9, v11\n"
    "v_cvt_pk_bf16_f32 v110, v9, v11\n"
    "v_and_b32_e32 v10, 0xffff0000, v110\n"
    "v_sub_f32_e32 v11, v11, v10\n"
    "v_cvt_pk_bf16_f32 v111, v11, 0\n"
    "v_cndmask_b32_e64 v110, 0, v110, s[80:81]\n"
    "v_cndmask_b32_e64 v111, 0, v111, s[80:81]\n"
    "s_nop 1\n"
    "v_mov_b32_e32 v128, 0\n"
    "v_mov_b32_e32 v129, 0\n"
    "v_mov_b32_e32 v130, 0\n"
    "v_mov_b32_e32 v131, 0\n"
    "v_mov_b32_e32 v132, 0\n"
    "v_mov_b32_e32 v133, 0\n"
    "v_mov_b32_e32 v134, 0\n"
    "v_mov_b32_e32 v135, 0\n"
    "v_mov_b32_e32 v136, 0\n"
    "v_mov_b32_e32 v137, 0\n"
    "v_mov_b32_e32 v138, 0\n"
    "v_mov_b32_e32 v139, 0\n"
    "v_mov_b32_e32 v140, 0\n"
    "v_mov_b32_e32 v141, 0\n"
    "v_mov_b32_e32 v142, 0\n"
    "v_mov_b32_e32 v143, 0\n"
    "s_mov_b32 s51, s52\n"
    ".Lmk_fb_112:\n"
    "s_mul_i32 s53, s51, 6656\n"
    "v_add_u32_e32 v10, s53, v12\n"
    "ds_read_b128 v[184:187], v10\n"
    "v_add_u32_e32 v10, s53, v13\n"
    "ds_read_b64 v[188:189], v10\n"
    "v_add_u32_e32 v10, s53, v14\n"
    "ds_read_b128 v[200:203], v10\n"
    "ds_read_b128 v[204:207], v10 offset:1024\n"
    "s_add_u32 s54, s50, 8\n"
    "s_cmp_eq_u32 s51, s54\n"
    "s_cselect_b64 s[82:83], -1, 0\n"
    "s_cmp_eq_u32 s51, s50\n"
    "s_cselect_b64 s[84:85], -1, 0\n"
    "v_cndmask_b32_e64 v144, 0, v216, s[82:83]\n"
    "v_cndmask_b32_e64 v144, v144, v232, s[84:85]\n"
    "v_cndmask_b32_e64 v145, 0, v217, s[82:83]\n"
    "v_cndmask_b32_e64 v145, v145, v233, s[84:85]\n"
    "v_cndmask_b32_e64 v146, 0, v218, s[82:83]\n"
    "v_cndmask_b32_e64 v146, v146, v234, s[84:85]\n"
    "v_cndmask_b32_e64 v147, 0, v219, s[82:83]\n"
    "v_cndmask_b32_e64 v147, v147, v235, s[84:85]\n"
    "v_cndmask_b32_e64 v148, 0, v220, s[82:83]\n"
    "v_cndmask_b32_e64 v148, v148, v236, s[84:85]\n"
    "v_cndmask_b32_e64 v149, 0, v221, s[82:83]\n"
    "v_cndmask_b32_e64 v149, v149, v237, s[84:85]\n"
    "v_cndmask_b32_e64 v150, 0, v222, s[82:83]\n"
    "v_cndmask_b32_e64 v150, v150, v238, s[84:85]\n"
    "v_cndmask_b32_e64 v151, 0, v223, s[82:83]\n"
    "v_cndmask_b32_e64 v151, v151, v239, s[84:85]\n"
    "v_cndmask_b32_e64 v152, 0, v224, s[82:83]\n"
    "v_cndmask_b32_e64 v152, v152, v240, s[84:85]\n"
    "v_cndmask_b32_e64 v153, 0, v225, s[82:83]\n"
    "v_cndmask_b32_e64 v153, v153, v241, s[84:85]\n"
    "v_cndmask_b32_e64 v154, 0, v226, s[82:83]\n"
    "v_cndmask_b32_e64 v154, v154, v242, s[84:85]\n"
    "v_cndmask_b32_e64 v155, 0, v227, s[82:83]\n"
    "v_cndmask_b32_e64 v155, v155, v243, s[84:85]\n"
    "v_cndmask_b32_e64 v156, 0, v228, s[82:83]\n"
    "v_cndmask_b32_e64 v156, v156, v244, s[84:85]\n"
    "v_cndmask_b32_e64 v157, 0, v229, s[82:83]\n"
    "v_cndmask_b32_e64 v157, v157, v245, s[84:85]\n"
    "v_cndmask_b32_e64 v158, 0, v230, s[82:83]\n"
    "v_cndmask_b32_e64 v158, v158, v246, s[84:85]\n"
    "v_cndmask_b32_e64 v159, 0, v231, s[82:83]\n"
    "v_cndmask_b32_e64 v159, v159, v247, s[84:85]\n"
    "s_waitcnt lgkmcnt(0)\n"
    "s_nop 1\n"
    "v_mfma_f32_32x32x16_bf16 v[144:159], v[184:187], v[104:107], v[144:159]\n"
    "v_mfma_f32_32x32x16_bf16 v[144:159], v[188:191], v[108:111], v[144:159]\n"
    "s_nop 11\n"
    "v_exp_f32_e32 v144, v144\n"
    "v_exp_f32_e32 v145, v145\n"
    "v_exp_f32_e32 v146, v146\n"
    "v_exp_f32_e32 v147, v147\n"
    "v_exp_f32_e32 v148, v148\n"
    "v_exp_f32_e32 v149, v149\n"
    "v_exp_f32_e32 v150, v150\n"
    "v_exp_f32_e32 v151, v151\n"
    "v_exp_f32_e32 v152, v152\n"
    "v_exp_f32_e32 v153, v153\n"
    "v_exp_f32_e32 v154, v154\n"
    "v_exp_f32_e32 v155, v155\n"
    "v_exp_f32_e32 v156, v156\n"
    "v_exp_f32_e32 v157, v157\n"
    "v_exp_f32_e32 v158, v158\n"
    "v_exp_f32_e32 v159, v159\n"
    "v_cvt_pk_bf16_f32 v176, v144, v145\n"
    "v_cvt_pk_bf16_f32 v177, v146, v147\n"
    "v_cvt_pk_bf16_f32 v178, v148, v149\n"
    "v_cvt_pk_bf16_f32 v179, v150, v151\n"
    "v_cvt_pk_bf16_f32 v180, v152, v153\n"
    "v_cvt_pk_bf16_f32 v181, v154, v155\n"
    "v_cvt_pk_bf16_f32 v182, v156, v157\n"
    "v_cvt_pk_bf16_f32 v183, v158, v159\n"
    "v_mfma_f32_32x32x16_bf16 v[128:143], v[200:203], v[176:179], v[128:143]\n"
    "s_nop 0\n"
    "v_mfma_f32_32x32x16_bf16 v[128:143], v[204:207], v[180:183], v[128:143]\n"
    "s_nop 12\n"
    "s_add_u32 s51, s51, 1\n"
    "s_add_u32 s54, s50, 8\n"
    "s_cmp_le_u32 s51, s54\n"
    "s_cbranch_scc1 .Lmk_fb_112\n"
    "s_nop 12\n"
    "s_branch .Lmk_back_48\n"
    ".Lmk_slow_13:\n"
    "s_add_u32 s36, s36, 1\n"
    "s_cmp_lt_u32 s36, 0xea60\n"
    "s_cbranch_scc0 .Lmk_tmo_115\n"
    "buffer_load_dwordx2 v[254:255], v12, s[16:19], s50 offen sc1\n"
    "s_waitcnt vmcnt(0)\n"
    "v_cmp_ne_u64_e32 vcc, s[24:25], v[254:255]\n"
    "s_cbranch_vccnz .Lmk_slow_13\n"
    "s_branch .Lmk_back_14\n"
    ".Lmk_tmo_115:\n"
    "s_waitcnt vmcnt(0)\n"
    "s_mov_b32 s79, 512\n"
    "v_mov_b32_e32 v254, 0x1001\n"
    "buffer_store_dword v254, off, s[16:19], s79 sc1\n"
    "s_waitcnt vmcnt(0)\n"
    "s_branch .Lmk_back_14\n"
    ".Lmk_again_15:\n"
    "s_add_u32 s36, s36, 1\n"
    "s_cmp_lt_u32 s36, 0xea60\n"
    "s_cbranch_scc0 .Lmk_seen_116\n"
    "buffer_load_dwordx2 v[20:21], v10, s[16:19], s51 offen sc1\n"
    "buffer_load_dwordx2 v[22:23], v10, s[16:19], s51 offen offset:128 sc1\n"
    "buffer_load_dwordx2 v[24:25], v10, s[16:19], s51 offen offset:256 sc1\n"
    "buffer_load_dwordx2 v[26:27], v10, s[16:19], s51 offen offset:384 sc1\n"
    "s_waitcnt vmcnt(0)\n"
    "v_max3_u32 v31, v20, v21, v22\n"
    "v_max3_u32 v31, v31, v23, v24\n"
    "v_max3_u32 v31, v31, v25, v26\n"
    "v_max_u32_e32 v31, v31, v27\n"
    "v_cmp_eq_u32_e32 vcc, -1, v31\n"
    "s_cbranch_vccnz .Lmk_again_15\n"
    ".Lmk_seen_116:\n"
    "s_add_u32 s79, s73, 0x0\n"
    "buffer_load_dwordx4 v[96:99], v0, s[16:19], s79 offen sc1\n"
    "buffer_load_dwordx4 v[100:103], v0, s[16:19], s79 offen offset:1024 sc1\n"
    "buffer_load_dwordx4 v[104:107], v0, s[16:19], s79 offen offset:2048 sc1\n"
    "buffer_load_dwordx4 v[108:111], v0, s[16:19], s79 offen offset:3072 sc1\n"
    "s_add_u32 s79, s73, 0x1000\n"
    "buffer_load_dwordx4 v[112:115], v0, s[16:19], s79 offen sc1\n"
    "buffer_load_dwordx4 v[116:119], v0, s[16:19], s79 offen offset:1024 sc1\n"
    "buffer_load_dwordx4 v[120:123], v0, s[16:19], s79 offen offset:2048 sc1\n"
    "buffer_load_dwordx4 v[124:127], v0, s[16:19], s79 offen offset:3072 sc1\n"
    "s_add_u32 s79, s73, 0x2000\n"
    "buffer_load_dwordx4 v[128:131], v0, s[16:19], s79 offen sc1\n"
    "buffer_load_dwordx4 v[132:135], v0, s[16:19], s79 offen offset:1024 sc1\n"
    "buffer_load_dwordx4 v[136:139], v0, s[16:19], s79 offen offset:2048 sc1\n"
    "buffer_load_dwordx4 v[140:143], v0, s[16:19], s79 offen offset:3072 sc1\n"
    "s_add_u32 s79, s73, 0x3000\n"
    "buffer_load_dwordx4 v[144:147], v0, s[16:19], s79 offen sc1\n"
    "s_branch .Lmk_there_16\n"
    ".Lmk_again_17:\n"
    "s_add_u32 s36, s36, 1\n"
    "s_cmp_lt_u32 s36, 0xea60\n"
    "s_cbranch_scc0 .Lmk_there_18\n"
    "s_add_u32 s79, s73, 0x0\n"
    "buffer_load_dwordx4 v[96:99], v0, s[16:19], s79 offen sc1\n"
    "buffer_load_dwordx4 v[100:103], v0, s[16:19], s79 offen offset:1024 sc1\n"
    "buffer_load_dwordx4 v[104:107], v0, s[16:19], s79 offen offset:2048 sc1\n"
    "buffer_load_dwordx4 v[108:111], v0, s[16:19], s79 offen offset:3072 sc1\n"
    "s_waitcnt vmcnt(0)\n"
    "v_max3_u32 v31, v96, v97, v98\n"
    "v_max_u32_e32 v31, v31, v99\n"
    "v_max3_u32 v31, v31, v100, v101\n"
    "v_max3_u32 v31, v31, v102, v103\n"
    "v_max3_u32 v31, v31, v104, v105\n"
    "v_max3_u32 v31, v31, v106, v107\n"
    "v_max3_u32 v31, v31, v108, v109\n"
    "v_max3_u32 v31, v31, v110, v111\n"
    "v_cmp_eq_u32_e32 vcc, -1, v31\n"
    "s_cbranch_vccnz .Lmk_again_17\n"
    "s_branch .Lmk_there_18\n"
    ".Lmk_again_19:\n"
    "s_add_u32 s36, s36, 1\n"
    "s_cmp_lt_u32 s36, 0xea60\n"
    "s_cbranch_scc0 .Lmk_there_20\n"
    "s_add_u32 s79, s73, 0x1000\n"
    "buffer_load_dwordx4 v[112:115], v0, s[16:19], s79 offen sc1\n"
    "buffer_load_dwordx4 v[116:119], v0, s[16:19], s79 offen offset:1024 sc1\n"
    "buffer_load_dwordx4 v[120:123], v0, s[16:19], s79 offen offset:2048 sc1\n"
    "buffer_load_dwordx4 v[124:127], v0, s[16:19], s79 offen offset:3072 sc1\n"
    "s_waitcnt vmcnt(0)\n"
    "v_max3_u32 v31, v112, v113, v114\n"
    "v_max_u32_e32 v31, v31, v115\n"
    "v_max3_u32 v31, v31, v116, v117\n"
    "v_max3_u32 v31, v31, v118, v119\n"
    "v_max3_u32 v31, v31, v120, v121\n"
    "v_max3_u32 v31, v31, v122, v123\n"
    "v_max3_u32 v31, v31, v124, v125\n"
    "v_max3_u32 v31, v31, v126, v127\n"
    "v_cmp_eq_u32_e32 vcc, -1, v31\n"
    "s_cbranch_vccnz .Lmk_again_19\n"
    "s_branch .Lmk_there_20\n"
    ".Lmk_again_21:\n"
    "s_add_u32 s36, s36, 1\n"
    "s_cmp_lt_u32 s36, 0xea60\n"
    "s_cbranch_scc0 .Lmk_there_22\n"
    "s_add_u32 s79, s73, 0x2000\n"
    "buffer_load_dwordx4 v[128:131], v0, s[16:19], s79 offen sc1\n"
    "buffer_load_dwordx4 v[132:135], v0, s[16:19], s79 offen offset:1024 sc1\n"
    "buffer_load_dwordx4 v[136:139], v0, s[16:19], s79 offen offset:2048 sc1\n"
    "buffer_load_dwordx4 v[140:143], v0, s[16:19], s79 offen offset:3072 sc1\n"
    "s_waitcnt vmcnt(0)\n"
    "v_max3_u32 v31, v128, v129, v130\n"
    "v_max_u32_e32 v31, v31, v131\n"
    "v_max3_u32 v31, v31, v132, v133\n"
    "v_max3_u32 v31, v31, v134, v135\n"
    "v_max3_u32 v31, v31, v136, v137\n"
    "v_max3_u32 v31, v31, v138, v139\n"
    "v_max3_u32 v31, v31, v140, v141\n"
    "v_max3_u32 v31, v31, v142, v143\n"
    "v_cmp_eq_u32_e32 vcc, -1, v31\n"
    "s_cbranch_vccnz .Lmk_again_21\n"
    "s_branch .Lmk_there_22\n"
    ".Lmk_again_23:\n"
    "s_add_u32 s36, s36, 1\n"
    "s_cmp_lt_u32 s36, 0xea60\n"
    "s_cbranch_scc0 .Lmk_there_24\n"
    "s_add_u32 s79, s73, 0x3000\n"
    "buffer_load_dwordx4 v[144:147], v0, s[16:19], s79 offen sc1\n"
    "buffer_load_dwordx4 v[148:151], v0, s[16:19], s79 offen offset:1024 sc1\n"
    "buffer_load_dwordx4 v[152:155], v0, s[16:19], s79 offen offset:2048 sc1\n"
    "buffer_load_dwordx4 v[156:159], v0, s[16:19], s79 offen offset:3072 sc1\n"
    "s_waitcnt vmcnt(0)\n"
    "v_max3_u32 v31, v144, v145, v146\n"
    "v_max_u32_e32 v31, v31, v147\n"
    "v_max3_u32 v31, v31, v148, v149\n"
    "v_max3_u32 v31, v31, v150, v151\n"
    "v_max3_u32 v31, v31, v152, v153\n"
    "v_max3_u32 v31, v31, v154, v155\n"
    "v_max3_u32 v31, v31, v156, v157\n"
    "v_max3_u32 v31, v31, v158, v159\n"
    "v_cmp_eq_u32_e32 vcc, -1, v31\n"
    "s_cbranch_vccnz .Lmk_again_23\n"
    "s_branch .Lmk_there_24\n"
    ".Lmk_again_25:\n"
    "s_add_u32 s36, s36, 1\n"
    "s_cmp_lt_u32 s36, 0xea60\n"
    "s_cbranch_scc0 .Lmk_there_26\n"
    "s_add_u32 s79, s73, 0x4000\n"
    "buffer_load_dwordx4 v[160:163], v0, s[16:19], s79 offen sc1\n"
    "buffer_load_dwordx4 v[164:167], v0, s[16:19], s79 offen offset:1024 sc1\n"
    "buffer_load_dwordx4 v[168:171], v0, s[16:19], s79 offen offset:2048 sc1\n"
    "buffer_load_dwordx4 v[172:175], v0, s[16:19], s79 offen offset:3072 sc1\n"
    "s_waitcnt vmcnt(0)\n"
    "v_max3_u32 v31, v160, v161, v162\n"
    "v_max_u32_e32 v31, v31, v163\n"
    "v_max3_u32 v31, v31, v164, v165\n"
    "v_max3_u32 v31, v31, v166, v167\n"
    "v_max3_u32 v31, v31, v168, v169\n"
    "v_max3_u32 v31, v31, v170, v171\n"
    "v_max3_u32 v31, v31, v172, v173\n"
    "v_max3_u32 v31, v31, v174, v175\n"
    "v_cmp_eq_u32_e32 vcc, -1, v31\n"
    "s_cbranch_vccnz .Lmk_again_25\n"
    "s_branch .Lmk_there_26\n"
    ".Lmk_again_27:\n"
    "s_add_u32 s36, s36, 1\n"
    "s_cmp_lt_u32 s36, 0xea60\n"
    "s_cbranch_scc0 .Lmk_there_28\n"
    "s_add_u32 s79, s73, 0x5000\n"
    "buffer_load_dwordx4 v[176:179], v0, s[16:19], s79 offen sc1\n"
    "buffer_load_dwordx4 v[180:183], v0, s[16:19], s79 offen offset:1024 sc1\n"
    "buffer_load_dwordx4 v[184:187], v0, s[16:19], s79 offen offset:2048 sc1\n"
    "buffer_load_dwordx4 v[188:191], v0, s[16:19], s79 offen offset:3072 sc1\n"
    "s_waitcnt vmcnt(0)\n"
    "v_max3_u32 v31, v176, v177, v178\n"
    "v_max_u32_e32 v31, v31, v179\n"
    "v_max3_u32 v31, v31, v180, v181\n"
    "v_max3_u32 v31, v31, v182, v183\n"
    "v_max3_u32 v31, v31, v184, v185\n"
    "v_max3_u32 v31, v31, v186, v187\n"
    "v_max3_u32 v31, v31, v188, v189\n"
    "v_max3_u32 v31, v31, v190, v191\n"
    "v_cmp_eq_u32_e32 vcc, -1, v31\n"
    "s_cbranch_vccnz .Lmk_again_27\n"
    "s_branch .Lmk_there_28\n"
    ".Lmk_again_29:\n"
    "s_add_u32 s36, s36, 1\n"
    "s_cmp_lt_u32 s36, 0xea60\n"
    "s_cbranch_scc0 .Lmk_there_30\n"
    "s_add_u32 s79, s73, 0x6000\n"
    "buffer_load_dwordx4 v[192:195], v0, s[16:19], s79 offen sc1\n"
    "buffer_load_dwordx4 v[196:199], v0, s[16:19], s79 offen offset:1024 sc1\n"
    "buffer_load_dwordx4 v[200:203], v0, s[16:19], s79 offen offset:2048 sc1\n"
    "buffer_load_dwordx4 v[204:207], v0, s[16:19], s79 offen offset:3072 sc1\n"
    "s_waitcnt vmcnt(0)\n"
    "v_max3_u32 v31, v192, v193, v194\n"
    "v_max_u32_e32 v31, v31, v195\n"
    "v_max3_u32 v31, v31, v196, v197\n"
    "v_max3_u32 v31, v31, v198, v199\n"
    "v_max3_u32 v31, v31, v200, v201\n"
    "v_max3_u32 v31, v31, v202, v203\n"
    "v_max3_u32 v31, v31, v204, v205\n"
    "v_max3_u32 v31, v31, v206, v207\n"
    "v_cmp_eq_u32_e32 vcc, -1, v31\n"
    "s_cbranch_vccnz .Lmk_again_29\n"
    "s_branch .Lmk_there_30\n"
    ".Lmk_again_31:\n"
    "s_add_u32 s36, s36, 1\n"
    "s_cmp_lt_u32 s36, 0xea60\n"
    "s_cbranch_scc0 .Lmk_there_32\n"
    "s_add_u32 s79, s73, 0x7000\n"
    "buffer_load_dwordx4 v[208:211], v0, s[16:19], s79 offen sc1\n"
    "buffer_load_dwordx4 v[212:215], v0, s[16:19], s79 offen offset:1024 sc1\n"
    "buffer_load_dwordx4 v[216:219], v0, s[16:19], s79 offen offset:2048 sc1\n"
    "buffer_load_dwordx4 v[220:223], v0, s[16:19], s79 offen offset:3072 sc1\n"
    "s_waitcnt vmcnt(0)\n"
    "v_max3_u32 v31, v208, v209, v210\n"
    "v_max_u32_e32 v31, v31, v211\n"
    "v_max3_u32 v31, v31, v212, v213\n"
    "v_max3_u32 v31, v31, v214, v215\n"
    "v_max3_u32 v31, v31, v216, v217\n"
    "v_max3_u32 v31, v31, v218, v219\n"
    "v_max3_u32 v31, v31, v220, v221\n"
    "v_max3_u32 v31, v31, v222, v223\n"
    "v_cmp_eq_u32_e32 vcc, -1, v31\n"
    "s_cbranch_vccnz .Lmk_again_31\n"
    "s_branch .Lmk_there_32\n"
    ".Lmk_again_33:\n"
    "s_add_u32 s36, s36, 1\n"
    "s_cmp_lt_u32 s36, 0xea60\n"
    "s_cbranch_scc0 .Lmk_there_34\n"
    "s_add_u32 s79, s73, 0x8000\n"
    "buffer_load_dwordx4 v[224:227], v0, s[16:19], s79 offen sc1\n"
    "buffer_load_dwordx4 v[228:231], v0, s[16:19], s79 offen offset:1024 sc1\n"
    "buffer_load_dwordx4 v[232:235], v0, s[16:19], s79 offen offset:2048 sc1\n"
    "buffer_load_dwordx4 v[236:239], v0, s[16:19], s79 offen offset:3072 sc1\n"
    "s_waitcnt vmcnt(0)\n"
    "v_max3_u32 v31, v224, v225, v226\n"
    "v_max_u32_e32 v31, v31, v227\n"
    "v_max3_u32 v31, v31, v228, v229\n"
    "v_max3_u32 v31, v31, v230, v231\n"
    "v_max3_u32 v31, v31, v232, v233\n"
    "v_max3_u32 v31, v31, v234, v235\n"
    "v_max3_u32 v31, v31, v236, v237\n"
    "v_max3_u32 v31, v31, v238, v239\n"
    "v_cmp_eq_u32_e32 vcc, -1, v31\n"
    "s_cbranch_vccnz .Lmk_again_33\n"
    "s_branch .Lmk_there_34\n"
    ".Lmk_again_35:\n"
    "s_add_u32 s36, s36, 1\n"
    "s_cmp_lt_u32 s36, 0xea60\n"
    "s_cbranch_scc0 .Lmk_there_36\n"
    "s_add_u32 s79, s73, 0x9000\n"
    "buffer_load_dwordx4 v[240:243], v0, s[16:19], s79 offen sc1\n"
    "buffer_load_dwordx4 v[244:247], v0, s[16:19], s79 offen offset:1024 sc1\n"
    "buffer_load_dwordx4 v[248:251], v0, s[16:19], s79 offen offset:2048 sc1\n"
    "buffer_load_dwordx4 v[252:255], v0, s[16:19], s79 offen offset:3072 sc1\n"
    "s_waitcnt vmcnt(0)\n"
    "v_max3_u32 v31, v240, v241, v242\n"
    "v_max_u32_e32 v31, v31, v243\n"
    "v_max3_u32 v31, v31, v244, v245\n"
    "v_max3_u32 v31, v31, v246, v247\n"
    "v_max3_u32 v31, v31, v248, v249\n"
    "v_max3_u32 v31, v31, v250, v251\n"
    "v_max3_u32 v31, v31, v252, v253\n"
    "v_max3_u32 v31, v31, v254, v255\n"
    "v_cmp_eq_u32_e32 vcc, -1, v31\n"
    "s_cbranch_vccnz .Lmk_again_35\n"
    "s_branch .Lmk_there_36\n"
    ".Lmk_slow_37:\n"
    "s_add_u32 s36, s36, 1\n"
    "s_cmp_lt_u32 s36, 0xea60\n"
    "s_cbranch_scc0 .Lmk_tmo_117\n"
    "buffer_load_dwordx2 v[30:31], v26, s[16:19], s57 offen sc1\n"
    "s_waitcnt vmcnt(0)\n"
    "v_cmp_ne_u64_e32 vcc, s[24:25], v[30:31]\n"
    "s_cbranch_vccnz .Lmk_slow_37\n"
    "s_branch .Lmk_back_38\n"
    ".Lmk_tmo_117:\n"
    "s_waitcnt vmcnt(0)\n"
    "s_mov_b32 s79, 512\n"
    "v_mov_b32_e32 v30, 0x1010\n"
    "buffer_store_dword v30, off, s[16:19], s79 sc1\n"
    "s_waitcnt vmcnt(0)\n"
    "s_branch .Lmk_back_38\n"
    ".Lmk_slow_41:\n"
    "s_add_u32 s36, s36, 1\n"
    "s_cmp_lt_u32 s36, 0xea60\n"
    "s_cbranch_scc0 .Lmk_tmo_118\n"
    "buffer_load_dwordx2 v[22:23], v21, s[16:19], s60 offen sc1\n"
    "s_waitcnt vmcnt(0)\n"
    "v_cmp_ne_u64_e32 vcc, s[24:25], v[22:23]\n"
    "s_cbranch_vccnz .Lmk_slow_41\n"
    "s_branch .Lmk_back_42\n"
    ".Lmk_tmo_118:\n"
    "s_waitcnt vmcnt(0)\n"
    "s_mov_b32 s79, 512\n"
    "v_mov_b32_e32 v22, 0x1002\n"
    "buffer_store_dword v22, off, s[16:19], s79 sc1\n"
    "s_waitcnt vmcnt(0)\n"
    "s_branch .Lmk_back_42\n"
    ".Lmk_slow_49:\n"
    "s_add_u32 s36, s36, 1\n"
    "s_cmp_lt_u32 s36, 0xea60\n"
    "s_cbranch_scc0 .Lmk_tmo_119\n"
    "buffer_load_dwordx2 v[254:255], v9, s[16:19], s60 offen sc1\n"
    "s_waitcnt vmcnt(0)\n"
    "v_cmp_ne_u64_e32 vcc, s[24:25], v[254:255]\n"
    "s_cbranch_vccnz .Lmk_slow_49\n"
    "s_branch .Lmk_back_50\n"
    ".Lmk_tmo_119:\n"
    "s_waitcnt vmcnt(0)\n"
    "s_mov_b32 s79, 512\n"
    "v_mov_b32_e32 v254, 0x1004\n"
    "buffer_store_dword v254, off, s[16:19], s79 sc1\n"
    "s_waitcnt vmcnt(0)\n"
    "s_branch .Lmk_back_50\n"
    ".Lmk_again_51:\n"
    "s_add_u32 s36, s36, 1\n"
    "s_cmp_lt_u32 s36, 0xea60\n"
    "s_cbranch_scc0 .Lmk_seen_120\n"
    "buffer_load_dwordx2 v[20:21], v10, s[16:19], s51 offen sc1\n"
    "buffer_load_dwordx2 v[22:23], v10, s[16:19], s51 offen offset:128 sc1\n"
    "buffer_load_dwordx2 v[24:25], v10, s[16:19], s51 offen offset:256 sc1\n"
    "buffer_load_dwordx2 v[26:27], v10, s[16:19], s51 offen offset:384 sc1\n"
    "s_waitcnt vmcnt(0)\n"
    "v_max3_u32 v40, v20, v21, v22\n"
    "v_max3_u32 v40, v40, v23, v24\n"
    "v_max3_u32 v40, v40, v25, v26\n"
    "v_max_u32_e32 v40, v40, v27\n"
    "v_cmp_eq_u32_e32 vcc, -1, v40\n"
    "s_cbranch_vccnz .Lmk_again_51\n"
    ".Lmk_seen_120:\n"
    "s_add_u32 s79, s73, 0x0\n"
    "buffer_load_dwordx4 v[96:99], v0, s[16:19], s79 offen sc1\n"
    "buffer_load_dwordx4 v[100:103], v0, s[16:19], s79 offen offset:1024 sc1\n"
    "buffer_load_dwordx4 v[104:107], v0, s[16:19], s79 offen offset:2048 sc1\n"
    "buffer_load_dwordx4 v[108:111], v0, s[16:19], s79 offen offset:3072 sc1\n"
    "s_add_u32 s79, s73, 0x1000\n"
    "buffer_load_dwordx4 v[112:115], v0, s[16:19], s79 offen sc1\n"
    "buffer_load_dwordx4 v[116:119], v0, s[16:19], s79 offen offset:1024 sc1\n"
    "buffer_load_dwordx4 v[120:123], v0, s[16:19], s79 offen offset:2048 sc1\n"
    "buffer_load_dwordx4 v[124:127], v0, s[16:19], s79 offen offset:3072 sc1\n"
    "s_add_u32 s79, s73, 0x2000\n"
    "buffer_load_dwordx4 v[128:131], v0, s[16:19], s79 offen sc1\n"
    "buffer_load_dwordx4 v[132:135], v0, s[16:19], s79 offen offset:1024 sc1\n"
    "buffer_load_dwordx4 v[136:139], v0, s[16:19], s79 offen offset:2048 sc1\n"
    "buffer_load_dwordx4 v[140:143], v0, s[16:19], s79 offen offset:3072 sc1\n"
    "s_add_u32 s79, s73, 0x3000\n"
    "buffer_load_dwordx4 v[144:147], v0, s[16:19], s79 offen sc1\n"
    "buffer_load_dwordx4 v[148:151], v0, s[16:19], s79 offen offset:1024 sc1\n"
    "s_branch .Lmk_there_52\n"
    ".Lmk_again_53:\n"
    "s_add_u32 s36, s36, 1\n"
    "s_cmp_lt_u32 s36, 0xea60\n"
    "s_cbranch_scc0 .Lmk_there_54\n"
    "s_add_u32 s79, s73, 0x0\n"
    "buffer_load_dwordx4 v[96:99], v0, s[16:19], s79 offen sc1\n"
    "buffer_load_dwordx4 v[100:103], v0, s[16:19], s79 offen offset:1024 sc1\n"
    "buffer_load_dwordx4 v[104:107], v0, s[16:19], s79 offen offset:2048 sc1\n"
    "buffer_load_dwordx4 v[108:111], v0, s[16:19], s79 offen offset:3072 sc1\n"
    "s_waitcnt vmcnt(0)\n"
    "v_max3_u32 v40, v96, v97, v98\n"
    "v_max_u32_e32 v40, v40, v99\n"
    "v_max3_u32 v40, v40, v100, v101\n"
    "v_max3_u32 v40, v40, v102, v103\n"
    "v_max3_u32 v40, v40, v104, v105\n"
    "v_max3_u32 v40, v40, v106, v107\n"
    "v_max3_u32 v40, v40, v108, v109\n"
    "v_max3_u32 v40, v40, v110, v111\n"
    "v_cmp_eq_u32_e32 vcc, -1, v40\n"
    "s_cbranch_vccnz .Lmk_again_53\n"
    "s_branch .Lmk_there_54\n"
    ".Lmk_again_55:\n"
    "s_add_u32 s36, s36, 1\n"
    "s_cmp_lt_u32 s36, 0xea60\n"
    "s_cbranch_scc0 .Lmk_there_56\n"
    "s_add_u32 s79, s73, 0x1000\n"
    "buffer_load_dwordx4 v[112:115], v0, s[16:19], s79 offen sc1\n"
    "buffer_load_dwordx4 v[116:119], v0, s[16:19], s79 offen offset:1024 sc1\n"
    "buffer_load_dwordx4 v[120:123], v0, s[16:19], s79 offen offset:2048 sc1\n"
    "buffer_load_dwordx4 v[124:127], v0, s[16:19], s79 offen offset:3072 sc1\n"
    "s_waitcnt vmcnt(0)\n"
    "v_max3_u32 v40, v112, v113, v114\n"
    "v_max_u32_e32 v40, v40, v115\n"
    "v_max3_u32 v40, v40, v116, v117\n"
    "v_max3_u32 v40, v40, v118, v119\n"
    "v_max3_u32 v40, v40, v120, v121\n"
    "v_max3_u32 v40, v40, v122, v123\n"
    "v_max3_u32 v40, v40, v124, v125\n"
    "v_max3_u32 v40, v40, v126, v127\n"
    "v_cmp_eq_u32_e32 vcc, -1, v40\n"
    "s_cbranch_vccnz .Lmk_again_55\n"
    "s_branch .Lmk_there_56\n"
    ".Lmk_again_57:\n"
    "s_add_u32 s36, s36, 1\n"
    "s_cmp_lt_u32 s36, 0xea60\n"
    "s_cbranch_scc0 .Lmk_there_58\n"
    "s_add_u32 s79, s73, 0x2000\n"
    "buffer_load_dwordx4 v[128:131], v0, s[16:19], s79 offen sc1\n"
    "buffer_load_dwordx4 v[132:135], v0, s[16:19], s79 offen offset:1024 sc1\n"
    "buffer_load_dwordx4 v[136:139], v0, s[16:19], s79 offen offset:2048 sc1\n"
    "buffer_load_dwordx4 v[140:143], v0, s[16:19], s79 offen offset:3072 sc1\n"
    "s_waitcnt vmcnt(0)\n"
    "v_max3_u32 v40, v128, v129, v130\n"
    "v_max_u32_e32 v40, v40, v131\n"
    "v_max3_u32 v40, v40, v132, v133\n"
    "v_max3_u32 v40, v40, v134, v135\n"
    "v_max3_u32 v40, v40, v136, v137\n"
    "v_max3_u32 v40, v40, v138, v139\n"
    "v_max3_u32 v40, v40, v140, v141\n"
    "v_max3_u32 v40, v40, v142, v143\n"
    "v_cmp_eq_u32_e32 vcc, -1, v40\n"
    "s_cbranch_vccnz .Lmk_again_57\n"
    "s_branch .Lmk_there_58\n"
    ".Lmk_again_59:\n"
    "s_add_u32 s36, s36, 1\n"
    "s_cmp_lt_u32 s36, 0xea60\n"
    "s_cbranch_scc0 .Lmk_there_60\n"
    "s_add_u32 s79, s73, 0x3000\n"
    "buffer_load_dwordx4 v[144:147], v0, s[16:19], s79 offen sc1\n"
    "buffer_load_dwordx4 v[148:151], v0, s[16:19], s79 offen offset:1024 sc1\n"
    "buffer_load_dwordx4 v[152:155], v0, s[16:19], s79 offen offset:2048 sc1\n"
    "buffer_load_dwordx4 v[156:159], v0, s[16:19], s79 offen offset:3072 sc1\n"
    "s_waitcnt vmcnt(0)\n"
    "v_max3_u32 v40, v144, v145, v146\n"
    "v_max_u32_e32 v40, v40, v147\n"
    "v_max3_u32 v40, v40, v148, v149\n"
    "v_max3_u32 v40, v40, v150, v151\n"
    "v_max3_u32 v40, v40, v152, v153\n"
    "v_max3_u32 v40, v40, v154, v155\n"
    "v_max3_u32 v40, v40, v156, v157\n"
    "v_max3_u32 v40, v40, v158, v159\n"
    "v_cmp_eq_u32_e32 vcc, -1, v40\n"
    "s_cbranch_vccnz .Lmk_again_59\n"
    "s_branch .Lmk_there_60\n"
    ".Lmk_again_61:\n"
    "s_add_u32 s36, s36, 1\n"
    "s_cmp_lt_u32 s36, 0xea60\n"
    "s_cbranch_scc0 .Lmk_there_62\n"
    "s_add_u32 s79, s73, 0x4000\n"
    "buffer_load_dwordx4 v[160:163], v0, s[16:19], s79 offen sc1\n"
    "buffer_load_dwordx4 v[164:167], v0, s[16:19], s79 offen offset:1024 sc1\n"
    "buffer_load_dwordx4 v[168:171], v0, s[16:19], s79 offen offset:2048 sc1\n"
    "buffer_load_dwordx4 v[172:175], v0, s[16:19], s79 offen offset:3072 sc1\n"
    "s_waitcnt vmcnt(0)\n"
    "v_max3_u32 v40, v160, v161, v162\n"
    "v_max_u32_e32 v40, v40, v163\n"
    "v_max3_u32 v40, v40, v164, v165\n"
    "v_max3_u32 v40, v40, v166, v167\n"
    "v_max3_u32 v40, v40, v168, v169\n"
    "v_max3_u32 v40, v40, v170, v171\n"
    "v_max3_u32 v40, v40, v172, v173\n"
    "v_max3_u32 v40, v40, v174, v175\n"
    "v_cmp_eq_u32_e32 vcc, -1, v40\n"
    "s_cbranch_vccnz .Lmk_again_61\n"
    "s_branch .Lmk_there_62\n"
    ".Lmk_again_63:\n"
    "s_add_u32 s36, s36, 1\n"
    "s_cmp_lt_u32 s36, 0xea60\n"
    "s_cbranch_scc0 .Lmk_there_64\n"
    "s_add_u32 s79, s73, 0x5000\n"
    "buffer_load_dwordx4 v[176:179], v0, s[16:19], s79 offen sc1\n"
    "buffer_load_dwordx4 v[180:183], v0, s[16:19], s79 offen offset:1024 sc1\n"
    "buffer_load_dwordx4 v[184:187], v0, s[16:19], s79 offen offset:2048 sc1\n"
    "buffer_load_dwordx4 v[188:191], v0, s[16:19], s79 offen offset:3072 sc1\n"
    "s_waitcnt vmcnt(0)\n"
    "v_max3_u32 v40, v176, v177, v178\n"
    "v_max_u32_e32 v40, v40, v179\n"
    "v_max3_u32 v40, v40, v180, v181\n"
    "v_max3_u32 v40, v40, v182, v183\n"
    "v_max3_u32 v40, v40, v184, v185\n"
    "v_max3_u32 v40, v40, v186, v187\n"
    "v_max3_u32 v40, v40, v188, v189\n"
    "v_max3_u32 v40, v40, v190, v191\n"
    "v_cmp_eq_u32_e32 vcc, -1, v40\n"
    "s_cbranch_vccnz .Lmk_again_63\n"
    "s_branch .Lmk_there_64\n"
    ".Lmk_again_65:\n"
    "s_add_u32 s36, s36, 1\n"
    "s_cmp_lt_u32 s36, 0xea60\n"
    "s_cbranch_scc0 .Lmk_there_66\n"
    "s_add_u32 s79, s73, 0x6000\n"
    "buffer_load_dwordx4 v[192:195], v0, s[16:19], s79 offen sc1\n"
    "buffer_load_dwordx4 v[196:199], v0, s[16:19], s79 offen offset:1024 sc1\n"
    "buffer_load_dwordx4 v[200:203], v0, s[16:19], s79 offen offset:2048 sc1\n"
    "buffer_load_dwordx4 v[204:207], v0, s[16:19], s79 offen offset:3072 sc1\n"
    "s_waitcnt vmcnt(0)\n"
    "v_max3_u32 v40, v192, v193, v194\n"
    "v_max_u32_e32 v40, v40, v195\n"
    "v_max3_u32 v40, v40, v196, v197\n"
    "v_max3_u32 v40, v40, v198, v199\n"
    "v_max3_u32 v40, v40, v200, v201\n"
    "v_max3_u32 v40, v40, v202, v203\n"
    "v_max3_u32 v40, v40, v204, v205\n"
    "v_max3_u32 v40, v40, v206, v207\n"
    "v_cmp_eq_u32_e32 vcc, -1, v40\n"
    "s_cbranch_vccnz .Lmk_again_65\n"
    "s_branch .Lmk_there_66\n"
    ".Lmk_again_67:\n"
    "s_add_u32 s36, s36, 1\n"
    "s_cmp_lt_u32 s36, 0xea60\n"
    "s_cbranch_scc0 .Lmk_there_68\n"
    "s_add_u32 s79, s73, 0x7000\n"
    "buffer_load_dwordx4 v[208:211], v0, s[16:19], s79 offen sc1\n"
    "buffer_load_dwordx4 v[212:215], v0, s[16:19], s79 offen offset:1024 sc1\n"
    "buffer_load_dwordx4 v[216:219], v0, s[16:19], s79 offen offset:2048 sc1\n"
    "buffer_load_dwordx4 v[220:223], v0, s[16:19], s79 offen offset:3072 sc1\n"
    "s_waitcnt vmcnt(0)\n"
    "v_max3_u32 v40, v208, v209, v210\n"
    "v_max_u32_e32 v40, v40, v211\n"
    "v_max3_u32 v40, v40, v212, v213\n"
    "v_max3_u32 v40, v40, v214, v215\n"
    "v_max3_u32 v40, v40, v216, v217\n"
    "v_max3_u32 v40, v40, v218, v219\n"
    "v_max3_u32 v40, v40, v220, v221\n"
    "v_max3_u32 v40, v40, v222, v223\n"
    "v_cmp_eq_u32_e32 vcc, -1, v40\n"
    "s_cbranch_vccnz .Lmk_again_67\n"
    "s_branch .Lmk_there_68\n"
    ".Lmk_again_69:\n"
    "s_add_u32 s36, s36, 1\n"
    "s_cmp_lt_u32 s36, 0xea60\n"
    "s_cbranch_scc0 .Lmk_there_70\n"
    "s_add_u32 s79, s73, 0x8000\n"
    "buffer_load_dwordx4 v[224:227], v0, s[16:19], s79 offen sc1\n"
    "buffer_load_dwordx4 v[228:231], v0, s[16:19], s79 offen offset:1024 sc1\n"
    "buffer_load_dwordx4 v[232:235], v0, s[16:19], s79 offen offset:2048 sc1\n"
    "buffer_load_dwordx4 v[236:239], v0, s[16:19], s79 offen offset:3072 sc1\n"
    "s_waitcnt vmcnt(0)\n"
    "v_max3_u32 v40, v224, v225, v226\n"
    "v_max_u32_e32 v40, v40, v227\n"
    "v_max3_u32 v40, v40, v228, v229\n"
    "v_max3_u32 v40, v40, v230, v231\n"
    "v_max3_u32 v40, v40, v232, v233\n"
    "v_max3_u32 v40, v40, v234, v235\n"
    "v_max3_u32 v40, v40, v236, v237\n"
    "v_max3_u32 v40, v40, v238, v239\n"
    "v_cmp_eq_u32_e32 vcc, -1, v40\n"
    "s_cbranch_vccnz .Lmk_again_69\n"
    "s_branch .Lmk_there_70\n"
    ".Lmk_again_71:\n"
    "s_add_u32 s36, s36, 1\n"
    "s_cmp_lt_u32 s36, 0xea60\n"
    "s_cbranch_scc0 .Lmk_there_72\n"
    "s_add_u32 s79, s73, 0x9000\n"
    "buffer_load_dwordx4 v[240:243], v0, s[16:19], s79 offen sc1\n"
    "buffer_load_dwordx4 v[244:247], v0, s[16:19], s79 offen offset:1024 sc1\n"
    "buffer_load_dwordx4 v[248:251], v0, s[16:19], s79 offen offset:2048 sc1\n"
    "buffer_load_dwordx4 v[252:255], v0, s[16:19], s79 offen offset:3072 sc1\n"
    "s_waitcnt vmcnt(0)\n"
    "v_max3_u32 v40, v240, v241, v242\n"
    "v_max_u32_e32 v40, v40, v243\n"
    "v_max3_u32 v40, v40, v244, v245\n"
    "v_max3_u32 v40, v40, v246, v247\n"
    "v_max3_u32 v40, v40, v248, v249\n"
    "v_max3_u32 v40, v40, v250, v251\n"
    "v_max3_u32 v40, v40, v252, v253\n"
    "v_max3_u32 v40, v40, v254, v255\n"
    "v_cmp_eq_u32_e32 vcc, -1, v40\n"
    "s_cbranch_vccnz .Lmk_again_71\n"
    "s_branch .Lmk_there_72\n"
    ".Lmk_hre_75:\n"
    "s_add_u32 s36, s36, 1\n"
    "s_cmp_lt_u32 s36, 0xea60\n"
    "s_cbranch_scc0 .Lmk_hok_76\n"
    "v_add_u32_e32 v13, s60, v0\n"
    "buffer_load_dwordx4 v[144:147], v13, s[16:19], s74 offen sc1\n"
    "buffer_load_dwordx4 v[148:151], v13, s[16:19], s75 offen sc1\n"
    "buffer_load_dwordx4 v[152:155], v13, s[16:19], s76 offen sc1\n"
    "buffer_load_dwordx4 v[156:159], v13, s[16:19], s77 offen sc1\n"
    "s_waitcnt vmcnt(0)\n"
    "v_max3_u32 v240, v144, v145, v146\n"
    "v_max3_u32 v240, v240, v147, v148\n"
    "v_max3_u32 v240, v240, v149, v150\n"
    "v_max3_u32 v240, v240, v151, v152\n"
    "v_max3_u32 v240, v240, v153, v154\n"
    "v_max3_u32 v240, v240, v155, v156\n"
    "v_max3_u32 v240, v240, v157, v158\n"
    "v_max_u32_e32 v240, v240, v159\n"
    "v_cmp_eq_u32_e32 vcc, -1, v240\n"
    "s_cbranch_vccnz .Lmk_hre_75\n"
    "s_branch .Lmk_hok_76\n"
    ".Lmk_hre_77:\n"
    "s_add_u32 s36, s36, 1\n"
    "s_cmp_lt_u32 s36, 0xea60\n"
    "s_cbranch_scc0 .Lmk_hok_78\n"
    "v_add_u32_e32 v14, s61, v0\n"
    "buffer_load_dwordx4 v[160:163], v14, s[16:19], s74 offen sc1\n"
    "buffer_load_dwordx4 v[164:167], v14, s[16:19], s75 offen sc1\n"
    "buffer_load_dwordx4 v[168:171], v14, s[16:19], s76 offen sc1\n"
    "buffer_load_dwordx4 v[172:175], v14, s[16:19], s77 offen sc1\n"
    "s_waitcnt vmcnt(0)\n"
    "v_max3_u32 v240, v160, v161, v162\n"
    "v_max3_u32 v240, v240, v163, v164\n"
    "v_max3_u32 v240, v240, v165, v166\n"
    "v_max3_u32 v240, v240, v167, v168\n"
    "v_max3_u32 v240, v240, v169, v170\n"
    "v_max3_u32 v240, v240, v171, v172\n"
    "v_max3_u32 v240, v240, v173, v174\n"
    "v_max_u32_e32 v240, v240, v175\n"
    "v_cmp_eq_u32_e32 vcc, -1, v240\n"
    "s_cbranch_vccnz .Lmk_hre_77\n"
    "s_branch .Lmk_hok_78\n"
    ".Lmk_hre_79:\n"
    "s_add_u32 s36, s36, 1\n"
    "s_cmp_lt_u32 s36, 0xea60\n"
    "s_cbranch_scc0 .Lmk_hok_80\n"
    "v_add_u32_e32 v13, s62, v0\n"
    "buffer_load_dwordx4 v[176:179], v13, s[16:19], s74 offen sc1\n"
    "buffer_load_dwordx4 v[180:183], v13, s[16:19], s75 offen sc1\n"
    "buffer_load_dwordx4 v[184:187], v13, s[16:19], s76 offen sc1\n"
    "buffer_load_dwordx4 v[188:191], v13, s[16:19], s77 offen sc1\n"
    "s_waitcnt vmcnt(0)\n"
    "v_max3_u32 v240, v176, v177, v178\n"
    "v_max3_u32 v240, v240, v179, v180\n"
    "v_max3_u32 v240, v240, v181, v182\n"
    "v_max3_u32 v240, v240, v183, v184\n"
    "v_max3_u32 v240, v240, v185, v186\n"
    "v_max3_u32 v240, v240, v187, v188\n"
    "v_max3_u32 v240, v240, v189, v190\n"
    "v_max_u32_e32 v240, v240, v191\n"
    "v_cmp_eq_u32_e32 vcc, -1, v240\n"
    "s_cbranch_vccnz .Lmk_hre_79\n"
    "s_branch .Lmk_hok_80\n"
    ".Lmk_hre_81:\n"
    "s_add_u32 s36, s36, 1\n"
    "s_cmp_lt_u32 s36, 0xea60\n"
    "s_cbranch_scc0 .Lmk_hok_82\n"
    "v_add_u32_e32 v14, s63, v0\n"
    "buffer_load_dwordx4 v[192:195], v14, s[16:19], s74 offen sc1\n"
    "buffer_load_dwordx4 v[196:199], v14, s[16:19], s75 offen sc1\n"
    "buffer_load_dwordx4 v[200:203], v14, s[16:19], s76 offen sc1\n"
    "buffer_load_dwordx4 v[204:207], v14, s[16:19], s77 offen sc1\n"
    "s_waitcnt vmcnt(0)\n"
    "v_max3_u32 v240, v192, v193, v194\n"
    "v_max3_u32 v240, v240, v195, v196\n"
    "v_max3_u32 v240, v240, v197, v198\n"
    "v_max3_u32 v240, v240, v199, v200\n"
    "v_max3_u32 v240, v240, v201, v202\n"
    "v_max3_u32 v240, v240, v203, v204\n"
    "v_max3_u32 v240, v240, v205, v206\n"
    "v_max_u32_e32 v240, v240, v207\n"
    "v_cmp_eq_u32_e32 vcc, -1, v240\n"
    "s_cbranch_vccnz .Lmk_hre_81\n"
    "s_branch .Lmk_hok_82\n"
    ".Lmk_hre_83:\n"
    "s_add_u32 s36, s36, 1\n"
    "s_cmp_lt_u32 s36, 0xea60\n"
    "s_cbranch_scc0 .Lmk_hok_84\n"
    "s_add_u32 s67, s60, 1024\n"
    "v_add_u32_e32 v13, s67, v0\n"
    "buffer_load_dwordx4 v[208:211], v13, s[16:19], s74 offen sc1\n"
    "buffer_load_dwordx4 v[212:215], v13, s[16:19], s75 offen sc1\n"
    "buffer_load_dwordx4 v[216:219], v13, s[16:19], s76 offen sc1\n"
    "buffer_load_dwordx4 v[220:223], v13, s[16:19], s77 offen sc1\n"
    "s_waitcnt vmcnt(0)\n"
    "v_max3_u32 v240, v208, v209, v210\n"
    "v_max3_u32 v240, v240, v211, v212\n"
    "v_max3_u32 v240, v240, v213, v214\n"
    "v_max3_u32 v240, v240, v215, v216\n"
    "v_max3_u32 v240, v240, v217, v218\n"
    "v_max3_u32 v240, v240, v219, v220\n"
    "v_max3_u32 v240, v240, v221, v222\n"
    "v_max_u32_e32 v240, v240, v223\n"
    "v_cmp_eq_u32_e32 vcc, -1, v240\n"
    "s_cbranch_vccnz .Lmk_hre_83\n"
    "s_branch .Lmk_hok_84\n"
    ".Lmk_hre_85:\n"
    "s_add_u32 s36, s36, 1\n"
    "s_cmp_lt_u32 s36, 0xea60\n"
    "s_cbranch_scc0 .Lmk_hok_86\n"
    "s_add_u32 s67, s61, 1024\n"
    "v_add_u32_e32 v14, s67, v0\n"
    "buffer_load_dwordx4 v[224:227], v14, s[16:19], s74 offen sc1\n"
    "buffer_load_dwordx4 v[228:231], v14, s[16:19], s75 offen sc1\n"
    "buffer_load_dwordx4 v[232:235], v14, s[16:19], s76 offen sc1\n"
    "buffer_load_dwordx4 v[236:239], v14, s[16:19], s77 offen sc1\n"
    "s_waitcnt vmcnt(0)\n"
    "v_max3_u32 v240, v224, v225, v226\n"
    "v_max3_u32 v240, v240, v227, v228\n"
    "v_max3_u32 v240, v240, v229, v230\n"
    "v_max3_u32 v240, v240, v231, v232\n"
    "v_max3_u32 v240, v240, v233, v234\n"
    "v_max3_u32 v240, v240, v235, v236\n"
    "v_max3_u32 v240, v240, v237, v238\n"
    "v_max_u32_e32 v240, v240, v239\n"
    "v_cmp_eq_u32_e32 vcc, -1, v240\n"
    "s_cbranch_vccnz .Lmk_hre_85\n"
    "s_branch .Lmk_hok_86\n"
    ".Lmk_hre_87:\n"
    "s_add_u32 s36, s36, 1\n"
    "s_cmp_lt_u32 s36, 0xea60\n"
    "s_cbranch_scc0 .Lmk_hok_88\n"
    "s_add_u32 s67, s62, 1024\n"
    "v_add_u32_e32 v13, s67, v0\n"
    "buffer_load_dwordx4 v[144:147], v13, s[16:19], s74 offen sc1\n"
    "buffer_load_dwordx4 v[148:151], v13, s[16:19], s75 offen sc1\n"
    "buffer_load_dwordx4 v[152:155], v13, s[16:19], s76 offen sc1\n"
    "buffer_load_dwordx4 v[156:159], v13, s[16:19], s77 offen sc1\n"
    "s_waitcnt vmcnt(0)\n"
    "v_max3_u32 v240, v144, v145, v146\n"
    "v_max3_u32 v240, v240, v147, v148\n"
    "v_max3_u32 v240, v240, v149, v150\n"
    "v_max3_u32 v240, v240, v151, v152\n"
    "v_max3_u32 v240, v240, v153, v154\n"
    "v_max3_u32 v240, v240, v155, v156\n"
    "v_max3_u32 v240, v240, v157, v158\n"
    "v_max_u32_e32 v240, v240, v159\n"
    "v_cmp_eq_u32_e32 vcc, -1, v240\n"
    "s_cbranch_vccnz .Lmk_hre_87\n"
    "s_branch .Lmk_hok_88\n"
    ".Lmk_hre_89:\n"
    "s_add_u32 s36, s36, 1\n"
    "s_cmp_lt_u32 s36, 0xea60\n"
    "s_cbranch_scc0 .Lmk_hok_90\n"
    "s_add_u32 s67, s63, 1024\n"
    "v_add_u32_e32 v14, s67, v0\n"
    "buffer_load_dwordx4 v[160:163], v14, s[16:19], s74 offen sc1\n"
    "buffer_load_dwordx4 v[164:167], v14, s[16:19], s75 offen sc1\n"
    "buffer_load_dwordx4 v[168:171], v14, s[16:19], s76 offen sc1\n"
    "buffer_load_dwordx4 v[172:175], v14, s[16:19], s77 offen sc1\n"
    "s_waitcnt vmcnt(0)\n"
    "v_max3_u32 v240, v160, v161, v162\n"
    "v_max3_u32 v240, v240, v163, v164\n"
    "v_max3_u32 v240, v240, v165, v166\n"
    "v_max3_u32 v240, v240, v167, v168\n"
    "v_max3_u32 v240, v240, v169, v170\n"
    "v_max3_u32 v240, v240, v171, v172\n"
    "v_max3_u32 v240, v240, v173, v174\n"
    "v_max_u32_e32 v240, v240, v175\n"
    "v_cmp_eq_u32_e32 vcc, -1, v240\n"
    "s_cbranch_vccnz .Lmk_hre_89\n"
    "s_branch .Lmk_hok_90\n"
    ".Lmk_hre_91:\n"
    "s_add_u32 s36, s36, 1\n"
    "s_cmp_lt_u32 s36, 0xea60\n"
    "s_cbranch_scc0 .Lmk_hok_92\n"
    "s_add_u32 s67, s63, 2048\n"
    "v_add_u32_e32 v13, s67, v0\n"
    "buffer_load_dwordx4 v[176:179], v13, s[16:19], s74 offen sc1\n"
    "buffer_load_dwordx4 v[180:183], v13, s[16:19], s75 offen sc1\n"
    "buffer_load_dwordx4 v[184:187], v13, s[16:19], s76 offen sc1\n"
    "buffer_load_dwordx4 v[188:191], v13, s[16:19], s77 offen sc1\n"
    "s_waitcnt vmcnt(0)\n"
    "v_max3_u32 v240, v176, v177, v178\n"
    "v_max3_u32 v240, v240, v179, v180\n"
    "v_max3_u32 v240, v240, v181, v182\n"
    "v_max3_u32 v240, v240, v183, v184\n"
    "v_max3_u32 v240, v240, v185, v186\n"
    "v_max3_u32 v240, v240, v187, v188\n"
    "v_max3_u32 v240, v240, v189, v190\n"
    "v_max_u32_e32 v240, v240, v191\n"
    "v_cmp_eq_u32_e32 vcc, -1, v240\n"
    "s_cbranch_vccnz .Lmk_hre_91\n"
    "s_branch .Lmk_hok_92\n"
    ".Lmk_hre_93:\n"
    "s_add_u32 s36, s36, 1\n"
    "s_cmp_lt_u32 s36, 0xea60\n"
    "s_cbranch_scc0 .Lmk_hok_94\n"
    "s_add_u32 s67, s62, 2048\n"
    "v_add_u32_e32 v14, s67, v0\n"
    "buffer_load_dwordx4 v[192:195], v14, s[16:19], s74 offen sc1\n"
    "buffer_load_dwordx4 v[196:199], v14, s[16:19], s75 offen sc1\n"
    "buffer_load_dwordx4 v[200:203], v14, s[16:19], s76 offen sc1\n"
    "buffer_load_dwordx4 v[204:207], v14, s[16:19], s77 offen sc1\n"
    "s_waitcnt vmcnt(0)\n"
    "v_max3_u32 v240, v192, v193, v194\n"
    "v_max3_u32 v240, v240, v195, v196\n"
    "v_max3_u32 v240, v240, v197, v198\n"
    "v_max3_u32 v240, v240, v199, v200\n"
    "v_max3_u32 v240, v240, v201, v202\n"
    "v_max3_u32 v240, v240, v203, v204\n"
    "v_max3_u32 v240, v240, v205, v206\n"
    "v_max_u32_e32 v240, v240, v207\n"
    "v_cmp_eq_u32_e32 vcc, -1, v240\n"
    "s_cbranch_vccnz .Lmk_hre_93\n"
    "s_branch .Lmk_hok_94\n"
    ".Lmk_hre_95:\n"
    "s_add_u32 s36, s36, 1\n"
    "s_cmp_lt_u32 s36, 0xea60\n"
    "s_cbranch_scc0 .Lmk_hok_96\n"
    "s_add_u32 s67, s61, 2048\n"
    "v_add_u32_e32 v13, s67, v0\n"
    "buffer_load_dwordx4 v[208:211], v13, s[16:19], s74 offen sc1\n"
    "buffer_load_dwordx4 v[212:215], v13, s[16:19], s75 offen sc1\n"
    "buffer_load_dwordx4 v[216:219], v13, s[16:19], s76 offen sc1\n"
    "buffer_load_dwordx4 v[220:223], v13, s[16:19], s77 offen sc1\n"
    "s_waitcnt vmcnt(0)\n"
    "v_max3_u32 v240, v208, v209, v210\n"
    "v_max3_u32 v240, v240, v211, v212\n"
    "v_max3_u32 v240, v240, v213, v214\n"
    "v_max3_u32 v240, v240, v215, v216\n"
    "v_max3_u32 v240, v240, v217, v218\n"
    "v_max3_u32 v240, v240, v219, v220\n"
    "v_max3_u32 v240, v240, v221, v222\n"
    "v_max_u32_e32 v240, v240, v223\n"
    "v_cmp_eq_u32_e32 vcc, -1, v240\n"
    "s_cbranch_vccnz .Lmk_hre_95\n"
    "s_branch .Lmk_hok_96\n"
    ".Lmk_hre_97:\n"
    "s_add_u32 s36, s36, 1\n"
    "s_cmp_lt_u32 s36, 0xea60\n"
    "s_cbranch_scc0 .Lmk_hok_98\n"
    "s_add_u32 s67, s60, 2048\n"
    "v_add_u32_e32 v14, s67, v0\n"
    "buffer_load_dwordx4 v[224:227], v14, s[16:19], s74 offen sc1\n"
    "buffer_load_dwordx4 v[228:231], v14, s[16:19], s75 offen sc1\n"
    "buffer_load_dwordx4 v[232:235], v14, s[16:19], s76 offen sc1\n"
    "buffer_load_dwordx4 v[236:239], v14, s[16:19], s77 offen sc1\n"
    "s_waitcnt vmcnt(0)\n"
    "v_max3_u32 v240, v224, v225, v226\n"
    "v_max3_u32 v240, v240, v227, v228\n"
    "v_max3_u32 v240, v240, v229, v230\n"
    "v_max3_u32 v240, v240, v231, v232\n"
    "v_max3_u32 v240, v240, v233, v234\n"
    "v_max3_u32 v240, v240, v235, v236\n"
    "v_max3_u32 v240, v240, v237, v238\n"
    "v_max_u32_e32 v240, v240, v239\n"
    "v_cmp_eq_u32_e32 vcc, -1, v240\n"
    "s_cbranch_vccnz .Lmk_hre_97\n"
    "s_branch .Lmk_hok_98\n"
    ".Lmk_hre_99:\n"
    "s_add_u32 s36, s36, 1\n"
    "s_cmp_lt_u32 s36, 0xea60\n"
    "s_cbranch_scc0 .Lmk_hok_100\n"
    "s_add_u32 s67, s60, 3072\n"
    "v_add_u32_e32 v13, s67, v0\n"
    "buffer_load_dwordx4 v[144:147], v13, s[16:19], s74 offen sc1\n"
    "buffer_load_dwordx4 v[148:151], v13, s[16:19], s75 offen sc1\n"
    "buffer_load_dwordx4 v[152:155], v13, s[16:19], s76 offen sc1\n"
    "buffer_load_dwordx4 v[156:159], v13, s[16:19], s77 offen sc1\n"
    "s_waitcnt vmcnt(0)\n"
    "v_max3_u32 v240, v144, v145, v146\n"
    "v_max3_u32 v240, v240, v147, v148\n"
    "v_max3_u32 v240, v240, v149, v150\n"
    "v_max3_u32 v240, v240, v151, v152\n"
    "v_max3_u32 v240, v240, v153, v154\n"
    "v_max3_u32 v240, v240, v155, v156\n"
    "v_max3_u32 v240, v240, v157, v158\n"
    "v_max_u32_e32 v240, v240, v159\n"
    "v_cmp_eq_u32_e32 vcc, -1, v240\n"
    "s_cbranch_vccnz .Lmk_hre_99\n"
    "s_branch .Lmk_hok_100\n"
    ".Lmk_hre_101:\n"
    "s_add_u32 s36, s36, 1\n"
    "s_cmp_lt_u32 s36, 0xea60\n"
    "s_cbranch_scc0 .Lmk_hok_102\n"
    "v_add_u32_e32 v14, s64, v0\n"
    "buffer_load_dwordx4 v[160:163], v14, s[16:19], s74 offen sc1\n"
    "buffer_load_dwordx4 v[164:167], v14, s[16:19], s75 offen sc1\n"
    "buffer_load_dwordx4 v[168:171], v14, s[16:19], s76 offen sc1\n"
    "buffer_load_dwordx4 v[172:175], v14, s[16:19], s77 offen sc1\n"
    "s_waitcnt vmcnt(0)\n"
    "v_max3_u32 v240, v160, v161, v162\n"
    "v_max3_u32 v240, v240, v163, v164\n"
    "v_max3_u32 v240, v240, v165, v166\n"
    "v_max3_u32 v240, v240, v167, v168\n"
    "v_max3_u32 v240, v240, v169, v170\n"
    "v_max3_u32 v240, v240, v171, v172\n"
    "v_max3_u32 v240, v240, v173, v174\n"
    "v_max_u32_e32 v240, v240, v175\n"
    "v_cmp_eq_u32_e32 vcc, -1, v240\n"
    "s_cbranch_vccnz .Lmk_hre_101\n"
    "s_branch .Lmk_hok_102\n"
    ".Lmk_out:\n"
    "s_mov_b32 s20, s46\n"
    "s_and_b32 s21, s47, 0xffff\n"
    "s_mov_b32 s22, 0x280000\n"
    "v_and_b32_e32 v10, 0xf0, v0\n"
    "v_lshrrev_b32_e32 v10, 4, v10\n"
    "v_mul_u32_u24_e32 v10, 1280, v10\n"
    "v_lshrrev_b32_e32 v11, 8, v0\n"
    "v_lshl_add_u32 v10, v11, 3, v10\n"
    "s_mul_i32 s50, s33, 20480\n"
    "s_mul_i32 s51, s31, 160\n"
    "s_add_u32 s50, s50, s51\n"
    "v_cvt_pk_bf16_f32 v100, v16, v17\n"
    "v_cvt_pk_bf16_f32 v101, v18, v19\n"
    "buffer_store_dwordx2 v[100:101], v10, s[20:23], s50 offen\n"
    "v_cvt_pk_bf16_f32 v102, v20, v21\n"
    "v_cvt_pk_bf16_f32 v103, v22, v23\n"
    "buffer_store_dwordx2 v[102:103], v10, s[20:23], s50 offen offset:32\n"
    "v_cvt_pk_bf16_f32 v104, v24, v25\n"
    "v_cvt_pk_bf16_f32 v105, v26, v27\n"
    "buffer_store_dwordx2 v[104:105], v10, s[20:23], s50 offen offset:64\n"
    "v_cvt_pk_bf16_f32 v106, v28, v29\n"
    "v_cvt_pk_bf16_f32 v107, v30, v31\n"
    "buffer_store_dwordx2 v[106:107], v10, s[20:23], s50 offen offset:96\n"
    "v_cvt_pk_bf16_f32 v108, v32, v33\n"
    "v_cvt_pk_bf16_f32 v109, v34, v35\n"
    "buffer_store_dwordx2 v[108:109], v10, s[20:23], s50 offen offset:128\n"
    ".Lmk_end:\n"
    "s_or_b32 s50, s78, s26\n"
    "s_cmp_eq_u32 s50, 0\n"
    "s_cbranch_scc0 .Lmk_noep_121\n"
    "s_mov_b32 s50, 0\n"
    "buffer_store_dwordx2 v[2:3], v4, s[16:19], s50 offen sc1\n"
    ".Lmk_noep_121:\n"
    "s_waitcnt vmcnt(0)\n"
    :
    : "s"(__builtin_amdgcn_kernarg_segment_ptr()), "s"(b), "s"(w)
    : "memory", "vcc", "scc", "m0", "v0", "v1", "v2", "v3", "v4", "v5", "v6", "v7", "v8", "v9", "v10", "v11", "v12", "v13", "v14", "v15", "v16", "v17", "v18", "v19", "v20", "v21", "v22", "v23", "v24", "v25", "v26", "v27", "v28", "v29", "v30", "v31", "v32", "v33", "v34", "v35", "v36", "v37", "v38", "v39", "v40", "v41", "v42", "v43", "v44", "v45", "v46", "v47", "v48", "v49", "v50", "v51", "v52", "v53", "v54", "v55", "v56", "v57", "v58", "v59", "v60", "v61", "v62", "v63", "v64", "v65", "v66", "v67", "v68", "v69", "v70", "v71", "v72", "v73", "v74", "v75", "v76", "v77", "v78", "v79", "v80", "v81", "v82", "v83", "v84", "v85", "v86", "v87", "v88", "v89", "v90", "v91", "v92", "v93", "v94", "v95", "v96", "v97", "v98", "v99", "v100", "v101", "v102", "v103", "v104", "v105", "v106", "v107", "v108", "v109", "v110", "v111", "v112", "v113", "v114", "v115", "v116", "v117", "v118", "v119", "v120", "v121", "v122", "v123", "v124", "v125", "v126", "v127", "v128", "v129", "v130", "v131", "v132", "v133", "v134", "v135", "v136", "v137", "v138", "v139", "v140", "v141", "v142", "v143", "v144", "v145", "v146", "v147", "v148", "v149", "v150", "v151", "v152", "v153", "v154", "v155", "v156", "v157", "v158", "v159", "v160", "v161", "v162", "v163", "v164", "v165", "v166", "v167", "v168", "v169", "v170", "v171", "v172", "v173", "v174", "v175", "v176", "v177", "v178", "v179", "v180", "v181", "v182", "v183", "v184", "v185", "v186", "v187", "v188", "v189", "v190", "v191", "v192", "v193", "v194", "v195", "v196", "v197", "v198", "v199", "v200", "v201", "v202", "v203", "v204", "v205", "v206", "v207", "v208", "v209", "v210", "v211", "v212", "v213", "v214", "v215", "v216", "v217", "v218", "v219", "v220", "v221", "v222", "v223", "v224", "v225", "v226", "v227", "v228", "v229", "v230", "v231", "v232", "v233", "v234", "v235", "v236", "v237", "v238", "v239", "v240", "v241", "v242", "v243", "v244", "v245", "v246", "v247", "v248", "v249", "v250", "v251", "v252", "v253", "v254", "v255", "a0", "a1", "a2", "a3", "a4", "a5", "a6", "a7", "a8", "a9", "a10", "a11", "a12", "a13", "a14", "a15", "a16", "a17", "a18", "a19", "a20", "a21", "a22", "a23", "a24", "a25", "a26", "a27", "a28", "a29", "a30", "a31", "a32", "a33", "a34", "a35", "a36", "a37", "a38", "a39", "a40", "a41", "a42", "a43", "a44", "a45", "a46", "a47", "a48", "a49", "a50", "a51", "a52", "a53", "a54", "a55", "a56", "a57", "a58", "a59", "a60", "a61", "a62", "a63", "a64", "a65", "a66", "a67", "a68", "a69", "a70", "a71", "a72", "a73", "a74", "a75", "a76", "a77", "a78", "a79", "a80", "a81", "a82", "a83", "a84", "a85", "a86", "a87", "a88", "a89", "a90", "a91", "a92", "a93", "a94", "a95", "a96", "a97", "a98", "a99", "a100", "a101", "a102", "a103", "a104", "a105", "a106", "a107", "a108", "a109", "a110", "a111", "a112", "a113", "a114", "a115", "a116", "a117", "a118", "a119", "a120", "a121", "a122", "a123", "a124", "a125", "a126", "a127", "a128", "a129", "a130", "a131", "a132", "a133", "a134", "a135", "a136", "a137", "a138", "a139", "a140", "a141", "a142", "a143", "a144", "a145", "a146", "a147", "a148", "a149", "a150", "a151", "a152", "a153", "a154", "a155", "a156", "a157", "a158", "a159", "a160", "a161", "a162", "a163", "a164", "a165", "a166", "a167", "a168", "a169", "a170", "a171", "a172", "a173", "a174", "a175", "a176", "a177", "a178", "a179", "a180", "a181", "a182", "a183", "a184", "a185", "a186", "a187", "a188", "a189", "a190", "a191", "a192", "a193", "a194", "a195", "a196", "a197", "a198", "a199", "a200", "a201", "a202", "a203", "a204", "a205", "a206", "a207", "a208", "a209", "a210", "a211", "a212", "a213", "a214", "a215", "a216", "a217", "a218", "a219", "a220", "a221", "a222", "a223", "a224", "a225", "a226", "a227", "a228", "a229", "a230", "a231", "a232", "a233", "a234", "a235", "a236", "a237", "a238", "a239", "a240", "a241", "a242", "a243", "a244", "a245", "a246", "a247", "a248", "a249", "a250", "a251", "a252", "a253", "a254", "a255", "s16", "s17", "s18", "s19", "s20", "s21", "s22", "s23", "s24", "s25", "s26", "s27", "s28", "s29", "s30", "s31", "s32", "s33", "s34", "s35", "s36", "s37", "s38", "s39", "s40", "s41", "s42", "s43", "s44", "s45", "s46", "s47", "s48", "s49", "s50", "s51", "s52", "s53", "s54", "s55", "s56", "s57", "s58", "s59", "s60", "s61", "s62", "s63", "s64", "s65", "s66", "s67", "s68", "s69", "s70", "s71", "s72", "s73", "s74", "s75", "s76", "s77", "s78", "s79", "s80", "s81", "s82", "s83", "s84", "s85", "s86", "s87", "s88", "s89", "s90", "s91", "s92", "s93", "s94", "s95", "s96", "s97");
}

}

extern "C" void kernel_launch(void* const* d_in, const int* in_sizes, int n_in, void* d_out, int out_size, void* d_ws, size_t ws_size,
                              hipStream_t stream) {
    using namespace mk;
    (void)in_sizes; (void)n_in; (void)out_size; (void)ws_size;
    hipFuncSetAttribute((const void*)mk_kernel, hipFuncAttributeMaxDynamicSharedMemorySize, L_BYTES);
    mk_kernel<<<256, 256, L_BYTES, stream>>>((const u16*)d_in[0], (const u16*)d_in[1], (const u16*)d_in[2], (const u16*)d_in[4],
                                             (const u16*)d_in[5], (const u16*)d_in[6], (const u16*)d_in[7], (u16*)d_out, (char*)d_ws);
}
```
